```python
import torch

def _softmax(x, axis=-1):
    return torch.softmax(x, dim=axis)
def _gelu(x):
    return torch.nn.functional.gelu(x, approximate="tanh")
def _silu(x):
    return torch.nn.functional.silu(x)
def _sigmoid(x):
    return torch.sigmoid(x)
def _layernorm(x, axis=-1, eps=1e-5):
    m = x.mean(dim=axis, keepdim=True)
    v = x.var(dim=axis, keepdim=True, unbiased=False)
    return (x - m) / torch.sqrt(v + eps)
def _scatter_add(tgt, idx, src, axis=0):
    return tgt.clone().index_add_(axis, idx, src)
def _segment_sum(src, seg, n):
    out = torch.zeros((n,) + src.shape[1:], dtype=src.dtype)
    return out.index_add_(0, seg, src)
def _pad(x, pw):
    flat = tuple(v for pair in reversed(pw) for v in pair)
    return torch.nn.functional.pad(x, flat)

kernel_name = 'recurrent_scan__compressed_kv_cla__dense_activation_squared_relu__prompt'

def setup_inputs():

    _w = (lambda a: (lambda t: t.to(torch.bfloat16) if t.is_floating_point() else t)(torch.as_tensor(a)))
    def _A_setup():
        B, H, Sq, Sk, D, M = 1, 16, 2048, 2048, 256, 448
        Dc = 4 * D
        return {
            "x": _w((lambda *s: torch.randn(*s, dtype=torch.bfloat16) * 0.05)(B, Sq, M)),
            "c_kv": _w((lambda *s: torch.randn(*s, dtype=torch.bfloat16) * 0.2)(B, Sk, Dc)),
            "w_q": _w((lambda *s: torch.randn(*s, dtype=torch.bfloat16) * 3.2)(M, H, D) * (M ** -0.5)),
            "w_uk": _w((lambda *s: torch.randn(*s, dtype=torch.bfloat16) * 3.2)(Dc, H, D) * (Dc ** -0.5)),
            "w_uv": _w((lambda *s: torch.randn(*s, dtype=torch.bfloat16) * 0.2)(Dc, H, D) * (Dc ** -0.5)),
            "w_o": _w((lambda *s: torch.randn(*s, dtype=torch.bfloat16) * 0.2)(H, D, M) * ((H * D) ** -0.5)),
        }
    _A_in = _A_setup()

    def _B_setup():
        B, S, D, F = 1, 2048, 448, 1792
        return {
            "x": _w((lambda *s: torch.randn(*s, dtype=torch.bfloat16) * 0.05)(B, S, D)),
            "w1": _w((lambda *s: torch.randn(*s, dtype=torch.bfloat16) * 0.1)(D, F)),
            "b1": _w((lambda *s: torch.randn(*s, dtype=torch.bfloat16) * 0.8)(F)),
            "w2": _w((lambda *s: torch.randn(*s, dtype=torch.bfloat16) * 0.1)(F, D)),
            "b2": _w((lambda *s: torch.randn(*s, dtype=torch.bfloat16) * 6.4)(D)),
        }
    _B_in = _B_setup()

    return {
        "A_x": _A_in["x"],
        "A_c_kv": _A_in["c_kv"],
        "A_w_q": _A_in["w_q"],
        "A_w_uk": _A_in["w_uk"],
        "A_w_uv": _A_in["w_uv"],
        "A_w_o": _A_in["w_o"],
        "B_x": _B_in["x"],
        "B_w1": _B_in["w1"],
        "B_b1": _B_in["b1"],
        "B_w2": _B_in["w2"],
        "B_b2": _B_in["b2"],
    }

def reference(A_x, A_c_kv, A_w_q, A_w_uk, A_w_uv, A_w_o, B_x, B_w1, B_b1, B_w2, B_b2):
    def _A_ref(x, c_kv, w_q, w_uk, w_uv, w_o):
        B, Sq, M = x.shape[0], x.shape[1], x.shape[2]
        Sk = c_kv.shape[1]
        H, D = w_q.shape[1], w_q.shape[2]
        q = torch.einsum("bqm,mhd->bhqd", x, w_q)
        k = torch.einsum("bkc,chd->bhkd", c_kv, w_uk)
        v = torch.einsum("bkc,chd->bhkd", c_kv, w_uv)
        scores = torch.einsum("bhqd,bhkd->bhqk", q, k) * (D ** -0.5)
        off = Sk - Sq
        qi = torch.arange(Sq)[:, None]
        kj = torch.arange(Sk)[None, :]
        cmask = kj <= (qi + off)
        scores = torch.where(cmask[None, None], scores, -1e9)
        p = _softmax(scores, axis=-1)
        ctx = torch.einsum("bhqk,bhkd->bhqd", p, v)
        return torch.einsum("bhqd,hdm->bqm", ctx, w_o)

    def _B_ref(x, w1, b1, w2, b2):
        h = torch.einsum("bsd,df->bsf", x, w1) + b1
        zero = torch.zeros_like(h)
        r = torch.where(h > zero, h, zero)
        h = r * r
        out = torch.einsum("bsf,fd->bsd", h, w2) + b2
        return out

    x = A_x
    xn = _layernorm(x, axis=-1)
    s = _A_ref(xn, A_c_kv, A_w_q, A_w_uk, A_w_uv, A_w_o)
    g = _silu(_B_ref(xn, B_w1, B_b1, B_w2, B_b2))
    return x + s * g
```

```cpp
#include <hip/hip_runtime.h>
#include <hip/hip_bf16.h>
#include <stdint.h>
typedef unsigned short u16;
__device__ __forceinline__ float bf2f(u16 b){ return __uint_as_float(((unsigned)b)<<16); }
__device__ __forceinline__ u16 f2bf(float f){ unsigned u=__float_as_uint(f); u += 0x7FFFu + ((u>>16)&1u); return (u16)(u>>16); }
__device__ __forceinline__ float rbf(float f){ return bf2f(f2bf(f)); }
constexpr int S=2048, M=448, H=16, D=256, DC=1024, F=1792;

__global__ void ln_k(const u16* x, u16* xn){
  int row=blockIdx.x; int t=threadIdx.x;
  float v[7]; float s=0;
  for(int i=0;i<7;i++){ v[i]=bf2f(x[row*M+t+64*i]); s+=v[i]; }
  for(int o=32;o>0;o>>=1) s+=__shfl_xor(s,o);
  float mean=s/M; float q=0;
  for(int i=0;i<7;i++){ float d=v[i]-mean; q+=d*d; }
  for(int o=32;o>0;o>>=1) q+=__shfl_xor(q,o);
  float var=q/M;
  float mb=rbf(mean), vb=rbf(var);
  float den=rbf(sqrtf(rbf(vb+1e-5f)));
  for(int i=0;i<7;i++){ xn[row*M+t+64*i]=f2bf(rbf(v[i]-mb)/den); }
}
__global__ void gemm_k(const u16* A,int lda,const u16* B,int ldb,u16* C,int ldc,int K,const u16* bias,int mode){
  __shared__ float As[16][65], Bs[16][65];
  int tx=threadIdx.x&15, ty=threadIdx.x>>4; int m0=blockIdx.y*64, n0=blockIdx.x*64;
  float acc[4][4]={};
  for(int k0=0;k0<K;k0+=16){
    for(int i=threadIdx.x;i<1024;i+=256){ int r=i>>4,c=i&15; As[c][r]=bf2f(A[(size_t)(m0+r)*lda+k0+c]); }
    for(int i=threadIdx.x;i<1024;i+=256){ int r=i>>6,c=i&63; Bs[r][c]=bf2f(B[(size_t)(k0+r)*ldb+n0+c]); }
    __syncthreads();
    for(int k=0;k<16;k++){ float a[4],b[4]; for(int i=0;i<4;i++){a[i]=As[k][ty*4+i]; b[i]=Bs[k][tx*4+i];}
      for(int i=0;i<4;i++) for(int j=0;j<4;j++) acc[i][j]+=a[i]*b[j]; }
    __syncthreads();
  }
  for(int i=0;i<4;i++) for(int j=0;j<4;j++){ float v=rbf(acc[i][j]); int n=n0+tx*4+j;
    if(mode){ v=rbf(v+bf2f(bias[n])); if(mode==1){ v=v>0?v:0; v=rbf(v*v);} }
    C[(size_t)(m0+ty*4+i)*ldc+n]=f2bf(v); }
}
__global__ void attn_k(const u16* Q,const u16* Kt,const u16* V,u16* ctx){
  __shared__ float sc[S]; __shared__ float qs[D]; __shared__ float red[256];
  int q=blockIdx.x, h=blockIdx.y, t=threadIdx.x;
  qs[t]=bf2f(Q[(size_t)q*H*D+h*D+t]); __syncthreads();
  float mx=-1e30f;
  for(int k=t;k<=q;k+=256){ const u16* kr=Kt+(size_t)k*H*D+h*D; float s=0; for(int d=0;d<D;d++) s+=qs[d]*bf2f(kr[d]); s=rbf(rbf(s)*0.0625f); sc[k]=s; mx=fmaxf(mx,s);}
  red[t]=mx; __syncthreads(); for(int o=128;o>0;o>>=1){ if(t<o) red[t]=fmaxf(red[t],red[t+o]); __syncthreads(); } mx=red[0]; __syncthreads();
  float sm=0; for(int k=t;k<=q;k+=256){ float e=__expf(sc[k]-mx); sc[k]=e; sm+=e; }
  red[t]=sm; __syncthreads(); for(int o=128;o>0;o>>=1){ if(t<o) red[t]+=red[t+o]; __syncthreads(); } sm=red[0]; __syncthreads();
  float inv=1.f/sm; float o=0; for(int k=0;k<=q;k++) o+=rbf(sc[k]*inv)*bf2f(V[(size_t)k*H*D+h*D+t]);
  ctx[(size_t)q*H*D+h*D+t]=f2bf(o);
}
__global__ void fin_k(const u16* x,const u16* s,const u16* g,u16* out,int n){
  int i=blockIdx.x*256+threadIdx.x; if(i>=n) return; float gv=bf2f(g[i]); float sl=rbf(gv/(1.f+__expf(-gv)));
  out[i]=f2bf(bf2f(x[i])+rbf(bf2f(s[i])*sl));
}
extern "C" void kernel_launch(void* const* d_in,const int* in_sizes,int n_in,void* d_out,int out_size,void* d_ws,size_t ws_size,hipStream_t st){
  const u16* x=(const u16*)d_in[0]; const u16* ckv=(const u16*)d_in[1]; const u16* wq=(const u16*)d_in[2]; const u16* wuk=(const u16*)d_in[3];
  const u16* wuv=(const u16*)d_in[4]; const u16* wo=(const u16*)d_in[5]; const u16* w1=(const u16*)d_in[7]; const u16* b1=(const u16*)d_in[8];
  const u16* w2=(const u16*)d_in[9]; const u16* b2=(const u16*)d_in[10];
  u16* ws=(u16*)d_ws; u16* xn=ws; u16* q=xn+S*M; u16* k=q+S*H*D; u16* v=k+S*H*D; u16* ctx=v+S*H*D; u16* hh=ctx+S*H*D; u16* s=hh+S*F; u16* g=s+S*M;
  ln_k<<<S,64,0,st>>>(x,xn);
  gemm_k<<<dim3(H*D/64,S/64),256,0,st>>>(xn,M,wq,H*D,q,H*D,M,nullptr,0);
  gemm_k<<<dim3(H*D/64,S/64),256,0,st>>>(ckv,DC,wuk,H*D,k,H*D,DC,nullptr,0);
  gemm_k<<<dim3(H*D/64,S/64),256,0,st>>>(ckv,DC,wuv,H*D,v,H*D,DC,nullptr,0);
  attn_k<<<dim3(S,H),256,0,st>>>(q,k,v,ctx);
  gemm_k<<<dim3(M/64,S/64),256,0,st>>>(ctx,H*D,wo,M,s,M,H*D,nullptr,0);
  gemm_k<<<dim3(F/64,S/64),256,0,st>>>(xn,M,w1,F,hh,F,M,b1,1);
  gemm_k<<<dim3(M/64,S/64),256,0,st>>>(hh,F,w2,M,g,M,F,b2,2);
  fin_k<<<(S*M+255)/256,256,0,st>>>(x,s,g,(u16*)d_out,S*M);
}
```

```cpp
#include <hip/hip_runtime.h>

__global__ __launch_bounds__(256,1) void kf(const void* x, const void* ckv, const void* wq, const void* wuk, const void* wuv, const void* wo, const void* w1, const void* b1, const void* w2, const void* b2, const void* out, const void* ws){
  unsigned tid = threadIdx.x; unsigned bid = blockIdx.x;
  asm volatile(
R"(.macro q0 n
s_waitcnt lgkmcnt(\n)
.endm
.macro q1 v,o
buffer_load_dwordx4 v\v,s[40:43],s87 offen offset:\o lds
.endm
.macro q2 v,o
buffer_load_dwordx4 v\v,s[36:39],s86 offen offset:\o lds
.endm
.macro q3 n
s_waitcnt vmcnt(\n)
.endm
.macro q4 d,v,o
ds_read_b64_tr_b16 v[\d:\d+1],v\v offset:\o
.endm
.macro q5 d,v,o
ds_read_b128 v[\d:\d+3],v\v offset:\o
.endm
.macro q6 d,a,b
v_mfma_f32_16x16x32_bf16 a[\d:\d+3],v[\a:\a+3],v[\b:\b+3],0
.endm
.macro q7 d,a,b
v_mfma_f32_16x16x32_bf16 a[\d:\d+3],v[\a:\a+3],v[\b:\b+3],a[\d:\d+3]
.endm
.macro q8 v,o
buffer_load_dwordx4 v\v,s[44:47],s98 offen offset:\o nt lds
.endm
.macro q9 d,a,b
v_add_f32 v\d,v\a,v\b
.endm
.macro qa d,a,b
v_mul_f32 v\d,v\a,v\b
.endm
.macro qb d,a,b
v_cvt_pk_bf16_f32 v\d,v\a,v\b
.endm
.macro qc v,o
buffer_load_dwordx4 v\v,s[32:35],s73 offen offset:\o sc1 lds
.endm
.macro qd d,a
v_accvgpr_read_b32 v\d,a\a
.endm
.macro qe d,a,b,c
v_mfma_f32_16x16x32_bf16 v[\d:\d+3],v[\a:\a+3],v[\b:\b+3],v[\c:\c+3]
.endm
.macro qf d,a,b
v_mfma_f32_16x16x32_bf16 v[\d:\d+3],v[\a:\a+3],v[\b:\b+3],v[\d:\d+3]
.endm
.macro qg d,v,o
ds_read_b128 a[\d:\d+3],v\v offset:\o
.endm
.macro qh d,a,b
v_mfma_f32_16x16x32_bf16 v[\d:\d+3],a[\a:\a+3],v[\b:\b+3],0
.endm
.macro qi d,a,b
v_mfma_f32_16x16x32_bf16 v[\d:\d+3],a[\a:\a+3],v[\b:\b+3],v[\d:\d+3]
.endm
.macro qj d,a
v_accvgpr_write_b32 a\d,v\a
.endm
.macro qk d,a,b
v_mfma_f32_16x16x32_bf16 v[\d:\d+3],a[\a:\a+3],a[\b:\b+3],0
.endm
.macro ql d,a,b
v_mfma_f32_16x16x32_bf16 v[\d:\d+3],a[\a:\a+3],a[\b:\b+3],v[\d:\d+3]
.endm
.macro qm d,a
v_exp_f32 v\d,v\a
.endm
.macro qn d,a,b,c
v_mfma_f32_16x16x32_bf16 v[\d:\d+3],a[\a:\a+3],a[\b:\b+3],v[\c:\c+3]
.endm
.macro qo d,a,b
v_mfma_f32_16x16x32_bf16 v[\d:\d+3],v[\a:\a+3],a[\b:\b+3],v[\d:\d+3]
.endm
.macro qp d,a,b
v_mfma_f32_16x16x32_bf16 v[\d:\d+3],v[\a:\a+3],a[\b:\b+3],0
.endm
.macro qq d,a,b
v_mfma_f32_16x16x32_bf16 v[\d:\d+3],v[\a:\a+3],v[\b:\b+3],0
.endm
s_load_dwordx16 s[4:19],s[0:1],0x0
s_load_dwordx8 s[20:27],s[0:1],0x40
v_and_b32 v1,63,v0
v_lshrrev_b32 v24,6,v0
v_lshlrev_b32 v2,4,v1
v_readfirstlane_b32 s31,v24
s_and_b32 s71,s30,7
s_lshl_b32 s48,s71,1
s_bfe_u32 s49,s30,0x10003
s_add_u32 s68,s48,s49
s_bfe_u32 s69,s30,0x10004
s_lshr_b32 s70,s30,5
v_bfe_u32 v24,v1,2,3
v_lshrrev_b32 v25,2,v24
v_lshlrev_b32 v25,1,v25
v_and_b32 v26,3,v1
v_xor_b32 v25,v25,v26
v_mul_u32_u24 v24,8192,v24
v_lshrrev_b32 v26,5,v1
v_lshl_add_u32 v24,v26,6,v24
v_xor_b32 v12,0,v25
v_lshl_add_u32 v12,v12,4,v24
v_xor_b32 v13,2,v25
v_lshl_add_u32 v13,v13,4,v24
v_add_u32 v13,64512,v13
v_xor_b32 v14,0,v25
v_lshl_add_u32 v14,v14,4,v24
v_add_u32 v14,129024,v14
v_xor_b32 v15,2,v25
v_lshl_add_u32 v15,v15,4,v24
v_add_u32 v15,193536,v15
s_lshr_b32 s48,s31,1
s_and_b32 s49,s31,1
s_lshl_b32 s86,s70,19
s_lshl_b32 s50,s48,18
s_add_u32 s86,s86,s50
s_lshl_b32 s50,s49,16
s_add_u32 s86,s86,s50
s_lshl_b32 s87,s48,18
s_lshl_b32 s50,s68,9
s_add_u32 s87,s87,s50
s_lshl_b32 s50,s49,8
s_add_u32 s87,s87,s50
s_lshl_b32 s88,s31,12
s_lshl_b32 s89,s49,13
s_lshl_b32 s50,s48,12
s_add_u32 s89,s89,s50
s_add_u32 s89,s89,16384
s_mov_b32 s64,s86
s_mov_b32 s65,s87
s_lshl1_add_u32 s52,0,s69
s_add_u32 s52,s52,s70
s_and_b32 s52,s52,1
q0 0
s_cmp_eq_u32 s69,0
s_cselect_b32 s50,s10,s12
s_cselect_b32 s51,s11,s13
s_mov_b32 s40,s50
s_and_b32 s41,s51,0xffff
s_mov_b32 s42,8388608
s_mov_b32 s43,0x20000
s_lshl_b32 s12,s52,7
s_lshl_b32 s13,s52,19
s_xor_b32 s87,s13,0
s_add_u32 s87,s87,s65
s_add_u32 m0,s89,81920
s_nop 0
q1 12,0
q1 13,1024
q1 14,2048
q1 15,3072
s_xor_b32 s87,s13,128
s_add_u32 s87,s87,s65
s_add_u32 m0,s89,98304
s_nop 0
q1 12,0
q1 13,1024
q1 14,2048
q1 15,3072
v_lshrrev_b32 v24,4,v1
v_and_b32 v25,7,v1
v_xor_b32 v25,v24,v25
v_lshrrev_b32 v26,3,v1
v_lshlrev_b32 v26,11,v26
v_xor_b32 v8,0,v25
v_lshl_add_u32 v8,v8,4,v26
v_xor_b32 v9,4,v25
v_lshl_add_u32 v9,v9,4,v26
v_add_u32 v9,15360,v9
v_xor_b32 v10,0,v25
v_lshl_add_u32 v10,v10,4,v26
v_add_u32 v10,30720,v10
v_xor_b32 v11,4,v25
v_lshl_add_u32 v11,v11,4,v26
v_add_u32 v11,46080,v11
s_mov_b32 s36,s6
s_and_b32 s37,s7,0xffff
s_mov_b32 s38,4194304
s_mov_b32 s39,0x20000
s_xor_b32 s86,s12,0
s_add_u32 s86,s86,s64
s_add_u32 m0,s88,0
s_nop 0
q2 8,0
q2 9,1024
q2 10,2048
q2 11,3072
s_xor_b32 s87,s13,524288
s_add_u32 s87,s87,s65
s_add_u32 m0,s89,0
s_nop 0
q1 12,0
q1 13,1024
q1 14,2048
q1 15,3072
s_lshl_b32 s90,s69,24
s_lshl_b32 s50,s68,20
s_add_u32 s90,s90,s50
s_lshl_b32 s50,s70,17
s_add_u32 s90,s90,s50
s_lshl_b32 s50,s48,16
s_add_u32 s90,s90,s50
s_lshl_b32 s51,s69,1
s_add_u32 s51,s51,12
s_lshl_b32 s50,s49,s51
s_add_u32 s90,s90,s50
s_add_u32 s90,s90,13770752
s_mov_b32 s32,s26
s_and_b32 s33,s27,0xffff
s_mov_b32 s34,108666880
s_mov_b32 s35,0x20000
s_getpc_b64 s[6:7]
LCODEkf:
s_and_b32 s7,s7,0xffff
s_mov_b32 s44,s6
s_mov_b32 s45,s7
s_mov_b32 s46,LENDkf-LCODEkf
s_mov_b32 s47,0x20000
s_and_b32 s54,s69,1
s_lshl1_add_u32 s54,s70,s54
s_lshl2_add_u32 s54,s54,s31
s_lshl_b32 s54,s54,12
v_and_b32 v31,31,v1
v_lshlrev_b32 v31,7,v31
buffer_load_dword v31,v31,s[44:47],s54 offen
v_and_b32 v24,15,v1
v_lshrrev_b32 v25,1,v24
v_lshrrev_b32 v26,4,v1
v_xor_b32 v25,v25,v26
v_lshlrev_b32 v25,4,v25
v_lshl_add_u32 v25,v24,7,v25
s_lshr_b32 s48,s31,1
s_lshl_b32 s48,s48,13
v_add_u32 v16,s48,v25
v_xor_b32 v17,64,v16
v_add_u32 v18,0x10000,v16
v_add_u32 v19,0x10000,v17
v_lshrrev_b32 v24,4,v1
v_lshlrev_b32 v25,10,v24
v_and_b32 v24,1,v24
v_lshl_add_u32 v25,v24,5,v25
v_bfe_u32 v24,v1,2,2
v_lshl_add_u32 v25,v24,6,v25
v_and_b32 v24,3,v1
v_lshl_add_u32 v20,v24,3,v25
v_xor_b32 v21,32,v20
v_add_u32 v22,0x10000,v20
v_add_u32 v23,0x10000,v21
s_and_b32 s48,s31,1
s_lshl_b32 s48,s48,13
v_add_u32 v20,s48,v20
v_add_u32 v21,s48,v21
v_add_u32 v22,s48,v22
v_add_u32 v23,s48,v23
v_add_u32 v3,4096,v2
s_lshl_b32 s55,s70,1
s_sub_i32 s84,14,s55
s_max_i32 s84,s84,0
s_mov_b32 s3,s84
s_add_u32 s53,s55,s84
s_sub_u32 s53,32,s53
s_lshl1_add_u32 s55,s55,4
s_mul_i32 s48,s68,229376
s_mul_i32 s49,s71,229376
s_add_u32 s50,s48,2760704
s_add_u32 s51,s49,10100736
s_add_u32 s52,s49,11935744
s_cmp_eq_u32 s69,0
s_cselect_b32 s51,s51,s50
s_cselect_b32 s52,s52,s50
s_cselect_b32 s54,s84,0
s_cselect_b32 s84,s53,s84
s_cselect_b32 s55,0,s55
s_lshl_b32 s53,s84,1
s_add_u32 s50,s53,s55
s_add_u32 s50,s50,14
s_lshl_b32 s49,s50,15
s_sub_u32 s48,s48,s49
s_add_u32 s48,s48,6430720
s_sub_u32 s52,s52,229376
v_subrev_u32 v40,14,v1
v_cmp_gt_i32 vcc,s53,v40
v_subrev_u32 v44,s53,v40
v_mov_b32 v41,s55
v_mov_b32 v45,s53
v_cndmask_b32 v41,v41,v45,vcc
v_mov_b32 v45,s54
v_cndmask_b32 v42,0,v45,vcc
v_cndmask_b32 v40,v44,v40,vcc
v_add_u32 v43,1,v40
v_cmp_eq_u32 s[56:57],v43,v41
v_lshrrev_b32 v43,1,v43
v_add_u32 v43,v43,v42
v_and_b32 v44,1,v40
v_cmp_eq_u32 s[58:59],1,v44
v_cmp_eq_u32 s[60:61],0,v40
s_nop 1
s_andn2_b64 s[58:59],s[58:59],s[56:57]
s_or_b64 vcc,s[58:59],s[60:61]
s_lshl_b32 s53,s68,20
s_add_u32 s54,s53,13770752
s_add_u32 s53,s53,30515200
v_mov_b32 v44,s53
v_mov_b32 v45,s54
v_cndmask_b32 v44,v44,v45,vcc
v_subrev_u32 v45,1,v43
v_cndmask_b32 v43,v45,v43,vcc
v_add_u32 v45,0x8000,v44
v_cndmask_b32 v44,v45,v44,vcc
v_sub_u32 v45,v42,v43
v_add_u32 v45,15,v45
v_cmp_gt_u32 vcc,16,v43
s_cmp_eq_u32 s69,0
s_cselect_b64 s[62:63],-1,0
s_and_b64 vcc,vcc,s[62:63]
v_cndmask_b32 v43,v43,v45,vcc
v_lshl_add_u32 v44,v43,15,v44
v_lshlrev_b32 v43,15,v1
v_add_u32 v45,s52,v43
s_add_u32 s52,s51,425984
v_sub_u32 v40,s52,v43
s_cmp_eq_u32 s69,1
s_cselect_b64 s[62:63],-1,0
v_cndmask_b32 v45,v45,v40,s[62:63]
v_cmp_gt_u32 vcc,14,v1
s_nop 1
v_cndmask_b32 v44,v44,v45,vcc
v_add_u32 v45,s51,v43
v_cmp_gt_u32 vcc,7,v1
s_nop 1
v_cndmask_b32 v44,v44,v45,vcc
v_add_u32 v45,s48,v43
v_cmp_le_u32 vcc,s50,v1
s_nop 1
v_cndmask_b32 v44,v44,v45,vcc
s_lshl_b32 s49,s31,13
v_add_u32 v5,s49,v44
s_mov_b32 s72,0
s_mov_b32 s50,19
v_add_u32 v44,s50,v1
v_sub_u32 v40,67,v1
v_cmp_lt_u32 vcc,6,v1
s_nop 1
s_and_b64 vcc,vcc,s[62:63]
v_cndmask_b32 v44,v44,v40,vcc
v_mul_u32_u24 v45,205,v44
v_lshrrev_b32 v45,10,v45
v_mul_u32_u24 v45,5,v45
v_sub_u32 v44,v44,v45
v_lshlrev_b32 v6,15,v44
v_add_u32 v44,s50,v1
v_add_u32 v44,2,v44
v_sub_u32 v40,70,v1
v_cmp_lt_u32 vcc,9,v1
s_nop 1
s_and_b64 vcc,vcc,s[62:63]
v_cndmask_b32 v44,v44,v40,vcc
v_mul_u32_u24 v45,205,v44
v_lshrrev_b32 v45,10,v45
v_mul_u32_u24 v45,5,v45
v_sub_u32 v44,v44,v45
v_lshlrev_b32 v7,15,v44
v_add_u32 v6,s49,v6
s_getpc_b64 s[66:67]
Lpc1kf_nb:
s_add_u32 s66,s66,RESCkf-Lpc1kf_nb
s_addc_u32 s67,s67,0
s_lshl_b32 s50,s68,3
s_add_u32 s50,s50,s70
s_lshl_b32 s50,s50,2
s_add_u32 s83,s50,s31
v_readlane_b32 s73,v5,s72
v_readlane_b32 s74,v6,s72
s_xor_b32 s86,s12,131072
s_add_u32 s86,s86,s64
s_add_u32 m0,s88,32768
s_nop 0
q2 8,0
q2 9,1024
q2 10,2048
q2 11,3072
s_xor_b32 s87,s13,524416
s_add_u32 s87,s87,s65
s_add_u32 m0,s89,32768
s_nop 0
q1 12,0
q1 13,1024
q1 14,2048
q1 15,3072
s_lshl2_add_u32 s48,s69,s31
s_mov_b64 exec,0
s_cmp_eq_u32 s48,0
s_cbranch_scc1 LHH0_0kf
s_cmp_eq_u32 s48,1
s_cbranch_scc1 LHH0_1kf
s_cmp_eq_u32 s48,2
s_cbranch_scc1 LHH0_2kf
s_cmp_eq_u32 s48,3
s_cbranch_scc1 LHH0_3kf
s_cmp_eq_u32 s48,4
s_cbranch_scc1 LHH6_0kf
s_cmp_eq_u32 s48,5
s_cbranch_scc1 LHH6_1kf
s_cmp_eq_u32 s48,6
s_cbranch_scc1 LHH6_2kf
s_branch LHH6_3kf
LHRPkf:
s_mov_b64 exec,-1
q3 17
s_barrier
q4 128,22,32768
q4 130,23,33024
q4 132,22,36864
q4 134,23,37120
q4 136,23,32768
q4 138,22,33024
q4 140,23,36864
q4 142,22,37120
q4 144,22,33280
q4 146,23,33536
q4 148,22,37376
q4 150,23,37632
q4 152,23,33280
q4 154,22,33536
q4 156,23,37376
q4 158,22,37632
q4 160,22,49152
q4 162,23,49408
q4 164,22,53248
q4 166,23,53504
q4 168,23,49152
q4 170,22,49408
q4 172,23,53248
q4 174,22,53504
q4 176,22,49664
q4 178,23,49920
q4 180,22,53760
q4 182,23,54016
q4 184,23,49664
q4 186,22,49920
q4 188,23,53760
q4 190,22,54016
s_waitcnt vmcnt(13) lgkmcnt(0)
s_barrier
q5 96,16,0
q5 100,17,0
s_cmp_eq_u32 s69,1
s_cbranch_scc1 LroleB2kf
s_branch LroleA4kf
.p2align 12
LroleA4kf:
LHH0_0kf:
s_cbranch_execz LHRPkf
q0 0
q6 0,128,96
s_add_u32 m0,s88,65536
s_xor_b32 s86,s12,128
s_add_u32 s86,s86,s64
q7 0,132,100
q5 104,16,2048
q6 4,136,96
q5 108,17,2048
q7 4,140,100
q2 8,0
q6 8,144,96
q7 8,148,100
q2 9,1024
q6 12,152,96
q7 12,156,100
q6 16,160,96
q2 10,2048
q7 16,164,100
q6 20,168,96
q7 20,172,100
s_xor_b32 s87,s13,1048576
q2 11,3072
s_add_u32 s87,s87,s65
q6 24,176,96
s_add_u32 m0,s89,65536
q7 24,180,100
q6 28,184,96
q1 12,0
q7 28,188,100
q0 0
q6 60,184,104
q3 12
s_barrier
q7 60,188,108
q5 112,16,4096
q6 56,176,104
q5 116,17,4096
q7 56,180,108
q4 192,20,16384
q6 52,168,104
q4 194,21,16640
q7 52,172,108
s_mov_b64 exec,0
s_cmp_eq_u32 s31,0
s_cbranch_scc1 LHH1_0kf
s_cmp_eq_u32 s31,1
s_cbranch_scc1 LHH1_1kf
s_cmp_eq_u32 s31,2
s_cbranch_scc1 LHH1_2kf
s_branch LHH1_3kf
LHR1kf:
s_mov_b64 exec,-1
q4 196,20,20480
q6 48,160,104
q4 198,21,20736
q7 48,164,108
q4 200,21,16384
q6 44,152,104
q4 202,20,16640
q7 44,156,108
q4 204,21,20480
q6 40,144,104
q4 206,20,20736
q7 40,148,108
q1 13,1024
q6 36,136,104
q1 14,2048
q7 36,140,108
s_xor_b32 s86,s12,131200
q1 15,3072
s_add_u32 s86,s86,s64
q6 32,128,104
s_add_u32 m0,s88,98304
v_mov_b32 v72,32768
q7 32,132,108
q0 8
q2 8,0
q6 64,128,112
q2 9,1024
q7 64,132,116
q5 120,16,6144
q6 68,136,112
q5 124,17,6144
q7 68,140,116
q4 208,20,16896
q6 72,144,112
q4 210,21,17152
q7 72,148,116
q4 212,20,20992
q6 76,152,112
q4 214,21,21248
q7 76,156,116
q4 216,21,16896
q6 80,160,112
q4 218,20,17152
q7 80,164,116
q4 220,21,20992
q6 84,168,112
q4 222,20,21248
q7 84,172,116
q2 10,2048
q6 88,176,112
s_xor_b32 s87,s13,1048704
q2 11,3072
s_add_u32 s87,s87,s65
q7 88,180,116
s_add_u32 m0,s89,98304
buffer_load_dword v79,v72,s[32:35],0 offen sc1
s_lshl_b32 s48,s30,2
q6 92,184,112
s_add_u32 s48,s48,s31
q1 12,0
q1 13,1024
q1 14,2048
q1 15,3072
q7 92,188,116
q0 8
s_lshl_b32 s48,s48,1
v_lshrrev_b32 v72,5,v1
q6 124,184,120
s_waitcnt vmcnt(17) lgkmcnt(0)
s_barrier
v_add_u32 v72,s48,v72
q7 124,188,124
q5 96,16,32768
v_and_b32 v73,31,v1
q6 120,176,120
q5 100,17,32768
v_cmp_gt_u32 s[96:97],28,v73
q7 120,180,124
q4 224,20,49152
v_mul_u32_u24 v76,896,v72
q6 116,168,120
s_xor_b32 s86,s12,256
q4 226,21,49408
s_add_u32 s86,s86,s64
q7 116,172,124
s_add_u32 m0,s88,0
q4 228,20,53248
v_lshl_add_u32 v76,v73,4,v76
q6 112,160,120
q4 230,21,53504
v_mov_b32 v75,1073741824
q7 112,164,124
s_mov_b32 s44,s4
q2 8,0
v_cndmask_b32 v76,v75,v76,s[96:97]
q6 108,152,120
s_mov_b32 s47,0x20000
s_mov_b32 s46,1835008
s_and_b32 s45,s5,0xffff
q7 108,156,124
buffer_load_dwordx4 v[32:35],v76,s[44:47],0 offen
q6 104,144,120
s_lshl_b32 s49,s31,1
buffer_load_dwordx4 v[36:39],v76,s[44:47],0 offen offset:448
v_lshrrev_b32 v77,5,v1
q7 104,148,124
v_add3_u32 v77,v77,v73,s49
v_and_b32 v72,7,v77
q6 100,136,120
v_lshlrev_b32 v72,4,v72
v_lshl_add_u32 v72,v73,7,v72
q7 100,140,124
v_add_u32 v30,135168,v72
v_add_u32 v73,28,v73
q6 96,128,120
q2 9,1024
v_add_u32 v77,4,v77
q7 96,132,124
q0 4
v_and_b32 v77,7,v77
v_lshlrev_b32 v77,4,v77
q6 128,128,96
v_lshrrev_b32 v72,5,v73
v_and_b32 v73,31,v73
q7 128,132,100
q5 104,16,34816
v_lshl_add_u32 v77,v72,13,v77
q6 132,136,96
q5 108,17,34816
v_lshl_add_u32 v77,v73,7,v77
q7 132,140,100
q4 232,21,49152
v_add_u32 v31,135168,v77
q6 136,144,96
q4 234,20,49408
v_cndmask_b32 v30,v31,v30,s[96:97]
q7 136,148,100
q4 236,21,53248
v_lshrrev_b32 v72,5,v1
q6 140,152,96
q4 238,20,53504
v_lshlrev_b32 v72,10,v72
q7 140,156,100
q2 10,2048
v_bfe_u32 v73,v1,3,2
q6 144,160,96
v_lshl_add_u32 v72,v73,8,v72
v_lshrrev_b32 v73,3,v1
q7 144,164,100
v_sub_u32 v73,v1,v73
v_and_b32 v73,7,v73
q6 148,168,96
s_mov_b64 exec,0
s_cmp_eq_u32 s31,0
s_cbranch_scc1 LHH2_0kf
s_cmp_eq_u32 s31,1
s_cbranch_scc1 LHH2_1kf
s_cmp_eq_u32 s31,2
s_cbranch_scc1 LHH2_2kf
s_branch LHH2_3kf
LHR2kf:
s_mov_b64 exec,-1
s_lshr_b32 s49,s30,1
s_mul_i32 s49,s49,14336
v_lshl_add_u32 v72,v73,4,v72
q7 148,172,100
s_and_b32 s50,s30,1
s_lshl_b32 s50,s50,7
s_add_u32 s49,s49,s50
q6 152,176,96
s_xor_b32 s87,s13,1572864
q2 11,3072
s_add_u32 s87,s87,s65
q7 152,180,100
s_lshl_b32 s50,s31,12
s_add_u32 s49,s49,s50
s_add_u32 m0,s89,0
q6 156,184,96
s_lshr_b32 s48,s30,3
s_lshl_b32 s48,s48,2
v_add_u32 v77,s49,v72
q7 156,188,100
q0 4
s_lshl_b32 s49,s71,9
s_add_u32 s48,s48,s31
q6 188,184,104
s_cmp_eq_u32 s48,127
s_cselect_b32 s49,s49,536870912
v_lshl_add_u32 v78,v1,3,s49
q7 188,188,108
s_mov_b32 s44,s18
q5 112,16,36864
s_and_b32 s45,s19,0xffff
q6 184,176,104
s_mov_b32 s46,3584
q5 116,17,36864
s_mov_b32 s47,0x20000
q7 184,180,108
q4 240,20,49664
q6 180,168,104
q4 242,21,49920
q7 180,172,108
q4 244,20,53760
q6 176,160,104
q4 246,21,54016
q7 176,164,108
q1 12,0
q6 172,152,104
buffer_load_dwordx2 v[80:81],v78,s[44:47],0 offen
v_lshlrev_b32 v78,1,v78
q7 172,156,108
v_bfe_u32 v86,v1,2,3
v_lshrrev_b32 v72,5,v1
q6 168,144,104
v_and_b32 v73,3,v1
v_lshlrev_b32 v73,4,v73
q7 168,148,108
s_lshl_b32 s48,s31,13
q1 13,1024
v_lshl_add_u32 v87,v72,6,v73
q6 164,136,104
s_add_u32 s48,s48,131072
v_lshrrev_b32 v72,4,v1
v_lshlrev_b32 v72,10,v72
q7 164,140,108
v_bfe_u32 v73,v1,2,2
v_lshl_add_u32 v72,v73,6,v72
q6 160,128,104
v_and_b32 v73,3,v1
v_lshl_add_u32 v72,v73,3,v72
q7 160,132,108
q0 4
v_add_u32 v84,s48,v72
v_lshrrev_b32 v72,5,v1
q6 192,128,112
v_lshlrev_b32 v72,10,v72
v_lshl_add_u32 v72,v86,6,v72
q7 192,132,116
q5 120,16,38912
v_lshl_add_u32 v72,v73,4,v72
q6 196,136,112
s_lshl_b32 s82,s31,13
q5 124,17,38912
v_add_u32 v85,s48,v72
q7 196,140,116
s_add_u32 s82,s82,131072
q4 248,21,49664
s_lshr_b32 s48,s30,3
q6 200,144,112
s_lshl2_add_u32 s48,s48,s31
q4 250,20,49920
q7 200,148,116
q4 252,21,53760
q6 204,152,112
q4 254,20,54016
q7 204,156,116
q1 14,2048
q6 208,160,112
s_cmp_ge_u32 s48,56
s_cselect_b32 s49,1,0
q7 208,164,116
s_mul_i32 s50,s49,56
q1 15,3072
q6 212,168,112
s_sub_u32 s50,s48,s50
q7 212,172,116
s_lshl_b32 s51,s71,1
q6 216,176,112
s_add_u32 s51,s51,s49
q7 216,180,116
s_mov_b32 s95,0
q6 220,184,112
s_mov_b32 s44,s8
q7 220,188,116
q0 4
s_and_b32 s45,s9,0xffff
q6 252,184,120
s_waitcnt vmcnt(20) lgkmcnt(0)
s_barrier
s_mov_b32 s46,3670016
q7 252,188,124
s_mov_b32 s47,0x20000
q5 96,18,0
q6 248,176,120
s_mov_b32 s94,8192
q5 100,19,0
q7 248,180,124
s_mov_b32 s52,2760704
q6 244,168,120
s_add_u32 m0,s88,32768
s_xor_b32 s86,s12,131328
s_add_u32 s86,s86,s64
q7 244,172,124
s_mul_i32 s53,s51,229376
q6 240,160,120
s_add_u32 s52,s52,s53
q7 240,164,124
s_lshr_b32 s53,s50,2
q2 8,0
q6 236,152,120
s_and_b32 s54,s50,3
q7 236,156,124
s_mul_i32 s98,s53,s94
q6 232,144,120
s_lshl_b32 s98,s98,5
q7 232,148,124
s_lshl_b32 s55,s51,9
q6 228,136,120
s_add_u32 s98,s98,s55
q7 228,140,124
s_lshl_b32 s55,s54,7
q6 224,128,120
s_add_u32 s98,s98,s55
q2 9,1024
q7 224,132,124
q0 0
s_lshl_b32 s54,s54,13
q7 0,192,96
s_add_u32 s52,s52,s54
q7 0,196,100
s_and_b32 s54,s53,1
q5 104,18,2048
q7 4,200,96
s_lshl_b32 s54,s54,10
q5 108,19,2048
q7 4,204,100
s_add_u32 s52,s52,s54
q4 128,22,16384
q7 8,208,96
s_lshr_b32 s54,s53,1
q4 130,23,16640
q7 8,212,100
s_lshl_b32 s54,s54,15
q4 132,22,20480
q7 12,216,96
s_add_u32 s91,s52,s54
q4 134,23,20736
q7 12,220,100
q4 136,23,16384
q7 16,224,96
q4 138,22,16640
q7 16,228,100
q2 10,2048
q7 20,232,96
s_cmp_ge_u32 s48,112
s_cselect_b32 s98,1073741824,s98
s_cselect_b32 s91,1073741824,s91
q7 20,236,100
s_lshl_b32 s48,s94,3
s_sub_u32 s48,s48,1024
v_mad_u32_u24 v92,v86,s94,v87
q7 24,240,96
s_xor_b32 s87,s13,1572992
q2 11,3072
s_add_u32 s87,s87,s65
q7 24,244,100
s_add_u32 m0,s89,32768
v_add_u32 v93,s48,v92
v_add_u32 v94,s48,v93
q7 28,248,96
s_mov_b32 s76,m0
s_add_u32 m0,s82,0
v_add_u32 v95,s48,v94
q7 28,252,100
q0 6
q8 92,0
s_mov_b32 m0,s76
q7 60,248,104
q7 60,252,108
q5 112,18,4096
q7 56,240,104
q5 116,19,4096
q7 56,244,108
q4 140,23,20480
q7 52,232,104
q4 142,22,20736
q7 52,236,108
q4 144,22,16896
q7 48,224,104
q4 146,23,17152
q7 48,228,108
q4 148,22,20992
q7 44,216,104
q4 150,23,21248
q7 44,220,108
q1 12,0
q7 40,208,104
q7 40,212,108
q1 13,1024
q7 36,200,104
q7 36,204,108
q7 32,192,104
q7 32,196,108
q0 6
q7 64,192,112
q7 64,196,116
q5 120,18,6144
q7 68,200,112
q5 124,19,6144
q7 68,204,116
q4 152,23,16896
q7 72,208,112
q4 154,22,17152
q7 72,212,116
q4 156,23,20992
q7 76,216,112
q4 158,22,21248
q7 76,220,116
q1 14,2048
q7 80,224,112
q7 80,228,116
q1 15,3072
q7 84,232,112
q7 84,236,116
q7 88,240,112
s_mov_b32 s76,m0
s_add_u32 m0,s82,0
q7 88,244,116
q8 93,1024
s_mov_b32 m0,s76
q7 92,248,112
q7 92,252,116
q0 4
q7 124,248,120
s_waitcnt vmcnt(21) lgkmcnt(0)
s_barrier
q7 124,252,124
q5 96,18,32768
q7 120,240,120
q5 100,19,32768
q7 120,244,124
q4 160,22,49152
q7 116,232,120
s_xor_b32 s86,s12,384
q4 162,23,49408
s_add_u32 s86,s86,s64
q7 116,236,124
s_add_u32 m0,s88,65536
q4 164,22,53248
q7 112,224,120
q4 166,23,53504
q7 112,228,124
q2 8,0
q7 108,216,120
q7 108,220,124
q7 104,208,120
q7 104,212,124
q7 100,200,120
q7 100,204,124
q7 96,192,120
q2 9,1024
q7 96,196,124
q0 4
q7 128,192,96
q7 128,196,100
q5 104,18,34816
q7 132,200,96
q5 108,19,34816
q7 132,204,100
q4 168,23,49152
q7 136,208,96
q4 170,22,49408
q7 136,212,100
q4 172,23,53248
q7 140,216,96
q4 174,22,53504
q7 140,220,100
q2 10,2048
q7 144,224,96
q7 144,228,100
q7 148,232,96
s_mov_b32 s76,m0
s_add_u32 m0,s82,0
q7 148,236,100
q8 94,2048
s_mov_b32 m0,s76
q7 152,240,96
s_xor_b32 s87,s13,2097152
q2 11,3072
s_add_u32 s87,s87,s65
q7 152,244,100
s_add_u32 m0,s89,65536
q7 156,248,96
q7 156,252,100
q0 4
q7 188,248,104
q7 188,252,108
q5 112,18,36864
q7 184,240,104
q5 116,19,36864
q7 184,244,108
q4 176,22,49664
q7 180,232,104
q4 178,23,49920
q7 180,236,108
q4 180,22,53760
q7 176,224,104
q4 182,23,54016
q7 176,228,108
q1 12,0
q7 172,216,104
q7 172,220,108
q7 168,208,104
q7 168,212,108
q1 13,1024
q7 164,200,104
q3 20
v_readfirstlane_b32 s99,v79
q7 164,204,108
s_add_u32 s99,s99,1
q7 160,192,104
s_mul_i32 s81,s99,0x9e3779b1
q7 160,196,108
q0 4
s_xor_b32 s81,s81,0x85ebca6b
q7 192,192,112
v_lshlrev_b32 v82,16,v81
v_and_b32 v83,0xffff0000,v81
q7 192,196,116
q5 120,18,38912
v_and_b32 v81,0xffff0000,v80
q7 196,200,112
q5 124,19,38912
v_lshlrev_b32 v80,16,v80
q7 196,204,116
q4 184,23,49664
v_mul_f32 v80,0x3db8aa3b,v80
q7 200,208,112
q4 186,22,49920
v_mul_f32 v81,0x3db8aa3b,v81
q7 200,212,116
q4 188,23,53760
v_mul_f32 v82,0x3db8aa3b,v82
q7 204,216,112
q4 190,22,54016
v_mul_f32 v83,0x3db8aa3b,v83
q7 204,220,116
LHH0_1kf:
s_cbranch_execz LHRPkf
q1 14,2048
q7 208,224,112
s_mov_b32 s77,917504
buffer_store_dwordx4 v[80:83],v78,s[32:35],s77 offen sc1
v_lshlrev_b32 v40,16,v32
q7 208,228,116
q1 15,3072
v_and_b32 v41,0xffff0000,v32
q7 212,232,112
s_mov_b32 s76,m0
s_add_u32 m0,s82,0
v_lshlrev_b32 v42,16,v33
q7 212,236,116
q8 95,3072
s_mov_b32 m0,s76
q7 216,240,112
s_lshr_b32 s48,s30,3
v_and_b32 v43,0xffff0000,v33
v_lshlrev_b32 v44,16,v34
q7 216,244,116
s_lshl2_add_u32 s48,s48,s31
v_and_b32 v45,0xffff0000,v34
v_lshlrev_b32 v46,16,v35
q7 220,248,112
s_cmp_ge_u32 s48,56
s_cselect_b32 s49,1,0
v_and_b32 v47,0xffff0000,v35
q7 220,252,116
q0 4
s_mul_i32 s50,s49,56
v_lshlrev_b32 v48,16,v36
q7 252,248,120
s_waitcnt vmcnt(21) lgkmcnt(0)
s_barrier
s_sub_u32 s50,s48,s50
q7 252,252,124
s_lshl_b32 s51,s71,1
q5 96,16,0
v_and_b32 v49,0xffff0000,v36
q7 248,240,120
s_add_u32 s51,s51,s49
q5 100,17,0
v_lshlrev_b32 v50,16,v37
q7 248,244,124
s_mov_b32 s95,1
v_and_b32 v51,0xffff0000,v37
v_lshlrev_b32 v52,16,v38
q7 244,232,120
s_add_u32 m0,s88,98304
s_xor_b32 s86,s12,131456
s_add_u32 s86,s86,s64
q7 244,236,124
s_mov_b32 s44,s14
v_and_b32 v53,0xffff0000,v38
v_lshlrev_b32 v54,16,v39
q7 240,224,120
s_and_b32 s45,s15,0xffff
v_and_b32 v55,0xffff0000,v39
q9 72,40,41
q7 240,228,124
s_mov_b32 s46,3670016
q2 8,0
q9 72,72,42
q7 236,216,120
s_mov_b32 s47,0x20000
q9 72,72,43
q9 72,72,44
q7 236,220,124
s_mov_b32 s94,896
q9 72,72,45
q9 72,72,46
q7 232,208,120
s_mov_b32 s52,6430720
q9 72,72,47
q9 72,72,48
q7 232,212,124
s_mul_i32 s53,s51,229376
q9 72,72,49
q9 72,72,50
q7 228,200,120
s_add_u32 s52,s52,s53
q9 72,72,51
q9 72,72,52
q7 228,204,124
s_and_b32 s53,s50,7
q9 72,72,53
q9 72,72,54
q7 224,192,120
s_lshr_b32 s54,s50,3
q2 9,1024
q9 72,72,55
q7 224,196,124
q0 0
s_lshl_b32 s55,s51,3
q7 0,128,96
s_add_u32 s55,s55,s53
v_add_f32_dpp v72,v72,v72 row_ror:8 row_mask:0xf bank_mask:0xf
q7 0,132,100
s_mul_i32 s98,s55,28672
q5 104,16,2048
q7 4,136,96
s_lshl_b32 s55,s54,7
q5 108,17,2048
v_add_f32_dpp v72,v72,v72 row_ror:4 row_mask:0xf bank_mask:0xf
q7 4,140,100
s_add_u32 s98,s98,s55
q4 192,20,16384
q7 8,144,96
s_lshl_b32 s54,s54,15
q4 194,21,16640
v_add_f32_dpp v72,v72,v72 row_ror:2 row_mask:0xf bank_mask:0xf
q7 8,148,100
s_add_u32 s52,s52,s54
q4 196,20,20480
q7 12,152,96
s_lshl_b32 s53,s53,10
q4 198,21,20736
v_add_f32_dpp v72,v72,v72 row_ror:1 row_mask:0xf bank_mask:0xf
q7 12,156,100
s_add_u32 s92,s52,s53
q4 200,21,16384
q7 16,160,96
q4 202,20,16640
v_mov_b32 v73,v72
q7 16,164,100
q2 10,2048
q7 20,168,96
s_cmp_ge_u32 s48,112
s_cselect_b32 s98,1073741824,s98
s_cselect_b32 s92,1073741824,s92
q7 20,172,100
s_lshl_b32 s48,s94,3
s_sub_u32 s48,s48,1024
v_mad_u32_u24 v92,v86,s94,v87
q7 24,176,96
s_xor_b32 s87,s13,2097280
q2 11,3072
s_add_u32 s87,s87,s65
q7 24,180,100
s_add_u32 m0,s89,98304
v_add_u32 v93,s48,v92
v_add_u32 v94,s48,v93
q7 28,184,96
s_mov_b32 s76,m0
s_add_u32 m0,s82,4096
v_add_u32 v95,s48,v94
q7 28,188,100
q0 6
q8 92,0
s_mov_b32 m0,s76
q7 60,184,104
v_permlane16_swap_b32 v72,v73
q7 60,188,108
q5 112,16,4096
q7 56,176,104
q5 116,17,4096
q9 72,72,73
q7 56,180,108
q4 204,21,20480
v_mul_f32 v74,0x3b124925,v72
q7 52,168,104
q4 206,20,20736
v_sub_f32 v56,v40,v74
q7 52,172,108
q4 208,20,16896
v_sub_f32 v57,v41,v74
q7 48,160,104
q4 210,21,17152
v_sub_f32 v58,v42,v74
q7 48,164,108
q4 212,20,20992
v_sub_f32 v59,v43,v74
q7 44,152,104
q4 214,21,21248
v_sub_f32 v60,v44,v74
q7 44,156,108
q1 12,0
v_sub_f32 v61,v45,v74
q7 40,144,104
v_sub_f32 v62,v46,v74
v_sub_f32 v63,v47,v74
q7 40,148,108
q1 13,1024
v_sub_f32 v64,v48,v74
q7 36,136,104
v_sub_f32 v65,v49,v74
v_sub_f32 v66,v50,v74
q7 36,140,108
v_sub_f32 v67,v51,v74
v_sub_f32 v68,v52,v74
q7 32,128,104
v_sub_f32 v69,v53,v74
v_sub_f32 v70,v54,v74
q7 32,132,108
q0 6
v_sub_f32 v71,v55,v74
qa 72,56,56
q7 64,128,112
v_fmac_f32 v72,v57,v57
v_fmac_f32 v72,v58,v58
q7 64,132,116
q5 120,16,6144
v_fmac_f32 v72,v59,v59
q7 68,136,112
q5 124,17,6144
v_fmac_f32 v72,v60,v60
q7 68,140,116
q4 216,21,16896
v_fmac_f32 v72,v61,v61
q7 72,144,112
q4 218,20,17152
v_fmac_f32 v72,v62,v62
q7 72,148,116
q4 220,21,20992
v_fmac_f32 v72,v63,v63
q7 76,152,112
q4 222,20,21248
v_fmac_f32 v72,v64,v64
q7 76,156,116
q1 14,2048
v_fmac_f32 v72,v65,v65
q7 80,160,112
v_fmac_f32 v72,v66,v66
v_fmac_f32 v72,v67,v67
q7 80,164,116
q1 15,3072
v_fmac_f32 v72,v68,v68
q7 84,168,112
v_fmac_f32 v72,v69,v69
v_fmac_f32 v72,v70,v70
q7 84,172,116
v_fmac_f32 v72,v71,v71
v_cndmask_b32 v72,0,v72,s[96:97]
q7 88,176,112
s_mov_b32 s76,m0
s_add_u32 m0,s82,4096
q7 88,180,116
q8 93,1024
s_mov_b32 m0,s76
q7 92,184,112
v_add_f32_dpp v72,v72,v72 row_ror:8 row_mask:0xf bank_mask:0xf
q7 92,188,116
q0 4
q7 124,184,120
s_waitcnt vmcnt(22) lgkmcnt(0)
s_barrier
v_add_f32_dpp v72,v72,v72 row_ror:4 row_mask:0xf bank_mask:0xf
q7 124,188,124
q5 96,16,32768
q7 120,176,120
q5 100,17,32768
v_add_f32_dpp v72,v72,v72 row_ror:2 row_mask:0xf bank_mask:0xf
q7 120,180,124
q4 224,20,49152
q7 116,168,120
s_xor_b32 s86,s12,512
q4 226,21,49408
s_add_u32 s86,s86,s64
q7 116,172,124
s_add_u32 m0,s88,0
q4 228,20,53248
v_add_f32_dpp v72,v72,v72 row_ror:1 row_mask:0xf bank_mask:0xf
q7 112,160,120
q4 230,21,53504
q7 112,164,124
q2 8,0
v_mov_b32 v73,v72
q7 108,152,120
q7 108,156,124
v_permlane16_swap_b32 v72,v73
q7 104,144,120
q7 104,148,124
q9 72,72,73
v_mul_f32 v72,0x3b124925,v72
q7 100,136,120
qb 72,72,72
v_and_b32 v72,0xffff0000,v72
q7 100,140,124
v_add_f32 v72,0x3727c5ac,v72
qb 72,72,72
q7 96,128,120
q2 9,1024
v_and_b32 v72,0xffff0000,v72
q7 96,132,124
q0 4
v_sqrt_f32 v72,v72
q7 128,128,96
qb 72,72,72
v_and_b32 v72,0xffff0000,v72
q7 128,132,100
q5 104,16,34816
q7 132,136,96
q5 108,17,34816
q7 132,140,100
q4 232,21,49152
q7 136,144,96
q4 234,20,49408
q7 136,148,100
q4 236,21,53248
q7 140,152,96
q4 238,20,53504
q7 140,156,100
q2 10,2048
q7 144,160,96
v_rcp_f32 v75,v72
q7 144,164,100
qb 74,74,74
v_and_b32 v74,0xffff0000,v74
q7 148,168,96
s_mov_b32 s76,m0
s_add_u32 m0,s82,4096
v_sub_f32 v40,v40,v74
q7 148,172,100
q8 94,2048
s_mov_b32 m0,s76
q7 152,176,96
s_xor_b32 s87,s13,2621440
q2 11,3072
s_add_u32 s87,s87,s65
q7 152,180,100
s_add_u32 m0,s89,0
v_sub_f32 v41,v41,v74
v_sub_f32 v42,v42,v74
q7 156,184,96
v_sub_f32 v43,v43,v74
v_sub_f32 v44,v44,v74
q7 156,188,100
q0 4
v_sub_f32 v45,v45,v74
v_sub_f32 v46,v46,v74
q7 188,184,104
v_sub_f32 v47,v47,v74
v_sub_f32 v48,v48,v74
q7 188,188,108
q5 112,16,36864
v_sub_f32 v49,v49,v74
q7 184,176,104
q5 116,17,36864
v_sub_f32 v50,v50,v74
q7 184,180,108
q4 240,20,49664
v_sub_f32 v51,v51,v74
q7 180,168,104
q4 242,21,49920
v_sub_f32 v52,v52,v74
q7 180,172,108
q4 244,20,53760
v_sub_f32 v53,v53,v74
q7 176,160,104
q4 246,21,54016
v_sub_f32 v54,v54,v74
q7 176,164,108
q1 12,0
v_sub_f32 v55,v55,v74
q7 172,152,104
v_mul_f32 v75,0x3db8aa3b,v75
qa 56,40,75
q7 172,156,108
qa 57,41,75
qa 58,42,75
q7 168,144,104
qa 59,43,75
qa 60,44,75
q7 168,148,108
q1 13,1024
qa 61,45,75
q7 164,136,104
qa 62,46,75
qa 63,47,75
q7 164,140,108
qa 64,48,75
qa 65,49,75
q7 160,128,104
qa 66,50,75
qa 67,51,75
q7 160,132,108
q0 4
qa 68,52,75
qa 69,53,75
q7 192,128,112
qa 70,54,75
qa 71,55,75
q7 192,132,116
q5 120,16,38912
qb 32,56,57
q7 196,136,112
q5 124,17,38912
qb 33,58,59
q7 196,140,116
q4 248,21,49664
qb 34,60,61
q7 200,144,112
q4 250,20,49920
qb 35,62,63
q7 200,148,116
q4 252,21,53760
qb 36,64,65
q7 204,152,112
q4 254,20,54016
qb 37,66,67
q7 204,156,116
q1 14,2048
qb 38,68,69
q7 208,160,112
qb 39,70,71
q7 208,164,116
q1 15,3072
q7 212,168,112
s_mov_b32 s76,m0
s_add_u32 m0,s82,4096
q7 212,172,116
q8 95,3072
s_mov_b32 m0,s76
q7 216,176,112
q3 20
q4 40,84,0
q7 216,180,116
q4 42,84,256
q7 220,184,112
q4 44,84,32
q7 220,188,116
q0 7
q4 46,84,288
q7 252,184,120
s_waitcnt vmcnt(21) lgkmcnt(0)
s_barrier
q7 252,188,124
q5 96,18,0
q7 248,176,120
q5 100,19,0
q7 248,180,124
q4 48,84,512
q7 244,168,120
s_add_u32 m0,s88,32768
s_xor_b32 s86,s12,131584
s_add_u32 s86,s86,s64
q7 244,172,124
q4 50,84,768
q7 240,160,120
q4 52,84,544
q7 240,164,124
q2 8,0
q7 236,152,120
q4 54,84,800
q7 236,156,124
q7 232,144,120
q7 232,148,124
q7 228,136,120
q7 228,140,124
q7 224,128,120
q2 9,1024
q7 224,132,124
q0 4
q7 0,192,96
)"
R"(buffer_store_dwordx4 v[40:43],v2,s[32:35],s91 offen sc1
s_add_u32 s91,s91,2048
q7 0,196,100
q5 104,18,2048
q7 4,200,96
q5 108,19,2048
q7 4,204,100
q4 128,22,16384
q7 8,208,96
q4 130,23,16640
q7 8,212,100
q4 132,22,20480
q7 12,216,96
q4 134,23,20736
q7 12,220,100
q4 136,23,16384
q7 16,224,96
q4 138,22,16640
q7 16,228,100
q2 10,2048
q7 20,232,96
q7 20,236,100
q7 24,240,96
s_xor_b32 s87,s13,2621568
q2 11,3072
s_add_u32 s87,s87,s65
q7 24,244,100
s_add_u32 m0,s89,32768
q7 28,248,96
q7 28,252,100
q0 6
q7 60,248,104
q7 60,252,108
q5 112,18,4096
q7 56,240,104
q5 116,19,4096
q7 56,244,108
q4 140,23,20480
q7 52,232,104
q4 142,22,20736
q7 52,236,108
q4 144,22,16896
q7 48,224,104
s_lshr_b32 s48,s30,3
q4 146,23,17152
q7 48,228,108
s_lshl2_add_u32 s48,s48,s31
q4 148,22,20992
q7 44,216,104
q4 150,23,21248
q7 44,220,108
q1 12,0
q7 40,208,104
s_cmp_ge_u32 s48,49
s_cselect_b32 s95,1,0
s_cselect_b32 s54,s20,s16
s_cselect_b32 s55,s21,s17
s_mov_b32 s94,3584
s_cselect_b32 s94,896,s94
q7 40,212,108
s_mov_b32 s44,s54
q1 13,1024
q7 36,200,104
s_and_b32 s45,s55,0xffff
buffer_store_dwordx4 v[44:47],v2,s[32:35],s91 offen sc1
s_add_u32 s91,s91,2048
q7 36,204,108
s_mov_b32 s46,1605632
q7 32,192,104
s_mov_b32 s47,0x20000
q7 32,196,108
q0 6
s_mul_i32 s49,s71,49
q7 64,192,112
s_add_u32 s49,s49,s48
q7 64,196,116
s_mul_i32 s50,s49,2341
q5 120,18,6144
q7 68,200,112
s_lshr_b32 s50,s50,15
q5 124,19,6144
q7 68,204,116
s_mul_i32 s51,s50,14
q4 152,23,16896
q7 72,208,112
s_sub_u32 s51,s49,s51
q4 154,22,17152
q7 72,212,116
s_mul_i32 s56,s51,114688
q4 156,23,20992
q7 76,216,112
s_lshl_b32 s52,s50,7
q4 158,22,21248
q7 76,220,116
s_add_u32 s56,s56,s52
q1 14,2048
q7 80,224,112
s_lshr_b32 s52,s51,1
q7 80,228,116
s_lshl_b32 s52,s52,15
q1 15,3072
q7 84,232,112
s_add_u32 s52,s52,10100736
q7 84,236,116
s_and_b32 s51,s51,1
q7 88,240,112
s_lshl2_add_u32 s53,s50,0
q7 88,244,116
s_mul_i32 s54,s53,2341
q7 92,248,112
s_lshr_b32 s54,s54,15
q7 92,252,116
q0 4
s_mul_i32 s55,s54,14
q7 124,248,120
s_waitcnt vmcnt(21) lgkmcnt(0)
s_barrier
s_sub_u32 s53,s53,s55
q7 124,252,124
s_lshl1_add_u32 s53,s53,s51
q5 96,18,32768
q7 120,240,120
s_mul_i32 s55,s53,37
q5 100,19,32768
q7 120,244,124
s_lshr_b32 s55,s55,8
q4 160,22,49152
q7 116,232,120
s_xor_b32 s86,s12,640
q4 162,23,49408
s_add_u32 s86,s86,s64
q7 116,236,124
s_add_u32 m0,s88,65536
q4 164,22,53248
s_add_u32 s53,s53,s55
q7 112,224,120
s_lshl_b32 s53,s53,10
q4 166,23,53504
q7 112,228,124
s_mul_i32 s54,s54,229376
q2 8,0
q7 108,216,120
s_add_u32 s53,s53,s54
buffer_store_dwordx4 v[48:51],v2,s[32:35],s91 offen sc1
s_add_u32 s91,s91,2048
q7 108,220,124
s_add_u32 s93,s53,s52
q7 104,208,120
s_lshl2_add_u32 s53,s50,1
q7 104,212,124
s_mul_i32 s54,s53,2341
q7 100,200,120
LHH0_2kf:
s_cbranch_execz LHRPkf
s_lshr_b32 s54,s54,15
q7 100,204,124
s_mul_i32 s55,s54,14
q7 96,192,120
s_sub_u32 s53,s53,s55
q2 9,1024
q7 96,196,124
q0 4
s_lshl1_add_u32 s53,s53,s51
q7 128,192,96
s_mul_i32 s55,s53,37
q7 128,196,100
s_lshr_b32 s55,s55,8
q5 104,18,34816
q7 132,200,96
s_add_u32 s53,s53,s55
q5 108,19,34816
q7 132,204,100
s_lshl_b32 s53,s53,10
q4 168,23,49152
q7 136,208,96
s_mul_i32 s54,s54,229376
q4 170,22,49408
q7 136,212,100
s_add_u32 s53,s53,s54
q4 172,23,53248
q7 140,216,96
s_add_u32 s78,s53,s52
q4 174,22,53504
q7 140,220,100
s_lshl2_add_u32 s53,s50,2
q2 10,2048
q7 144,224,96
s_mul_i32 s54,s53,2341
q7 144,228,100
s_lshr_b32 s54,s54,15
q7 148,232,96
s_mul_i32 s55,s54,14
q7 148,236,100
s_sub_u32 s53,s53,s55
q7 152,240,96
s_xor_b32 s87,s13,3145728
q2 11,3072
s_add_u32 s87,s87,s65
q7 152,244,100
s_lshl1_add_u32 s53,s53,s51
s_add_u32 m0,s89,65536
q7 156,248,96
s_mul_i32 s55,s53,37
q7 156,252,100
q0 4
s_lshr_b32 s55,s55,8
q7 188,248,104
s_add_u32 s53,s53,s55
q7 188,252,108
s_lshl_b32 s53,s53,10
q5 112,18,36864
q7 184,240,104
s_mul_i32 s54,s54,229376
q5 116,19,36864
q7 184,244,108
s_add_u32 s53,s53,s54
q4 176,22,49664
q7 180,232,104
s_add_u32 s79,s53,s52
q4 178,23,49920
q7 180,236,108
s_lshl2_add_u32 s53,s50,3
q4 180,22,53760
q7 176,224,104
s_mul_i32 s54,s53,2341
q4 182,23,54016
q7 176,228,108
s_lshr_b32 s54,s54,15
q1 12,0
q7 172,216,104
s_mul_i32 s55,s54,14
buffer_store_dwordx4 v[52:55],v2,s[32:35],s91 offen sc1
q7 172,220,108
s_sub_u32 s53,s53,s55
q7 168,208,104
s_lshl1_add_u32 s53,s53,s51
q7 168,212,108
s_mul_i32 s55,s53,37
q1 13,1024
q7 164,200,104
s_lshr_b32 s55,s55,8
q7 164,204,108
s_add_u32 s53,s53,s55
q7 160,192,104
s_lshl_b32 s53,s53,10
q7 160,196,108
q0 4
s_mul_i32 s54,s54,229376
q7 192,192,112
s_add_u32 s53,s53,s54
q7 192,196,116
s_add_u32 s80,s53,s52
q5 120,18,38912
q7 196,200,112
s_sub_u32 s49,s48,49
q5 124,19,38912
q7 196,204,116
s_mul_i32 s50,s49,37
q4 184,23,49664
q7 200,208,112
s_lshr_b32 s50,s50,8
q4 186,22,49920
q7 200,212,116
s_mul_i32 s51,s50,7
q4 188,23,53760
q7 204,216,112
s_sub_u32 s51,s49,s51
q4 190,22,54016
q7 204,220,116
s_mul_i32 s52,s71,7
q1 14,2048
q7 208,224,112
s_add_u32 s52,s52,s51
q7 208,228,116
s_mul_i32 s52,s52,28672
q1 15,3072
q7 212,232,112
s_lshl_b32 s53,s50,7
q7 212,236,116
s_add_u32 s52,s52,s53
q7 216,240,112
s_mul_i32 s53,s71,229376
q7 216,244,116
s_lshl_b32 s50,s50,15
q7 220,248,112
s_add_u32 s53,s53,s50
q7 220,252,116
q0 4
s_lshl_b32 s51,s51,10
q7 252,248,120
s_waitcnt vmcnt(21) lgkmcnt(0)
s_barrier
s_add_u32 s53,s53,s51
q7 252,252,124
s_add_u32 s53,s53,11935744
q5 96,16,0
q7 248,240,120
q5 100,17,0
q7 248,244,124
s_cmp_ge_u32 s48,98
s_cselect_b32 s52,1073741824,s52
s_cselect_b32 s53,1073741824,s53
q7 244,232,120
s_add_u32 m0,s88,98304
s_xor_b32 s86,s12,131712
s_add_u32 s86,s86,s64
q7 244,236,124
s_add_u32 s54,s53,8192
q7 240,224,120
s_add_u32 s55,s53,16384
q7 240,228,124
s_add_u32 s51,s53,24576
q2 8,0
q7 236,216,120
s_cmp_eq_u32 s95,0
s_cselect_b32 s98,s56,s52
s_cselect_b32 s93,s93,s53
s_cselect_b32 s78,s78,s54
q7 236,220,124
s_cmp_eq_u32 s95,0
s_cselect_b32 s79,s79,s55
s_cselect_b32 s80,s80,s51
q7 232,208,120
s_lshl_b32 s48,s94,3
s_sub_u32 s48,s48,1024
v_mad_u32_u24 v92,v86,s94,v87
q7 232,212,124
v_add_u32 v93,s48,v92
v_add_u32 v94,s48,v93
q7 228,200,120
s_mov_b32 s76,m0
s_add_u32 m0,s82,0
v_add_u32 v95,s48,v94
q7 228,204,124
q8 92,0
s_mov_b32 m0,s76
q7 224,192,120
q2 9,1024
q7 224,196,124
q0 0
q7 0,128,96
q7 0,132,100
q5 104,16,2048
q7 4,136,96
q5 108,17,2048
q7 4,140,100
q4 192,20,16384
q7 8,144,96
q4 194,21,16640
q7 8,148,100
q4 196,20,20480
q7 12,152,96
q4 198,21,20736
q7 12,156,100
q4 200,21,16384
q7 16,160,96
q4 202,20,16640
q7 16,164,100
q2 10,2048
q7 20,168,96
q7 20,172,100
q7 24,176,96
s_xor_b32 s87,s13,3145856
q2 11,3072
s_add_u32 s87,s87,s65
q7 24,180,100
s_add_u32 m0,s89,98304
q7 28,184,96
q7 28,188,100
q0 6
q7 60,184,104
q7 60,188,108
q5 112,16,4096
q7 56,176,104
q5 116,17,4096
q7 56,180,108
q4 204,21,20480
q7 52,168,104
q4 206,20,20736
q7 52,172,108
q4 208,20,16896
q7 48,160,104
q4 210,21,17152
q7 48,164,108
q4 212,20,20992
q7 44,152,104
q4 214,21,21248
q7 44,156,108
q1 12,0
q7 40,144,104
q7 40,148,108
q1 13,1024
q7 36,136,104
s_mov_b32 s76,m0
s_add_u32 m0,s82,0
q7 36,140,108
q8 93,1024
s_mov_b32 m0,s76
q7 32,128,104
q7 32,132,108
q0 6
q7 64,128,112
q7 64,132,116
q5 120,16,6144
q7 68,136,112
q5 124,17,6144
q7 68,140,116
q4 216,21,16896
q7 72,144,112
q4 218,20,17152
q7 72,148,116
q4 220,21,20992
q7 76,152,112
q4 222,20,21248
q7 76,156,116
q1 14,2048
q7 80,160,112
q7 80,164,116
q1 15,3072
q7 84,168,112
q7 84,172,116
q7 88,176,112
q7 88,180,116
q7 92,184,112
q7 92,188,116
q0 4
q7 124,184,120
s_waitcnt vmcnt(20) lgkmcnt(0)
s_barrier
q7 124,188,124
q5 96,16,32768
q7 120,176,120
q5 100,17,32768
q7 120,180,124
q4 224,20,49152
q7 116,168,120
s_xor_b32 s86,s12,768
q4 226,21,49408
s_add_u32 s86,s86,s64
q7 116,172,124
s_add_u32 m0,s88,0
q4 228,20,53248
q7 112,160,120
q4 230,21,53504
q7 112,164,124
q2 8,0
q7 108,152,120
q7 108,156,124
q7 104,144,120
s_mov_b32 s76,m0
s_add_u32 m0,s82,0
q7 104,148,124
q8 94,2048
s_mov_b32 m0,s76
q7 100,136,120
q7 100,140,124
q7 96,128,120
q2 9,1024
q7 96,132,124
q0 4
q7 128,128,96
q7 128,132,100
q5 104,16,34816
q7 132,136,96
q5 108,17,34816
q7 132,140,100
q4 232,21,49152
q7 136,144,96
q4 234,20,49408
q7 136,148,100
q4 236,21,53248
q7 140,152,96
q4 238,20,53504
q7 140,156,100
q2 10,2048
q7 144,160,96
q7 144,164,100
q7 148,168,96
q7 148,172,100
q7 152,176,96
s_xor_b32 s87,s13,3670016
q2 11,3072
s_add_u32 s87,s87,s65
q7 152,180,100
s_add_u32 m0,s89,0
q7 156,184,96
q7 156,188,100
q0 4
q7 188,184,104
q7 188,188,108
q5 112,16,36864
q7 184,176,104
q5 116,17,36864
q7 184,180,108
q4 240,20,49664
q7 180,168,104
q4 242,21,49920
q7 180,172,108
q4 244,20,53760
q7 176,160,104
q4 246,21,54016
q7 176,164,108
q1 12,0
q7 172,152,104
s_mov_b32 s76,m0
s_add_u32 m0,s82,0
q7 172,156,108
q8 95,3072
s_mov_b32 m0,s76
q7 168,144,104
q3 37
q4 56,85,4096
q7 168,148,108
q1 13,1024
q7 164,136,104
q4 58,85,6144
q7 164,140,108
q4 60,85,4104
q7 160,128,104
q4 62,85,6152
q7 160,132,108
q0 8
q4 64,85,4608
q7 192,128,112
q4 66,85,6656
q7 192,132,116
q5 120,16,38912
q7 196,136,112
q5 124,17,38912
q7 196,140,116
q4 248,21,49664
q7 200,144,112
q4 250,20,49920
q7 200,148,116
q4 252,21,53760
q7 204,152,112
q4 254,20,54016
q7 204,156,116
q1 14,2048
q7 208,160,112
q4 68,85,4616
q7 208,164,116
q1 15,3072
q7 212,168,112
q4 70,85,6664
q7 212,172,116
q7 216,176,112
q7 216,180,116
q7 220,184,112
q7 220,188,116
q0 6
q7 252,184,120
s_waitcnt vmcnt(20) lgkmcnt(0)
s_barrier
q7 252,188,124
q5 96,18,0
q7 248,176,120
q5 100,19,0
q7 248,180,124
buffer_store_dwordx4 v[56:59],v2,s[32:35],s92 offen sc1
s_add_u32 s92,s92,8192
q7 244,168,120
s_add_u32 m0,s88,32768
s_xor_b32 s86,s12,131840
s_add_u32 s86,s86,s64
q7 244,172,124
q7 240,160,120
q7 240,164,124
q2 8,0
q7 236,152,120
q7 236,156,124
q7 232,144,120
q7 232,148,124
q7 228,136,120
q7 228,140,124
q7 224,128,120
q2 9,1024
q7 224,132,124
q0 0
q7 0,192,96
q7 0,196,100
q5 104,18,2048
q7 4,200,96
q5 108,19,2048
q7 4,204,100
q4 128,22,16384
q7 8,208,96
q4 130,23,16640
q7 8,212,100
q4 132,22,20480
q7 12,216,96
q4 134,23,20736
q7 12,220,100
q4 136,23,16384
q7 16,224,96
q4 138,22,16640
q7 16,228,100
q2 10,2048
q7 20,232,96
q7 20,236,100
q7 24,240,96
s_xor_b32 s87,s13,3670144
q2 11,3072
s_add_u32 s87,s87,s65
q7 24,244,100
s_add_u32 m0,s89,32768
q7 28,248,96
q7 28,252,100
q0 6
buffer_store_dwordx4 v[60:63],v2,s[32:35],s92 offen sc1
s_add_u32 s92,s92,8192
q7 60,248,104
q7 60,252,108
q5 112,18,4096
q7 56,240,104
q5 116,19,4096
q7 56,244,108
q4 140,23,20480
q7 52,232,104
q4 142,22,20736
q7 52,236,108
q4 144,22,16896
q7 48,224,104
q4 146,23,17152
q7 48,228,108
q4 148,22,20992
q7 44,216,104
q4 150,23,21248
q7 44,220,108
q1 12,0
q7 40,208,104
q7 40,212,108
q1 13,1024
q7 36,200,104
q7 36,204,108
q7 32,192,104
q7 32,196,108
q0 6
q7 64,192,112
q7 64,196,116
q5 120,18,6144
q7 68,200,112
q5 124,19,6144
q7 68,204,116
q4 152,23,16896
q7 72,208,112
q4 154,22,17152
q7 72,212,116
q4 156,23,20992
q7 76,216,112
q4 158,22,21248
q7 76,220,116
q1 14,2048
q7 80,224,112
q7 80,228,116
q1 15,3072
q7 84,232,112
q7 84,236,116
buffer_store_dwordx4 v[64:67],v2,s[32:35],s92 offen sc1
s_add_u32 s92,s92,8192
q7 88,240,112
q7 88,244,116
q7 92,248,112
q7 92,252,116
q0 4
q7 124,248,120
s_waitcnt vmcnt(21) lgkmcnt(0)
s_barrier
q7 124,252,124
q5 96,18,32768
q7 120,240,120
q5 100,19,32768
q7 120,244,124
q4 160,22,49152
q7 116,232,120
s_xor_b32 s86,s12,896
q4 162,23,49408
s_add_u32 s86,s86,s64
q7 116,236,124
s_add_u32 m0,s88,65536
q4 164,22,53248
q7 112,224,120
q4 166,23,53504
q7 112,228,124
q2 8,0
q7 108,216,120
ds_write_b128 v30,v[32:35]
q7 108,220,124
ds_write_b128 v31,v[36:39]
q7 104,208,120
q7 104,212,124
q7 100,200,120
q7 100,204,124
q7 96,192,120
q2 9,1024
q7 96,196,124
q0 6
q7 128,192,96
q7 128,196,100
q5 104,18,34816
q7 132,200,96
q5 108,19,34816
q7 132,204,100
q4 168,23,49152
q7 136,208,96
q4 170,22,49408
q7 136,212,100
q4 172,23,53248
q7 140,216,96
q4 174,22,53504
q7 140,220,100
q2 10,2048
q7 144,224,96
buffer_store_dwordx4 v[68:71],v2,s[32:35],s92 offen sc1
q7 144,228,100
q7 148,232,96
q7 148,236,100
q7 152,240,96
LHH0_3kf:
s_cbranch_execz LHRPkf
s_xor_b32 s87,s13,4194304
q2 11,3072
s_add_u32 s87,s87,s65
q7 152,244,100
s_add_u32 m0,s89,65536
q7 156,248,96
q7 156,252,100
q0 4
q7 188,248,104
q7 188,252,108
q5 112,18,36864
q7 184,240,104
q5 116,19,36864
q7 184,244,108
q4 176,22,49664
q7 180,232,104
q4 178,23,49920
q7 180,236,108
q4 180,22,53760
q7 176,224,104
q4 182,23,54016
q7 176,228,108
q1 12,0
q7 172,216,104
q7 172,220,108
q7 168,208,104
q7 168,212,108
q1 13,1024
q7 164,200,104
q7 164,204,108
q7 160,192,104
q7 160,196,108
q0 4
q7 192,192,112
q7 192,196,116
q5 120,18,38912
q7 196,200,112
q5 124,19,38912
q7 196,204,116
q4 184,23,49664
q7 200,208,112
q4 186,22,49920
q7 200,212,116
q4 188,23,53760
q7 204,216,112
q4 190,22,54016
q7 204,220,116
q1 14,2048
q7 208,224,112
s_cmp_eq_u32 s95,0
s_cselect_b64 vcc,-1,0
s_mov_b32 s49,2048
s_cselect_b32 s49,256,s49
s_cselect_b32 s50,32,8
q7 208,228,116
q1 15,3072
v_cndmask_b32 v88,v85,v84,vcc
q7 212,232,112
v_add_u32 v89,s49,v88
q7 212,236,116
v_add_u32 v90,s50,v88
q7 216,240,112
v_add_u32 v91,s50,v89
q7 216,244,116
q3 23
q4 56,88,0
q7 220,248,112
q4 58,89,0
q7 220,252,116
q0 6
q4 60,90,0
q7 252,248,120
s_waitcnt vmcnt(20) lgkmcnt(0)
s_barrier
q7 252,252,124
q5 96,16,0
q7 248,240,120
q5 100,17,0
q7 248,244,124
q4 62,91,0
q7 244,232,120
s_add_u32 m0,s88,98304
s_xor_b32 s86,s12,131968
s_add_u32 s86,s86,s64
q7 244,236,124
q4 64,88,512
q7 240,224,120
q4 66,89,512
q7 240,228,124
q2 8,0
q7 236,216,120
q4 68,90,512
q7 236,220,124
q4 70,91,512
q7 232,208,120
q7 232,212,124
q7 228,200,120
q7 228,204,124
q7 224,192,120
q2 9,1024
q7 224,196,124
q0 5
q7 0,128,96
q7 0,132,100
q5 104,16,2048
q7 4,136,96
q5 108,17,2048
q7 4,140,100
q4 192,20,16384
q7 8,144,96
q4 194,21,16640
q7 8,148,100
q4 196,20,20480
q7 12,152,96
q4 198,21,20736
q7 12,156,100
q4 200,21,16384
q7 16,160,96
q4 202,20,16640
q7 16,164,100
q2 10,2048
q7 20,168,96
buffer_store_dwordx4 v[56:59],v2,s[32:35],s93 offen sc1
q7 20,172,100
q7 24,176,96
s_xor_b32 s87,s13,4194432
q2 11,3072
s_add_u32 s87,s87,s65
q7 24,180,100
s_add_u32 m0,s89,98304
q7 28,184,96
q7 28,188,100
q0 6
q7 60,184,104
q7 60,188,108
q5 112,16,4096
q7 56,176,104
q5 116,17,4096
q7 56,180,108
q4 204,21,20480
q7 52,168,104
q4 206,20,20736
q7 52,172,108
q4 208,20,16896
q7 48,160,104
q4 210,21,17152
q7 48,164,108
q4 212,20,20992
q7 44,152,104
q4 214,21,21248
q7 44,156,108
q1 12,0
q7 40,144,104
q7 40,148,108
q1 13,1024
q7 36,136,104
q7 36,140,108
q7 32,128,104
q7 32,132,108
q0 6
q7 64,128,112
q7 64,132,116
q5 120,16,6144
q7 68,136,112
q5 124,17,6144
q7 68,140,116
q4 216,21,16896
q7 72,144,112
q4 218,20,17152
q7 72,148,116
q4 220,21,20992
q7 76,152,112
q4 222,20,21248
q7 76,156,116
q1 14,2048
q7 80,160,112
buffer_store_dwordx4 v[60:63],v2,s[32:35],s78 offen sc1
q7 80,164,116
q1 15,3072
q7 84,168,112
q7 84,172,116
q7 88,176,112
q7 88,180,116
q7 92,184,112
q7 92,188,116
q0 4
q7 124,184,120
s_waitcnt vmcnt(20) lgkmcnt(0)
s_barrier
q7 124,188,124
q5 96,16,32768
q7 120,176,120
q5 100,17,32768
q7 120,180,124
q4 224,20,49152
q7 116,168,120
s_xor_b32 s86,s12,1024
q4 226,21,49408
s_add_u32 s86,s86,s64
q7 116,172,124
s_add_u32 m0,s88,0
q4 228,20,53248
q7 112,160,120
q4 230,21,53504
q7 112,164,124
q2 8,0
q7 108,152,120
q7 108,156,124
q7 104,144,120
q7 104,148,124
q7 100,136,120
q7 100,140,124
q7 96,128,120
q2 9,1024
q7 96,132,124
q0 4
q7 128,128,96
q7 128,132,100
q5 104,16,34816
q7 132,136,96
q5 108,17,34816
q7 132,140,100
q4 232,21,49152
q7 136,144,96
q4 234,20,49408
q7 136,148,100
q4 236,21,53248
q7 140,152,96
q4 238,20,53504
q7 140,156,100
q2 10,2048
q7 144,160,96
buffer_store_dwordx4 v[64:67],v2,s[32:35],s79 offen sc1
q7 144,164,100
q7 148,168,96
q7 148,172,100
q7 152,176,96
s_xor_b32 s87,s13,4718592
q2 11,3072
s_add_u32 s87,s87,s65
q7 152,180,100
s_add_u32 m0,s89,0
q7 156,184,96
q7 156,188,100
q0 4
q7 188,184,104
q7 188,188,108
q5 112,16,36864
q7 184,176,104
q5 116,17,36864
q7 184,180,108
q4 240,20,49664
q7 180,168,104
q4 242,21,49920
q7 180,172,108
q4 244,20,53760
q7 176,160,104
q4 246,21,54016
q7 176,164,108
q1 12,0
q7 172,152,104
q7 172,156,108
q7 168,144,104
q7 168,148,108
q1 13,1024
q7 164,136,104
q7 164,140,108
q7 160,128,104
q7 160,132,108
q0 4
q7 192,128,112
q7 192,132,116
q5 120,16,38912
q7 196,136,112
q5 124,17,38912
q7 196,140,116
q4 248,21,49664
q7 200,144,112
q4 250,20,49920
q7 200,148,116
q4 252,21,53760
q7 204,152,112
q4 254,20,54016
q7 204,156,116
q1 14,2048
q7 208,160,112
buffer_store_dwordx4 v[68:71],v2,s[32:35],s80 offen sc1
q7 208,164,116
q1 15,3072
q7 212,168,112
q7 212,172,116
q7 216,176,112
q7 216,180,116
q7 220,184,112
q7 220,188,116
q0 4
q7 252,184,120
s_waitcnt vmcnt(20) lgkmcnt(0)
s_barrier
q7 252,188,124
q5 96,18,0
q7 248,176,120
q5 100,19,0
q7 248,180,124
q7 244,168,120
s_add_u32 m0,s88,32768
s_xor_b32 s86,s12,132096
s_add_u32 s86,s86,s64
q7 244,172,124
q7 240,160,120
q7 240,164,124
q2 8,0
q7 236,152,120
q7 236,156,124
q7 232,144,120
q7 232,148,124
q7 228,136,120
q7 228,140,124
q7 224,128,120
q2 9,1024
q7 224,132,124
q0 0
q7 0,192,96
q7 0,196,100
q5 104,18,2048
q7 4,200,96
q5 108,19,2048
q7 4,204,100
q4 128,22,16384
q7 8,208,96
q4 130,23,16640
q7 8,212,100
q4 132,22,20480
q7 12,216,96
q4 134,23,20736
q7 12,220,100
q4 136,23,16384
q7 16,224,96
q4 138,22,16640
q7 16,228,100
q2 10,2048
q7 20,232,96
s_lshr_b32 s52,s31,1
s_lshl_b32 s52,s52,13
q7 20,236,100
s_and_b32 s53,s31,1
s_lshl_b32 s53,s53,11
q7 24,240,96
s_xor_b32 s87,s13,4718720
q2 11,3072
s_add_u32 s87,s87,s65
q7 24,244,100
s_add_u32 s52,s52,s53
s_add_u32 s52,s52,135168
s_add_u32 m0,s89,32768
q7 28,248,96
v_add_u32 v72,s52,v2
q7 28,252,100
q0 6
ds_read_b128 v[40:43],v72
q7 60,248,104
q5 44,72,1024
q7 60,252,108
q5 112,18,4096
q7 56,240,104
q5 116,19,4096
q7 56,244,108
q4 140,23,20480
q7 52,232,104
q4 142,22,20736
q7 52,236,108
q4 144,22,16896
q7 48,224,104
q4 146,23,17152
q7 48,228,108
q4 148,22,20992
q7 44,216,104
q4 150,23,21248
q7 44,220,108
q1 12,0
q7 40,208,104
s_mov_b32 s51,925696
s_cmp_eq_u32 s31,3
s_cselect_b32 s52,1073741824,s51
q7 40,212,108
q1 13,1024
q7 36,200,104
q7 36,204,108
q7 32,192,104
q7 32,196,108
q0 6
q7 64,192,112
q7 64,196,116
q5 120,18,6144
q7 68,200,112
q5 124,19,6144
q7 68,204,116
q4 152,23,16896
q7 72,208,112
q4 154,22,17152
q7 72,212,116
q4 156,23,20992
q7 76,216,112
q4 158,22,21248
q7 76,220,116
q1 14,2048
q7 80,224,112
buffer_store_dwordx4 v[40:43],v77,s[32:35],s51 offen sc1
q7 80,228,116
q1 15,3072
q7 84,232,112
q7 84,236,116
q7 88,240,112
q7 88,244,116
q7 92,248,112
q7 92,252,116
q0 4
q7 124,248,120
s_waitcnt vmcnt(19) lgkmcnt(0)
s_barrier
q7 124,252,124
q5 96,18,32768
q7 120,240,120
q5 100,19,32768
q7 120,244,124
q4 160,22,49152
q7 116,232,120
s_xor_b32 s86,s12,1152
q4 162,23,49408
s_add_u32 s86,s86,s64
q7 116,236,124
s_add_u32 m0,s88,65536
q4 164,22,53248
q7 112,224,120
q4 166,23,53504
q7 112,228,124
q2 8,0
q7 108,216,120
q7 108,220,124
q7 104,208,120
q7 104,212,124
q7 100,200,120
q7 100,204,124
q7 96,192,120
q2 9,1024
q7 96,196,124
q0 4
q7 128,192,96
q7 128,196,100
q5 104,18,34816
q7 132,200,96
q5 108,19,34816
q7 132,204,100
q4 168,23,49152
q7 136,208,96
q4 170,22,49408
q7 136,212,100
q4 172,23,53248
q7 140,216,96
q4 174,22,53504
q7 140,220,100
q2 10,2048
q7 144,224,96
buffer_store_dwordx4 v[44:47],v77,s[32:35],s52 offen offset:2048 sc1
q7 144,228,100
q7 148,232,96
q7 148,236,100
q7 152,240,96
s_xor_b32 s87,s13,5242880
q2 11,3072
s_add_u32 s87,s87,s65
q7 152,244,100
s_add_u32 m0,s89,65536
q7 156,248,96
q7 156,252,100
q0 4
q7 188,248,104
q7 188,252,108
q5 112,18,36864
q7 184,240,104
q5 116,19,36864
q7 184,244,108
q4 176,22,49664
q7 180,232,104
q4 178,23,49920
q7 180,236,108
q4 180,22,53760
q7 176,224,104
q4 182,23,54016
q7 176,228,108
q1 12,0
q7 172,216,104
q7 172,220,108
q7 168,208,104
q7 168,212,108
q1 13,1024
q7 164,200,104
q7 164,204,108
q7 160,192,104
q7 160,196,108
q0 4
q7 192,192,112
q7 192,196,116
q5 120,18,38912
q7 196,200,112
q5 124,19,38912
q7 196,204,116
q4 184,23,49664
q7 200,208,112
q4 186,22,49920
q7 200,212,116
q4 188,23,53760
q7 204,216,112
q4 190,22,54016
q7 204,220,116
q1 14,2048
q7 208,224,112
q7 208,228,116
q1 15,3072
q7 212,232,112
q7 212,236,116
q7 216,240,112
q7 216,244,116
q7 220,248,112
q7 220,252,116
q0 4
q7 252,248,120
s_waitcnt vmcnt(18) lgkmcnt(0)
s_barrier
q7 252,252,124
q5 96,16,0
q7 248,240,120
q5 100,17,0
q7 248,244,124
q7 244,232,120
s_add_u32 m0,s88,98304
s_xor_b32 s86,s12,132224
s_add_u32 s86,s86,s64
q7 244,236,124
q7 240,224,120
LHH1_0kf:
s_cbranch_execz LHR1kf
q7 240,228,124
q2 8,0
q7 236,216,120
q7 236,220,124
q7 232,208,120
q7 232,212,124
q7 228,200,120
q7 228,204,124
q7 224,192,120
q2 9,1024
q7 224,196,124
q0 0
q7 0,128,96
q7 0,132,100
q5 104,16,2048
q7 4,136,96
q5 108,17,2048
q7 4,140,100
q4 192,20,16384
q7 8,144,96
q4 194,21,16640
q7 8,148,100
q4 196,20,20480
q7 12,152,96
q4 198,21,20736
q7 12,156,100
q4 200,21,16384
q7 16,160,96
q4 202,20,16640
q7 16,164,100
q2 10,2048
q7 20,168,96
q7 20,172,100
q7 24,176,96
s_xor_b32 s87,s13,5243008
q2 11,3072
s_add_u32 s87,s87,s65
q7 24,180,100
s_add_u32 m0,s89,98304
q7 28,184,96
q7 28,188,100
q0 6
q7 60,184,104
q7 60,188,108
q5 112,16,4096
q7 56,176,104
q5 116,17,4096
q7 56,180,108
q4 204,21,20480
q7 52,168,104
q4 206,20,20736
q7 52,172,108
q4 208,20,16896
q7 48,160,104
q4 210,21,17152
q7 48,164,108
q4 212,20,20992
q7 44,152,104
q4 214,21,21248
q7 44,156,108
q1 12,0
q7 40,144,104
q7 40,148,108
q1 13,1024
q7 36,136,104
q7 36,140,108
q7 32,128,104
q7 32,132,108
q0 6
q7 64,128,112
q7 64,132,116
q5 120,16,6144
q7 68,136,112
q5 124,17,6144
q7 68,140,116
q4 216,21,16896
q7 72,144,112
q4 218,20,17152
q7 72,148,116
q4 220,21,20992
q7 76,152,112
q4 222,20,21248
q7 76,156,116
q1 14,2048
q7 80,160,112
q7 80,164,116
q1 15,3072
q7 84,168,112
q7 84,172,116
q7 88,176,112
q7 88,180,116
q7 92,184,112
q7 92,188,116
q0 4
q7 124,184,120
s_waitcnt vmcnt(17) lgkmcnt(0)
s_barrier
q7 124,188,124
q5 96,16,32768
q7 120,176,120
q5 100,17,32768
q7 120,180,124
q4 224,20,49152
q7 116,168,120
s_xor_b32 s86,s12,1280
q4 226,21,49408
s_add_u32 s86,s86,s64
q7 116,172,124
s_add_u32 m0,s88,0
q4 228,20,53248
q7 112,160,120
q4 230,21,53504
q7 112,164,124
q2 8,0
q7 108,152,120
q7 108,156,124
q7 104,144,120
q7 104,148,124
q7 100,136,120
q7 100,140,124
q7 96,128,120
q2 9,1024
q7 96,132,124
q0 4
q7 128,128,96
q7 128,132,100
q5 104,16,34816
q7 132,136,96
q5 108,17,34816
q7 132,140,100
q4 232,21,49152
q7 136,144,96
q4 234,20,49408
q7 136,148,100
q4 236,21,53248
q7 140,152,96
q4 238,20,53504
q7 140,156,100
q2 10,2048
q7 144,160,96
q7 144,164,100
q7 148,168,96
q7 148,172,100
q7 152,176,96
s_xor_b32 s87,s13,5767168
q2 11,3072
s_add_u32 s87,s87,s65
q7 152,180,100
s_add_u32 m0,s89,0
q7 156,184,96
q7 156,188,100
q0 4
q7 188,184,104
q7 188,188,108
q5 112,16,36864
q7 184,176,104
q5 116,17,36864
q7 184,180,108
q4 240,20,49664
q7 180,168,104
q4 242,21,49920
q7 180,172,108
q4 244,20,53760
q7 176,160,104
q4 246,21,54016
q7 176,164,108
q1 12,0
q7 172,152,104
q7 172,156,108
q7 168,144,104
q7 168,148,108
q1 13,1024
q7 164,136,104
q7 164,140,108
q7 160,128,104
q7 160,132,108
q0 4
q7 192,128,112
q7 192,132,116
q5 120,16,38912
q7 196,136,112
q5 124,17,38912
q7 196,140,116
q4 248,21,49664
q7 200,144,112
q4 250,20,49920
q7 200,148,116
q4 252,21,53760
q7 204,152,112
q4 254,20,54016
q7 204,156,116
q1 14,2048
q7 208,160,112
q7 208,164,116
q1 15,3072
q7 212,168,112
q7 212,172,116
q7 216,176,112
q7 216,180,116
q7 220,184,112
q7 220,188,116
s_waitcnt vmcnt(21) lgkmcnt(4)
q7 252,184,120
s_waitcnt vmcnt(16) lgkmcnt(0)
s_barrier
q7 252,188,124
s_lshl_b32 s48,s30,2
q5 96,18,0
s_add_u32 s48,s48,0
q7 248,176,120
q5 100,19,0
q7 248,180,124
s_cmp_eq_u32 s31,0
s_cselect_b32 s48,s48,1073741824
v_mov_b32 v24,s81
q7 244,168,120
s_add_u32 m0,s88,32768
s_xor_b32 s86,s12,132352
s_add_u32 s86,s86,s64
q7 244,172,124
v_mov_b32 v25,0
q7 240,160,120
buffer_store_dword v24,v25,s[32:35],s48 offen sc1
q7 240,164,124
q2 8,0
q7 236,152,120
q7 236,156,124
q7 232,144,120
q7 232,148,124
q7 228,136,120
q7 228,140,124
q7 224,128,120
q2 9,1024
q7 224,132,124
q0 0
q7 0,192,96
q7 0,196,100
q5 104,18,2048
)"
R"(q7 4,200,96
q5 108,19,2048
q7 4,204,100
q4 128,22,16384
q7 8,208,96
q4 130,23,16640
q7 8,212,100
q4 132,22,20480
q7 12,216,96
q4 134,23,20736
q7 12,220,100
q4 136,23,16384
q7 16,224,96
q4 138,22,16640
q7 16,228,100
q2 10,2048
q7 20,232,96
q7 20,236,100
q7 24,240,96
s_xor_b32 s87,s13,5767296
q2 11,3072
s_add_u32 s87,s87,s65
q7 24,244,100
s_add_u32 m0,s89,32768
q7 28,248,96
q7 28,252,100
q0 6
q7 60,248,104
q7 60,252,108
q5 112,18,4096
q7 56,240,104
q5 116,19,4096
q7 56,244,108
q4 140,23,20480
q7 52,232,104
q4 142,22,20736
q7 52,236,108
q4 144,22,16896
q7 48,224,104
q4 146,23,17152
q7 48,228,108
q4 148,22,20992
q7 44,216,104
q4 150,23,21248
q7 44,220,108
q1 12,0
q7 40,208,104
q7 40,212,108
q1 13,1024
q7 36,200,104
q7 36,204,108
q7 32,192,104
q7 32,196,108
q0 6
q7 64,192,112
q7 64,196,116
q5 120,18,6144
q7 68,200,112
q5 124,19,6144
q7 68,204,116
q4 152,23,16896
q7 72,208,112
q4 154,22,17152
q7 72,212,116
q4 156,23,20992
q7 76,216,112
q4 158,22,21248
q7 76,220,116
q1 14,2048
q7 80,224,112
q7 80,228,116
q1 15,3072
q7 84,232,112
q7 84,236,116
q7 88,240,112
q7 88,244,116
q7 92,248,112
q7 92,252,116
q0 4
q7 124,248,120
s_waitcnt vmcnt(17) lgkmcnt(0)
s_barrier
q7 124,252,124
q5 96,18,32768
q7 120,240,120
q5 100,19,32768
q7 120,244,124
q4 160,22,49152
q7 116,232,120
s_xor_b32 s86,s12,1408
q4 162,23,49408
s_add_u32 s86,s86,s64
q7 116,236,124
s_add_u32 m0,s88,65536
q4 164,22,53248
q7 112,224,120
q4 166,23,53504
q7 112,228,124
q2 8,0
q7 108,216,120
q7 108,220,124
q7 104,208,120
q7 104,212,124
q7 100,200,120
q7 100,204,124
q7 96,192,120
q2 9,1024
q7 96,196,124
q0 4
q7 128,192,96
q7 128,196,100
q5 104,18,34816
q7 132,200,96
q5 108,19,34816
q7 132,204,100
q4 168,23,49152
q7 136,208,96
q4 170,22,49408
q7 136,212,100
q4 172,23,53248
q7 140,216,96
q4 174,22,53504
q7 140,220,100
q2 10,2048
q7 144,224,96
q7 144,228,100
q7 148,232,96
q7 148,236,100
q7 152,240,96
s_xor_b32 s87,s13,6291456
q2 11,3072
s_add_u32 s87,s87,s65
q7 152,244,100
s_add_u32 m0,s89,65536
q7 156,248,96
q7 156,252,100
q0 4
q7 188,248,104
q7 188,252,108
q5 112,18,36864
q7 184,240,104
q5 116,19,36864
q7 184,244,108
q4 176,22,49664
q7 180,232,104
q4 178,23,49920
q7 180,236,108
q4 180,22,53760
q7 176,224,104
q4 182,23,54016
q7 176,228,108
q1 12,0
q7 172,216,104
q7 172,220,108
q7 168,208,104
q7 168,212,108
q1 13,1024
q7 164,200,104
q7 164,204,108
q7 160,192,104
q7 160,196,108
q0 4
q7 192,192,112
q7 192,196,116
q5 120,18,38912
q7 196,200,112
q5 124,19,38912
q7 196,204,116
q4 184,23,49664
q7 200,208,112
q4 186,22,49920
q7 200,212,116
q4 188,23,53760
q7 204,216,112
q4 190,22,54016
q7 204,220,116
q1 14,2048
q7 208,224,112
q7 208,228,116
q1 15,3072
q7 212,232,112
q7 212,236,116
q7 216,240,112
q7 216,244,116
q7 220,248,112
q7 220,252,116
q0 4
q7 252,248,120
s_waitcnt vmcnt(17) lgkmcnt(0)
s_barrier
q7 252,252,124
q5 96,16,0
q7 248,240,120
q5 100,17,0
q7 248,244,124
q7 244,232,120
s_add_u32 m0,s88,98304
s_xor_b32 s86,s12,132480
s_add_u32 s86,s86,s64
q7 244,236,124
q7 240,224,120
q7 240,228,124
q2 8,0
q7 236,216,120
q7 236,220,124
q7 232,208,120
q7 232,212,124
q7 228,200,120
q7 228,204,124
q7 224,192,120
q2 9,1024
q7 224,196,124
q0 0
q7 0,128,96
q7 0,132,100
q5 104,16,2048
q7 4,136,96
q5 108,17,2048
q7 4,140,100
q4 192,20,16384
q7 8,144,96
q4 194,21,16640
q7 8,148,100
q4 196,20,20480
q7 12,152,96
q4 198,21,20736
q7 12,156,100
q4 200,21,16384
q7 16,160,96
q4 202,20,16640
q7 16,164,100
q2 10,2048
q7 20,168,96
q7 20,172,100
q7 24,176,96
s_xor_b32 s87,s13,6291584
q2 11,3072
s_add_u32 s87,s87,s65
q7 24,180,100
s_mov_b32 s93,0
buffer_load_dwordx4 v[80:83],v2,s[32:35],s93 offen sc1
s_add_u32 m0,s89,98304
q7 28,184,96
q7 28,188,100
q0 6
q7 60,184,104
q7 60,188,108
q5 112,16,4096
q7 56,176,104
q5 116,17,4096
q7 56,180,108
q4 204,21,20480
q7 52,168,104
q4 206,20,20736
q7 52,172,108
q4 208,20,16896
q7 48,160,104
q4 210,21,17152
q7 48,164,108
q4 212,20,20992
q7 44,152,104
q4 214,21,21248
q7 44,156,108
q1 12,0
q7 40,144,104
q7 40,148,108
q1 13,1024
q7 36,136,104
q7 36,140,108
q7 32,128,104
q7 32,132,108
q0 6
q7 64,128,112
q7 64,132,116
q5 120,16,6144
q7 68,136,112
q5 124,17,6144
q7 68,140,116
q4 216,21,16896
q7 72,144,112
q4 218,20,17152
q7 72,148,116
q4 220,21,20992
q7 76,152,112
q4 222,20,21248
q7 76,156,116
q1 14,2048
q7 80,160,112
q7 80,164,116
q1 15,3072
q7 84,168,112
q7 84,172,116
q7 88,176,112
q7 88,180,116
q7 92,184,112
q7 92,188,116
LHH1_1kf:
s_cbranch_execz LHR1kf
q0 4
q7 124,184,120
s_waitcnt vmcnt(17) lgkmcnt(0)
s_barrier
q7 124,188,124
q5 96,16,32768
q7 120,176,120
q5 100,17,32768
q7 120,180,124
q4 224,20,49152
q7 116,168,120
s_xor_b32 s86,s12,1536
q4 226,21,49408
s_add_u32 s86,s86,s64
q7 116,172,124
s_add_u32 m0,s88,0
q4 228,20,53248
q7 112,160,120
q4 230,21,53504
q7 112,164,124
q2 8,0
q7 108,152,120
q7 108,156,124
q7 104,144,120
q7 104,148,124
q7 100,136,120
q7 100,140,124
q7 96,128,120
q2 9,1024
q7 96,132,124
q0 4
q7 128,128,96
q7 128,132,100
q5 104,16,34816
q7 132,136,96
q5 108,17,34816
q7 132,140,100
q4 232,21,49152
q7 136,144,96
q4 234,20,49408
q7 136,148,100
q4 236,21,53248
q7 140,152,96
q4 238,20,53504
q7 140,156,100
q2 10,2048
q7 144,160,96
q7 144,164,100
q7 148,168,96
q7 148,172,100
q7 152,176,96
s_xor_b32 s87,s13,6815744
q2 11,3072
s_add_u32 s87,s87,s65
q7 152,180,100
s_add_u32 m0,s89,0
q7 156,184,96
q7 156,188,100
q0 4
q7 188,184,104
q7 188,188,108
q5 112,16,36864
q7 184,176,104
q5 116,17,36864
q7 184,180,108
q4 240,20,49664
q7 180,168,104
q4 242,21,49920
q7 180,172,108
q4 244,20,53760
q7 176,160,104
q4 246,21,54016
q7 176,164,108
q1 12,0
q7 172,152,104
q7 172,156,108
q7 168,144,104
q7 168,148,108
q1 13,1024
q7 164,136,104
q7 164,140,108
q7 160,128,104
q7 160,132,108
q0 4
q7 192,128,112
q7 192,132,116
q5 120,16,38912
q7 196,136,112
q5 124,17,38912
q7 196,140,116
q4 248,21,49664
q7 200,144,112
q4 250,20,49920
q7 200,148,116
q4 252,21,53760
q7 204,152,112
q4 254,20,54016
q7 204,156,116
q1 14,2048
q7 208,160,112
q7 208,164,116
q1 15,3072
q7 212,168,112
q7 212,172,116
q7 216,176,112
q7 216,180,116
q7 220,184,112
q7 220,188,116
q0 4
q7 252,184,120
s_waitcnt vmcnt(17) lgkmcnt(0)
s_barrier
q7 252,188,124
q5 96,18,0
q7 248,176,120
q5 100,19,0
q7 248,180,124
q7 244,168,120
s_add_u32 m0,s88,32768
s_xor_b32 s86,s12,132608
s_add_u32 s86,s86,s64
q7 244,172,124
q7 240,160,120
q7 240,164,124
q2 8,0
q7 236,152,120
q7 236,156,124
q7 232,144,120
q7 232,148,124
q7 228,136,120
q7 228,140,124
q7 224,128,120
q2 9,1024
q7 224,132,124
q0 0
q7 0,192,96
q7 0,196,100
q5 104,18,2048
q7 4,200,96
q5 108,19,2048
q7 4,204,100
q4 128,22,16384
q7 8,208,96
q4 130,23,16640
q7 8,212,100
q4 132,22,20480
q7 12,216,96
q4 134,23,20736
q7 12,220,100
q4 136,23,16384
q7 16,224,96
q4 138,22,16640
q7 16,228,100
q2 10,2048
q7 20,232,96
q7 20,236,100
q7 24,240,96
s_xor_b32 s87,s13,6815872
q2 11,3072
s_add_u32 s87,s87,s65
q7 24,244,100
s_add_u32 m0,s89,32768
q7 28,248,96
q7 28,252,100
s_waitcnt vmcnt(16) lgkmcnt(6)
q7 60,248,104
v_xor_b32 v80,s81,v80
q7 60,252,108
q5 112,18,4096
v_xor_b32 v81,s81,v81
q7 56,240,104
q5 116,19,4096
v_xor_b32 v82,s81,v82
q7 56,244,108
q4 140,23,20480
v_xor_b32 v83,s81,v83
q7 52,232,104
q4 142,22,20736
v_or3_b32 v80,v80,v81,v82
q7 52,236,108
q4 144,22,16896
v_or_b32 v80,v80,v83
q7 48,224,104
q4 146,23,17152
v_cmp_ne_u32 vcc,0,v80
q7 48,228,108
q4 148,22,20992
s_cbranch_vccnz Lslow5kf_nb
Lback6kf_nb:
q7 44,216,104
s_or_b32 s49,s30,s31
q4 150,23,21248
q7 44,220,108
q1 12,0
q7 40,208,104
s_cmp_eq_u32 s49,0
s_mov_b32 s49,1073741824
s_cselect_b32 s49,32768,s49
q7 40,212,108
q1 13,1024
v_mov_b32 v30,s99
q7 36,200,104
v_mov_b32 v31,0
q7 36,204,108
s_lshl_b32 s92,s68,11
buffer_store_dword v30,v31,s[32:35],s49 offen sc1
s_add_u32 s92,s92,65536
q7 32,192,104
s_lshl3_add_u32 s48,s69,s70
s_lshl_b32 s48,s48,7
s_add_u32 s48,s48,s92
q7 32,196,108
q0 6
s_cmp_eq_u32 s31,0
s_cselect_b32 s91,s48,1073741824
q7 64,192,112
s_and_b32 s50,s68,1
s_lshl_b32 s50,s50,3
s_add_u32 s82,s50,s70
q7 64,196,116
s_lshl_b32 s48,s82,3
q5 120,18,6144
s_add_u32 s49,s48,s31
q7 68,200,112
s_mul_i32 s49,s49,14336
q5 124,19,6144
s_add_u32 s78,s49,925696
q7 68,204,116
s_sub_u32 s49,s48,s31
q4 152,23,16896
s_add_u32 s49,s49,7
q7 72,208,112
s_mul_i32 s49,s49,14336
q4 154,22,17152
s_add_u32 s79,s49,925696
q7 72,212,116
q4 156,23,20992
q7 76,216,112
q4 158,22,21248
q7 76,220,116
q1 14,2048
q7 80,224,112
q7 80,228,116
q1 15,3072
q7 84,232,112
q7 84,236,116
q7 88,240,112
q7 88,244,116
q7 92,248,112
q7 92,252,116
q0 4
q7 124,248,120
s_waitcnt vmcnt(17) lgkmcnt(0)
s_barrier
q7 124,252,124
q5 96,18,32768
q7 120,240,120
q5 100,19,32768
q7 120,244,124
q4 160,22,49152
q7 116,232,120
s_xor_b32 s86,s12,1664
q4 162,23,49408
s_add_u32 s86,s86,s64
q7 116,236,124
s_add_u32 m0,s88,65536
q4 164,22,53248
q7 112,224,120
q4 166,23,53504
q7 112,228,124
q2 8,0
q7 108,216,120
q7 108,220,124
q7 104,208,120
q7 104,212,124
q7 100,200,120
q7 100,204,124
q7 96,192,120
q2 9,1024
q7 96,196,124
q0 4
q7 128,192,96
q7 128,196,100
q5 104,18,34816
q7 132,200,96
q5 108,19,34816
q7 132,204,100
q4 168,23,49152
q7 136,208,96
q4 170,22,49408
q7 136,212,100
q4 172,23,53248
q7 140,216,96
q4 174,22,53504
q7 140,220,100
q2 10,2048
q7 144,224,96
q7 144,228,100
q7 148,232,96
q7 148,236,100
q7 152,240,96
s_xor_b32 s87,s13,7340032
q2 11,3072
s_add_u32 s87,s87,s65
q7 152,244,100
s_add_u32 m0,s89,65536
q7 156,248,96
q7 156,252,100
q0 4
q7 188,248,104
q7 188,252,108
q5 112,18,36864
q7 184,240,104
q5 116,19,36864
q7 184,244,108
q4 176,22,49664
q7 180,232,104
q4 178,23,49920
q7 180,236,108
q4 180,22,53760
q7 176,224,104
q4 182,23,54016
q7 176,228,108
q1 12,0
q7 172,216,104
q7 172,220,108
q7 168,208,104
q7 168,212,108
q1 13,1024
q7 164,200,104
q7 164,204,108
q7 160,192,104
q7 160,196,108
q0 4
q7 192,192,112
q7 192,196,116
q5 120,18,38912
q7 196,200,112
q5 124,19,38912
q7 196,204,116
q4 184,23,49664
q7 200,208,112
q4 186,22,49920
q7 200,212,116
q4 188,23,53760
q7 204,216,112
q4 190,22,54016
q7 204,220,116
q1 14,2048
q7 208,224,112
q7 208,228,116
q1 15,3072
q7 212,232,112
q7 212,236,116
q7 216,240,112
q7 216,244,116
q7 220,248,112
q7 220,252,116
q0 4
q7 252,248,120
s_waitcnt vmcnt(17) lgkmcnt(0)
s_barrier
q7 252,252,124
q5 96,16,0
q7 248,240,120
q5 100,17,0
q7 248,244,124
q7 244,232,120
s_add_u32 m0,s88,98304
s_xor_b32 s86,s12,132736
s_add_u32 s86,s86,s64
q7 244,236,124
q7 240,224,120
q7 240,228,124
q2 8,0
q7 236,216,120
q7 236,220,124
q7 232,208,120
q7 232,212,124
q7 228,200,120
q7 228,204,124
q7 224,192,120
q2 9,1024
q7 224,196,124
q0 0
q7 0,128,96
q7 0,132,100
q5 104,16,2048
q7 4,136,96
q5 108,17,2048
q7 4,140,100
q4 192,20,16384
q7 8,144,96
q4 194,21,16640
q7 8,148,100
q4 196,20,20480
q7 12,152,96
q4 198,21,20736
q7 12,156,100
q4 200,21,16384
q7 16,160,96
q4 202,20,16640
q7 16,164,100
q2 10,2048
q7 20,168,96
q7 20,172,100
q7 24,176,96
s_xor_b32 s87,s13,7340160
q2 11,3072
s_add_u32 s87,s87,s65
q7 24,180,100
s_add_u32 m0,s89,98304
q7 28,184,96
q7 28,188,100
q0 6
q7 60,184,104
q7 60,188,108
q5 112,16,4096
q7 56,176,104
q5 116,17,4096
q7 56,180,108
q4 204,21,20480
q7 52,168,104
q4 206,20,20736
q7 52,172,108
q4 208,20,16896
q7 48,160,104
q4 210,21,17152
q7 48,164,108
q4 212,20,20992
q7 44,152,104
q4 214,21,21248
q7 44,156,108
q1 12,0
q7 40,144,104
q7 40,148,108
q1 13,1024
q7 36,136,104
q7 36,140,108
q7 32,128,104
q7 32,132,108
q0 6
q7 64,128,112
q7 64,132,116
q5 120,16,6144
q7 68,136,112
q5 124,17,6144
q7 68,140,116
q4 216,21,16896
q7 72,144,112
q4 218,20,17152
q7 72,148,116
q4 220,21,20992
q7 76,152,112
q4 222,20,21248
q7 76,156,116
q1 14,2048
q7 80,160,112
q7 80,164,116
q1 15,3072
q7 84,168,112
q7 84,172,116
q7 88,176,112
q7 88,180,116
q7 92,184,112
q7 92,188,116
q0 4
q7 124,184,120
s_waitcnt vmcnt(16) lgkmcnt(0)
s_barrier
q7 124,188,124
q5 96,16,32768
q7 120,176,120
q5 100,17,32768
q7 120,180,124
q4 224,20,49152
q7 116,168,120
s_xor_b32 s86,s12,1792
q4 226,21,49408
s_add_u32 s86,s86,s64
q7 116,172,124
s_add_u32 m0,s88,0
q4 228,20,53248
q7 112,160,120
q4 230,21,53504
q7 112,164,124
q2 8,0
q7 108,152,120
q7 108,156,124
q7 104,144,120
q7 104,148,124
q7 100,136,120
q7 100,140,124
q7 96,128,120
q2 9,1024
q7 96,132,124
q0 4
q7 128,128,96
s_mov_b32 s46,LENDkf-LCODEkf
s_mov_b32 s45,s7
s_mov_b32 s44,s6
q7 128,132,100
s_mov_b32 s47,0x20000
q5 104,16,34816
s_and_b32 s54,s68,1
q7 132,136,96
s_lshl1_add_u32 s54,s70,s54
q5 108,17,34816
s_lshl2_add_u32 s54,s54,s31
q7 132,140,100
s_lshl_b32 s54,s54,12
q4 232,21,49152
v_and_b32 v31,31,v1
q7 136,144,96
q4 234,20,49408
v_lshlrev_b32 v31,7,v31
q7 136,148,100
q4 236,21,53248
q7 140,152,96
q4 238,20,53504
q7 140,156,100
q2 10,2048
q7 144,160,96
buffer_load_dword v31,v31,s[44:47],s54 offen
q7 144,164,100
q7 148,168,96
q7 148,172,100
q7 152,176,96
s_xor_b32 s87,s13,7864320
q2 11,3072
s_add_u32 s87,s87,s65
q7 152,180,100
s_add_u32 m0,s89,0
q7 156,184,96
q7 156,188,100
q0 4
q7 188,184,104
q7 188,188,108
q5 112,16,36864
q7 184,176,104
q5 116,17,36864
q7 184,180,108
q4 240,20,49664
q7 180,168,104
q4 242,21,49920
q7 180,172,108
q4 244,20,53760
q7 176,160,104
q4 246,21,54016
q7 176,164,108
q1 12,0
q7 172,152,104
LHH1_2kf:
s_cbranch_execz LHR1kf
q7 172,156,108
q7 168,144,104
q7 168,148,108
q1 13,1024
q7 164,136,104
q7 164,140,108
q7 160,128,104
q7 160,132,108
q0 4
q7 192,128,112
q7 192,132,116
q5 120,16,38912
q7 196,136,112
q5 124,17,38912
q7 196,140,116
q4 248,21,49664
q7 200,144,112
q4 250,20,49920
q7 200,148,116
q4 252,21,53760
q7 204,152,112
q4 254,20,54016
q7 204,156,116
q1 14,2048
q7 208,160,112
q7 208,164,116
q1 15,3072
q7 212,168,112
q7 212,172,116
q7 216,176,112
q7 216,180,116
q7 220,184,112
q7 220,188,116
q0 4
q7 252,184,120
s_waitcnt vmcnt(17) lgkmcnt(0)
s_barrier
q7 252,188,124
q5 96,18,0
q7 248,176,120
q5 100,19,0
q7 248,180,124
q7 244,168,120
s_add_u32 m0,s88,32768
s_xor_b32 s86,s12,132864
s_add_u32 s86,s86,s64
q7 244,172,124
q7 240,160,120
q7 240,164,124
q2 8,0
q7 236,152,120
q7 236,156,124
q7 232,144,120
q7 232,148,124
q7 228,136,120
q7 228,140,124
q7 224,128,120
q2 9,1024
q7 224,132,124
q0 0
q7 0,192,96
q7 0,196,100
q5 104,18,2048
q7 4,200,96
q5 108,19,2048
q7 4,204,100
q4 128,22,16384
q7 8,208,96
q4 130,23,16640
q7 8,212,100
q4 132,22,20480
q7 12,216,96
q4 134,23,20736
q7 12,220,100
q4 136,23,16384
q7 16,224,96
q4 138,22,16640
q7 16,228,100
q2 10,2048
q7 20,232,96
q7 20,236,100
q7 24,240,96
s_xor_b32 s87,s13,7864448
q2 11,3072
s_add_u32 s87,s87,s65
q7 24,244,100
s_add_u32 m0,s89,32768
q7 28,248,96
q7 28,252,100
q0 6
q7 60,248,104
q7 60,252,108
q5 112,18,4096
q7 56,240,104
q5 116,19,4096
q7 56,244,108
q4 140,23,20480
q7 52,232,104
q4 142,22,20736
q7 52,236,108
q4 144,22,16896
q7 48,224,104
q4 146,23,17152
q7 48,228,108
q4 148,22,20992
q7 44,216,104
q4 150,23,21248
q7 44,220,108
q1 12,0
q7 40,208,104
q7 40,212,108
q1 13,1024
q7 36,200,104
q7 36,204,108
q7 32,192,104
q7 32,196,108
q0 6
q7 64,192,112
q7 64,196,116
q5 120,18,6144
q7 68,200,112
q5 124,19,6144
q7 68,204,116
q4 152,23,16896
q7 72,208,112
s_add_u32 s48,s78,0
q4 154,22,17152
q7 72,212,116
q4 156,23,20992
q7 76,216,112
q4 158,22,21248
q7 76,220,116
q1 14,2048
q7 80,224,112
buffer_load_dwordx4 v[32:35],v2,s[32:35],s48 offen offset:0 sc1
q7 80,228,116
q1 15,3072
q7 84,232,112
q7 84,236,116
q7 88,240,112
q7 88,244,116
q7 92,248,112
q7 92,252,116
q0 4
q7 124,248,120
s_waitcnt vmcnt(18) lgkmcnt(0)
s_barrier
q7 124,252,124
q5 96,18,32768
q7 120,240,120
q5 100,19,32768
q7 120,244,124
q4 160,22,49152
q7 116,232,120
s_xor_b32 s86,s12,1920
q4 162,23,49408
s_add_u32 s86,s86,s64
q7 116,236,124
s_add_u32 m0,s88,65536
q4 164,22,53248
q7 112,224,120
q4 166,23,53504
q7 112,228,124
q2 8,0
q7 108,216,120
buffer_load_dwordx4 v[36:39],v2,s[32:35],s48 offen offset:1024 sc1
s_add_u32 s48,s79,0
q7 108,220,124
q7 104,208,120
q7 104,212,124
q7 100,200,120
q7 100,204,124
q7 96,192,120
q7 96,196,124
q0 4
q7 128,192,96
q7 128,196,100
q5 104,18,34816
q7 132,200,96
q5 108,19,34816
q7 132,204,100
q4 168,23,49152
q7 136,208,96
q4 170,22,49408
q7 136,212,100
q4 172,23,53248
q7 140,216,96
q4 174,22,53504
q7 140,220,100
q2 9,1024
q7 144,224,96
buffer_load_dwordx4 v[40:43],v2,s[32:35],s48 offen offset:0 sc1
q7 144,228,100
q7 148,232,96
q7 148,236,100
q7 152,240,96
q7 152,244,100
q7 156,248,96
q7 156,252,100
q0 4
q7 188,248,104
q7 188,252,108
q5 112,18,36864
q7 184,240,104
q5 116,19,36864
q7 184,244,108
q4 176,22,49664
q7 180,232,104
q4 178,23,49920
q7 180,236,108
q4 180,22,53760
q7 176,224,104
q4 182,23,54016
q7 176,228,108
q2 10,2048
q7 172,216,104
buffer_load_dwordx4 v[44:47],v2,s[32:35],s48 offen offset:1024 sc1
q7 172,220,108
q7 168,208,104
q7 168,212,108
q7 164,200,104
q7 164,204,108
q7 160,192,104
q7 160,196,108
q0 4
q7 192,192,112
q3 20
q7 192,196,116
q5 120,18,38912
q7 196,200,112
q5 124,19,38912
q7 196,204,116
s_add_u32 s48,s78,0
q4 184,23,49664
q7 200,208,112
q4 186,22,49920
q7 200,212,116
q4 188,23,53760
q7 204,216,112
q4 190,22,54016
q7 204,220,116
q2 11,3072
q7 208,224,112
buffer_load_dwordx4 v[48:51],v2,s[32:35],s48 offen offset:2048 sc1
q7 208,228,116
q7 212,232,112
q7 212,236,116
q7 216,240,112
q7 216,244,116
q7 220,248,112
q7 220,252,116
q0 4
q7 252,248,120
s_waitcnt vmcnt(17) lgkmcnt(0)
s_barrier
q7 252,252,124
q5 96,16,0
q7 248,240,120
q5 100,17,0
q7 248,244,124
buffer_load_dwordx4 v[52:55],v2,s[32:35],s48 offen offset:3072 sc1
s_add_u32 s48,s79,0
q7 244,232,120
s_add_u32 m0,s88,98304
s_xor_b32 s86,s12,132992
s_add_u32 s86,s86,s64
q7 244,236,124
q7 240,224,120
q7 240,228,124
q2 8,0
q7 236,216,120
q7 236,220,124
q7 232,208,120
q7 232,212,124
q7 228,200,120
q7 228,204,124
q7 224,192,120
buffer_load_dwordx4 v[56:59],v2,s[32:35],s48 offen offset:2048 sc1
q7 224,196,124
q0 0
s_mul_i32 s50,s71,896
s_add_u32 s50,s50,917504
q7 0,128,96
s_lshl_b32 s51,s31,10
s_add_u32 s51,s51,114688
q7 0,132,100
q5 104,16,2048
q7 4,136,96
q5 108,17,2048
q7 4,140,100
q4 192,20,16384
q7 8,144,96
q4 194,21,16640
q7 8,148,100
q4 196,20,20480
q7 12,152,96
q4 198,21,20736
q7 12,156,100
q4 200,21,16384
q7 16,160,96
q4 202,20,16640
q7 16,164,100
q2 9,1024
q7 20,168,96
buffer_load_dwordx4 v[60:63],v2,s[32:35],s48 offen offset:3072 sc1
q7 20,172,100
s_mov_b32 s76,m0
s_mov_b32 m0,s51
s_nop 0
buffer_load_dwordx4 v2,s[32:35],s50 offen sc1 lds
s_mov_b32 m0,s76
q7 24,176,96
v_and_b32 v30,48,v1
v_add_u32 v30,s51,v30
q7 24,180,100
q7 28,184,96
q7 28,188,100
q0 6
q7 60,184,104
q7 60,188,108
q5 112,16,4096
q7 56,176,104
q5 116,17,4096
q7 56,180,108
q4 204,21,20480
q7 52,168,104
q4 206,20,20736
q7 52,172,108
s_add_u32 s48,s78,4096
q4 208,20,16896
q7 48,160,104
q4 210,21,17152
q7 48,164,108
q4 212,20,20992
q7 44,152,104
q4 214,21,21248
q7 44,156,108
q2 10,2048
q7 40,144,104
buffer_load_dwordx4 v[64:67],v2,s[32:35],s48 offen offset:0 sc1
q7 40,148,108
q7 36,136,104
q7 36,140,108
q7 32,128,104
q7 32,132,108
q0 6
q7 64,128,112
q7 64,132,116
q5 120,16,6144
q7 68,136,112
q5 124,17,6144
q7 68,140,116
q4 216,21,16896
q7 72,144,112
q4 218,20,17152
q7 72,148,116
q4 220,21,20992
q7 76,152,112
q4 222,20,21248
q7 76,156,116
q2 11,3072
q7 80,160,112
buffer_load_dwordx4 v[68:71],v2,s[32:35],s48 offen offset:1024 sc1
s_add_u32 s48,s79,4096
q7 80,164,116
q7 84,168,112
q7 84,172,116
q7 88,176,112
q7 88,180,116
q7 92,184,112
q7 92,188,116
q0 4
q7 124,184,120
s_waitcnt vmcnt(18) lgkmcnt(0)
s_barrier
q7 124,188,124
q5 96,16,32768
q7 120,176,120
q5 100,17,32768
q7 120,180,124
q4 224,20,49152
q7 116,168,120
s_mov_b32 m0,s74
q4 226,21,49408
q7 116,172,124
q4 228,20,53248
q7 112,160,120
q4 230,21,53504
q7 112,164,124
qc 2,0
q7 108,152,120
buffer_load_dwordx4 v[72:75],v2,s[32:35],s48 offen offset:0 sc1
q7 108,156,124
q7 104,144,120
q7 104,148,124
q7 100,136,120
q7 100,140,124
q7 96,128,120
qc 2,1024
q7 96,132,124
q0 4
q7 128,128,96
q7 128,132,100
q5 104,16,34816
q7 132,136,96
q5 108,17,34816
q7 132,140,100
q4 232,21,49152
q7 136,144,96
q4 234,20,49408
q7 136,148,100
q4 236,21,53248
q7 140,152,96
q4 238,20,53504
q7 140,156,100
qc 2,2048
q7 144,160,96
buffer_load_dwordx4 v[76:79],v2,s[32:35],s48 offen offset:1024 sc1
q7 144,164,100
q7 148,168,96
q7 148,172,100
q7 152,176,96
qc 2,3072
s_add_u32 m0,s74,4096
q7 152,180,100
q7 156,184,96
q7 156,188,100
q0 4
q7 188,184,104
q7 188,188,108
q5 112,16,36864
q7 184,176,104
q5 116,17,36864
q7 184,180,108
s_add_u32 s48,s78,4096
q4 240,20,49664
q7 180,168,104
q4 242,21,49920
q7 180,172,108
q4 244,20,53760
q7 176,160,104
q4 246,21,54016
q7 176,164,108
qc 3,0
q7 172,152,104
buffer_load_dwordx4 v[80:83],v2,s[32:35],s48 offen offset:2048 sc1
q7 172,156,108
q7 168,144,104
qc 3,1024
q7 168,148,108
q7 164,136,104
q7 164,140,108
q7 160,128,104
q7 160,132,108
q0 4
q7 192,128,112
q7 192,132,116
q5 120,16,38912
q7 196,136,112
q5 124,17,38912
q7 196,140,116
q4 248,21,49664
q7 200,144,112
q4 250,20,49920
q7 200,148,116
q4 252,21,53760
q7 204,152,112
q4 254,20,54016
q7 204,156,116
s_add_u32 s72,s72,1
qc 3,2048
v_readlane_b32 s73,v5,s72
q7 208,160,112
v_readlane_b32 s74,v6,s72
v_readlane_b32 s75,v7,s72
q7 208,164,116
buffer_load_dwordx4 v[84:87],v2,s[32:35],s48 offen offset:3072 sc1
s_add_u32 s48,s79,4096
q7 212,168,112
q7 212,172,116
q7 216,176,112
q7 216,180,116
q7 220,184,112
q7 220,188,116
q0 4
q7 252,184,120
s_waitcnt vmcnt(22) lgkmcnt(0)
s_barrier
q7 252,188,124
q5 96,18,0
q7 248,176,120
q5 100,19,0
q7 248,180,124
q7 244,168,120
s_mov_b32 m0,s74
buffer_load_dwordx4 v[88:91],v2,s[32:35],s48 offen offset:2048 sc1
q7 244,172,124
q7 240,160,120
q7 240,164,124
qc 2,0
q7 236,152,120
q7 236,156,124
q7 232,144,120
q7 232,148,124
q7 228,136,120
q7 228,140,124
q7 224,128,120
qc 2,1024
q7 224,132,124
q0 0
buffer_load_dwordx4 v[92:95],v2,s[32:35],s48 offen offset:3072 sc1
q7 0,192,96
q7 0,196,100
q5 104,18,2048
q7 4,200,96
q5 108,19,2048
q7 4,204,100
q7 8,208,96
q7 8,212,100
qc 2,2048
q3 20
q7 12,216,96
q5 128,30,0
q7 12,220,100
q5 132,30,64
q7 16,224,96
q5 136,30,128
q7 16,228,100
q5 140,30,192
q7 20,232,96
q5 144,30,256
q7 20,236,100
q5 148,30,320
q7 24,240,96
qc 2,3072
s_add_u32 m0,s74,4096
q7 24,244,100
LHH1_3kf:
s_cbranch_execz LHR1kf
q5 152,30,384
q7 28,248,96
q5 156,30,448
q7 28,252,100
q0 8
q5 160,30,512
q7 60,248,104
q5 164,30,576
q7 60,252,108
q5 112,18,4096
q7 56,240,104
q5 116,19,4096
q7 56,244,108
qc 3,0
q7 52,232,104
q5 168,30,640
q7 52,236,108
q5 172,30,704
q7 48,224,104
q5 176,30,768
q7 48,228,108
q5 180,30,832
q7 44,216,104
q7 44,220,108
q7 40,208,104
qc 3,1024
q7 40,212,108
q7 36,200,104
q7 36,204,108
q7 32,192,104
q7 32,196,108
q0 4
q7 64,192,112
q7 64,196,116
q5 120,18,6144
q7 68,200,112
q5 124,19,6144
q7 68,204,116
s_add_u32 s72,s72,1
qc 3,2048
v_readlane_b32 s73,v5,s72
q7 72,208,112
v_readlane_b32 s74,v6,s72
v_readlane_b32 s75,v7,s72
q7 72,212,116
s_add_u32 s49,s90,0
qd 24,0
qd 25,1
q7 76,216,112
qd 26,2
qd 27,3
q7 76,220,116
qd 28,4
qd 29,5
q7 80,224,112
qd 30,6
qd 31,7
q7 80,228,116
qb 8,24,25
qb 9,26,27
q7 84,232,112
qb 10,28,29
qb 11,30,31
q7 84,236,116
buffer_store_dwordx4 v[8:11],v2,s[32:35],s49 offen offset:0 sc1
q7 88,240,112
q7 88,244,116
q7 92,248,112
q7 92,252,116
q0 0
qd 24,8
qd 25,9
q7 124,248,120
s_waitcnt vmcnt(22) lgkmcnt(0)
s_barrier
qd 26,10
q7 124,252,124
q5 96,18,32768
qd 27,11
q7 120,240,120
q5 100,19,32768
qd 28,12
q7 120,244,124
qd 29,13
qd 30,14
q7 116,232,120
s_mov_b32 m0,s74
qd 31,15
qb 12,24,25
q7 116,236,124
qb 13,26,27
qb 14,28,29
q7 112,224,120
qb 15,30,31
q7 112,228,124
qc 2,0
q7 108,216,120
buffer_store_dwordx4 v[12:15],v2,s[32:35],s49 offen offset:1024 sc1
q7 108,220,124
q7 104,208,120
qd 24,16
qd 25,17
q7 104,212,124
qd 26,18
qd 27,19
q7 100,200,120
qd 28,20
qd 29,21
q7 100,204,124
)"
R"(qd 30,22
qd 31,23
q7 96,192,120
qc 2,1024
qb 8,24,25
q7 96,196,124
q0 0
qb 9,26,27
qb 10,28,29
q7 128,192,96
qb 11,30,31
q7 128,196,100
q5 104,18,34816
q7 132,200,96
q5 108,19,34816
q7 132,204,100
buffer_store_dwordx4 v[8:11],v2,s[32:35],s49 offen offset:2048 sc1
q7 136,208,96
qd 24,24
qd 25,25
q7 136,212,100
qc 2,2048
qd 26,26
q7 140,216,96
qd 27,27
qd 28,28
q7 140,220,100
qd 29,29
qd 30,30
q7 144,224,96
qd 31,31
qb 12,24,25
q7 144,228,100
qb 13,26,27
qb 14,28,29
q7 148,232,96
qb 15,30,31
q7 148,236,100
buffer_store_dwordx4 v[12:15],v2,s[32:35],s49 offen offset:3072 sc1
q7 152,240,96
qc 2,3072
s_add_u32 m0,s74,4096
q7 152,244,100
q7 156,248,96
q7 156,252,100
q0 0
s_add_u32 s49,s90,8192
qd 24,32
q7 188,248,104
qd 25,33
qd 26,34
q7 188,252,108
q5 112,18,36864
qd 27,35
q7 184,240,104
q5 116,19,36864
qd 28,36
q7 184,244,108
qc 3,0
qd 29,37
q7 180,232,104
qd 30,38
qd 31,39
q7 180,236,108
qb 8,24,25
qb 9,26,27
q7 176,224,104
qb 10,28,29
qb 11,30,31
q7 176,228,108
buffer_store_dwordx4 v[8:11],v2,s[32:35],s49 offen offset:0 sc1
q7 172,216,104
q7 172,220,108
q7 168,208,104
qc 3,1024
qd 24,40
q7 168,212,108
qd 25,41
qd 26,42
q7 164,200,104
qd 27,43
qd 28,44
q7 164,204,108
qd 29,45
qd 30,46
q7 160,192,104
qd 31,47
qb 12,24,25
q7 160,196,108
q0 0
qb 13,26,27
qb 14,28,29
q7 192,192,112
qb 15,30,31
q7 192,196,116
q5 120,18,38912
q7 196,200,112
q5 124,19,38912
q7 196,204,116
s_add_u32 s72,s72,1
qc 3,2048
v_readlane_b32 s73,v5,s72
q7 200,208,112
v_readlane_b32 s74,v6,s72
v_readlane_b32 s75,v7,s72
q7 200,212,116
buffer_store_dwordx4 v[12:15],v2,s[32:35],s49 offen offset:1024 sc1
qd 24,48
q7 204,216,112
qd 25,49
qd 26,50
q7 204,220,116
qd 27,51
qd 28,52
q7 208,224,112
qd 29,53
qd 30,54
q7 208,228,116
qd 31,55
qb 8,24,25
q7 212,232,112
qb 9,26,27
qb 10,28,29
q7 212,236,116
qb 11,30,31
q7 216,240,112
buffer_store_dwordx4 v[8:11],v2,s[32:35],s49 offen offset:2048 sc1
q7 216,244,116
q7 220,248,112
q7 220,252,116
q0 0
qd 24,56
qd 25,57
q7 252,248,120
q3 24
s_barrier
v_add_u32 v4,s75,v2
q7 252,252,124
qd 26,58
qd 27,59
q7 248,240,120
q5 96,4,0
qd 28,60
q7 248,244,124
qd 29,61
qd 30,62
q7 244,232,120
q5 100,4,1024
qd 31,63
q7 244,236,124
qb 12,24,25
qb 13,26,27
q7 240,224,120
q5 104,4,2048
qb 14,28,29
q7 240,228,124
qb 15,30,31
q7 236,216,120
q5 108,4,3072
q7 236,220,124
buffer_store_dwordx4 v[12:15],v2,s[32:35],s49 offen offset:3072 sc1
q7 232,208,120
q5 112,4,4096
q7 232,212,124
q7 228,200,120
q5 116,4,5120
q7 228,204,124
q7 224,192,120
q7 224,196,124
s_waitcnt vmcnt(47) lgkmcnt(4)
qe 192,96,40,128
s_mov_b32 m0,s74
q5 120,4,6144
qf 192,100,44
qc 2,0
qf 128,100,36
s_add_u32 s49,s90,32768
q5 124,4,8192
qd 24,128
qf 128,96,32
q0 4
qd 25,129
qd 26,130
qe 196,104,40,132
q5 96,4,9216
qd 27,131
qf 196,108,44
qd 28,132
qd 29,133
qf 132,108,36
q5 100,4,10240
qd 30,134
qf 132,104,32
q0 4
qc 2,1024
qd 31,135
qe 200,112,40,136
q5 104,4,11264
qb 8,24,25
qf 200,116,44
qb 9,26,27
qb 10,28,29
qf 136,116,36
q5 108,4,12288
qb 11,30,31
qf 136,112,32
q0 4
qc 2,2048
qe 204,120,40,140
q5 112,4,13312
qf 204,124,44
buffer_store_dwordx4 v[8:11],v2,s[32:35],s49 offen offset:0 sc1
qf 140,124,36
q5 116,4,14336
qf 140,120,32
q0 4
qe 208,96,40,144
q5 120,4,16384
qd 24,136
qf 208,100,44
qc 2,3072
s_add_u32 m0,s74,4096
qf 144,100,36
q5 124,4,17408
qd 25,137
qf 144,96,32
q0 4
qd 26,138
qd 27,139
qe 212,104,40,148
q5 96,4,18432
qd 28,140
qf 212,108,44
qc 3,0
qd 29,141
qf 148,108,36
q5 100,4,19456
qd 30,142
qf 148,104,32
q0 4
qd 31,143
qb 12,24,25
qe 216,112,40,152
q5 104,4,20480
qb 13,26,27
qf 216,116,44
qb 14,28,29
qb 15,30,31
qf 152,116,36
q5 108,4,21504
qf 152,112,32
q0 4
qc 3,1024
qe 220,120,40,156
q5 112,4,22528
qf 220,124,44
buffer_store_dwordx4 v[12:15],v2,s[32:35],s49 offen offset:1024 sc1
qf 156,124,36
q5 116,4,24576
qd 24,144
qf 156,120,32
q0 4
qc 3,2048
s_add_u32 s72,s72,1
qe 224,96,40,160
q5 120,4,25600
v_readlane_b32 s73,v5,s72
qf 224,100,44
v_readlane_b32 s74,v6,s72
v_readlane_b32 s75,v7,s72
qf 160,100,36
q5 124,4,26624
qd 25,145
qf 160,96,32
q0 4
qd 26,146
qd 27,147
qe 228,104,40,164
q5 96,4,27648
qd 28,148
qf 228,108,44
qd 29,149
qd 30,150
qf 164,108,36
q5 100,4,28672
qd 31,151
qf 164,104,32
q0 4
qb 8,24,25
qb 9,26,27
qe 232,112,40,168
q5 104,4,29696
qb 10,28,29
qf 232,116,44
qb 11,30,31
qf 168,116,36
q5 108,4,30720
qf 168,112,32
s_waitcnt vmcnt(24) lgkmcnt(4)
s_barrier
v_add_u32 v4,s75,v2
qe 236,120,40,172
q5 112,4,0
qf 236,124,44
buffer_store_dwordx4 v[8:11],v2,s[32:35],s49 offen offset:2048 sc1
qd 24,152
qf 172,124,36
q5 116,4,1024
qd 25,153
qf 172,120,32
q0 4
qd 26,154
qd 27,155
qe 240,96,40,176
q5 120,4,2048
qd 28,156
qf 240,100,44
qd 29,157
qd 30,158
qf 176,100,36
q5 124,4,3072
qd 31,159
qf 176,96,32
q0 4
qb 12,24,25
qb 13,26,27
qe 244,104,40,180
q5 96,4,4096
qb 14,28,29
qf 244,108,44
qb 15,30,31
qf 180,108,36
q5 100,4,5120
qf 180,104,32
s_waitcnt vmcnt(50) lgkmcnt(4)
buffer_store_dwordx4 v[12:15],v2,s[32:35],s49 offen offset:3072 sc1
qf 192,112,56
s_mov_b32 m0,s74
q5 104,4,6144
qf 192,116,60
s_mov_b64 exec,0
s_cmp_eq_u32 s31,0
s_cbranch_scc1 LHH3_0kf
s_cmp_eq_u32 s31,1
s_cbranch_scc1 LHH3_1kf
s_cmp_eq_u32 s31,2
s_cbranch_scc1 LHH3_2kf
s_branch LHH3_3kf
LHR3kf:
s_mov_b64 exec,-1
qc 2,0
qf 128,116,52
s_add_u32 s49,s90,40960
q5 108,4,8192
qd 24,160
qf 128,112,48
q0 4
qd 25,161
qd 26,162
qf 132,120,48
q5 112,4,9216
qd 27,163
qf 132,124,52
s_add_u32 s48,s78,8192
buffer_load_dwordx4 v[32:35],v2,s[32:35],s48 offen offset:0 sc1
qd 28,164
qf 196,124,60
q5 116,4,10240
qd 29,165
qf 196,120,56
q0 4
qc 2,1024
qd 30,166
qf 200,96,56
q5 120,4,11264
qd 31,167
qf 200,100,60
qb 8,24,25
qb 9,26,27
qf 136,100,52
q5 124,4,12288
qb 10,28,29
qf 136,96,48
q0 4
qc 2,2048
qb 11,30,31
qf 140,104,48
q5 96,4,13312
qf 140,108,52
buffer_store_dwordx4 v[8:11],v2,s[32:35],s49 offen offset:0 sc1
qf 204,108,60
q5 100,4,14336
qf 204,104,56
q0 4
buffer_load_dwordx4 v[36:39],v2,s[32:35],s48 offen offset:1024 sc1
s_add_u32 s48,s79,8192
qf 208,112,56
q5 104,4,16384
qd 24,168
qf 208,116,60
qc 2,3072
s_add_u32 m0,s74,4096
qf 144,116,52
q5 108,4,17408
qd 25,169
qf 144,112,48
q0 4
qd 26,170
qd 27,171
qf 148,120,48
q5 112,4,18432
qd 28,172
qf 148,124,52
qc 3,0
qd 29,173
qf 212,124,60
q5 116,4,19456
qd 30,174
qf 212,120,56
q0 4
qd 31,175
qb 12,24,25
qf 216,96,56
q5 120,4,20480
qb 13,26,27
qf 216,100,60
buffer_load_dwordx4 v[40:43],v2,s[32:35],s48 offen offset:0 sc1
qb 14,28,29
qf 152,100,52
LHH2_0kf:
s_cbranch_execz LHR2kf
q5 124,4,21504
qb 15,30,31
qf 152,96,48
q0 4
qc 3,1024
qf 156,104,48
q5 96,4,22528
qf 156,108,52
buffer_store_dwordx4 v[12:15],v2,s[32:35],s49 offen offset:1024 sc1
qf 220,108,60
q5 100,4,24576
qd 24,176
qf 220,104,56
q0 4
qc 3,2048
s_add_u32 s72,s72,1
qf 224,112,56
q5 104,4,25600
v_readlane_b32 s73,v5,s72
qf 224,116,60
v_readlane_b32 s74,v6,s72
v_readlane_b32 s75,v7,s72
qf 160,116,52
q5 108,4,26624
qd 25,177
qf 160,112,48
q0 4
buffer_load_dwordx4 v[44:47],v2,s[32:35],s48 offen offset:1024 sc1
qd 26,178
qf 164,120,48
q5 112,4,27648
qd 27,179
qf 164,124,52
qd 28,180
qd 29,181
qf 228,124,60
q5 116,4,28672
qd 30,182
qf 228,120,56
q0 4
qd 31,183
qb 8,24,25
qf 232,96,56
q5 120,4,29696
qb 9,26,27
qf 232,100,60
qb 10,28,29
qb 11,30,31
qf 168,100,52
q5 124,4,30720
qf 168,96,48
s_waitcnt vmcnt(27) lgkmcnt(4)
s_barrier
v_add_u32 v4,s75,v2
qf 172,104,48
q5 96,4,0
qf 172,108,52
buffer_store_dwordx4 v[8:11],v2,s[32:35],s49 offen offset:2048 sc1
qd 24,184
qf 236,108,60
q5 100,4,1024
qd 25,185
qf 236,104,56
q0 4
qd 26,186
qd 27,187
qf 240,112,56
q5 104,4,2048
qd 28,188
qf 240,116,60
qd 29,189
qd 30,190
qf 176,116,52
s_mov_b64 exec,0
s_cmp_eq_u32 s31,0
s_cbranch_scc1 LHH4_0kf
s_cmp_eq_u32 s31,1
s_cbranch_scc1 LHH4_1kf
s_cmp_eq_u32 s31,2
s_cbranch_scc1 LHH4_2kf
s_branch LHH4_3kf
LHR4kf:
s_mov_b64 exec,-1
q5 108,4,3072
qd 31,191
qf 176,112,48
q0 4
qb 12,24,25
qb 13,26,27
qf 180,120,48
q5 112,4,4096
qb 14,28,29
qf 180,124,52
qb 15,30,31
qf 244,124,60
q5 116,4,5120
qf 244,120,56
s_waitcnt vmcnt(55) lgkmcnt(4)
buffer_store_dwordx4 v[12:15],v2,s[32:35],s49 offen offset:3072 sc1
qf 192,96,72
s_mov_b32 m0,s74
q5 120,4,6144
qf 192,100,76
qc 2,0
qf 128,100,68
s_add_u32 s49,s90,16384
q5 124,4,8192
qd 24,64
qf 128,96,64
q0 4
qd 25,65
qd 26,66
qf 132,104,64
q5 96,4,9216
qd 27,67
qf 132,108,68
s_add_u32 s48,s78,8192
buffer_load_dwordx4 v[48:51],v2,s[32:35],s48 offen offset:2048 sc1
qd 28,68
qf 196,108,76
q5 100,4,10240
qd 29,69
qf 196,104,72
q0 4
qc 2,1024
qd 30,70
qf 200,112,72
q5 104,4,11264
qd 31,71
qf 200,116,76
qb 8,24,25
qb 9,26,27
qf 136,116,68
q5 108,4,12288
qb 10,28,29
qf 136,112,64
q0 4
qc 2,2048
qb 11,30,31
qf 140,120,64
q5 112,4,13312
qf 140,124,68
buffer_store_dwordx4 v[8:11],v2,s[32:35],s49 offen offset:0 sc1
qf 204,124,76
q5 116,4,14336
qf 204,120,72
q0 4
buffer_load_dwordx4 v[52:55],v2,s[32:35],s48 offen offset:3072 sc1
s_add_u32 s48,s79,8192
qf 208,96,72
q5 120,4,16384
qd 24,72
qf 208,100,76
qc 2,3072
s_add_u32 m0,s74,4096
qf 144,100,68
q5 124,4,17408
qd 25,73
qf 144,96,64
q0 4
qd 26,74
qd 27,75
qf 148,104,64
q5 96,4,18432
qd 28,76
qf 148,108,68
qc 3,0
qd 29,77
qf 212,108,76
q5 100,4,19456
qd 30,78
qf 212,104,72
q0 4
qd 31,79
qb 12,24,25
qf 216,112,72
q5 104,4,20480
qb 13,26,27
qf 216,116,76
buffer_load_dwordx4 v[56:59],v2,s[32:35],s48 offen offset:2048 sc1
qb 14,28,29
qf 152,116,68
q5 108,4,21504
qb 15,30,31
qf 152,112,64
q0 4
qc 3,1024
qf 156,120,64
q5 112,4,22528
qf 156,124,68
buffer_store_dwordx4 v[12:15],v2,s[32:35],s49 offen offset:1024 sc1
qf 220,124,76
q5 116,4,24576
qd 24,80
qf 220,120,72
q0 4
qc 3,2048
s_add_u32 s72,s72,1
qf 224,96,72
q5 120,4,25600
v_readlane_b32 s73,v5,s72
qf 224,100,76
v_readlane_b32 s74,v6,s72
v_readlane_b32 s75,v7,s72
qf 160,100,68
q5 124,4,26624
qd 25,81
qf 160,96,64
q0 4
buffer_load_dwordx4 v[60:63],v2,s[32:35],s48 offen offset:3072 sc1
qd 26,82
qf 164,104,64
q5 96,4,27648
qd 27,83
qf 164,108,68
qd 28,84
qd 29,85
qf 228,108,76
q5 100,4,28672
qd 30,86
qf 228,104,72
q0 4
qd 31,87
qb 8,24,25
qf 232,112,72
q5 104,4,29696
qb 9,26,27
qf 232,116,76
qb 10,28,29
qb 11,30,31
qf 168,116,68
q5 108,4,30720
qf 168,112,64
s_waitcnt vmcnt(30) lgkmcnt(4)
s_barrier
v_add_u32 v4,s75,v2
qf 172,120,64
s_mov_b64 exec,0
s_cmp_eq_u32 s31,0
s_cbranch_scc1 LHH5_0kf
s_cmp_eq_u32 s31,1
s_cbranch_scc1 LHR5kf
s_cmp_eq_u32 s31,2
s_cbranch_scc1 LHR5kf
s_branch LHR5kf
LHR5kf:
s_mov_b64 exec,-1
q5 112,4,0
qf 172,124,68
buffer_store_dwordx4 v[8:11],v2,s[32:35],s49 offen offset:2048 sc1
qd 24,88
qf 236,124,76
q5 116,4,1024
qd 25,89
qf 236,120,72
q0 4
qd 26,90
qd 27,91
qf 240,96,72
q5 120,4,2048
qd 28,92
qf 240,100,76
qd 29,93
qd 30,94
qf 176,100,68
q5 124,4,3072
qd 31,95
qf 176,96,64
q0 4
qb 12,24,25
qb 13,26,27
qf 180,104,64
q5 96,4,4096
qb 14,28,29
qf 180,108,68
qb 15,30,31
qf 244,108,76
q5 100,4,5120
qf 244,104,72
s_waitcnt vmcnt(60) lgkmcnt(4)
buffer_store_dwordx4 v[12:15],v2,s[32:35],s49 offen offset:3072 sc1
qf 192,112,88
s_mov_b32 m0,s74
q5 104,4,6144
qf 192,116,92
qc 2,0
qf 128,116,84
s_add_u32 s49,s90,24576
q5 108,4,8192
qd 24,96
qf 128,112,80
q0 4
qd 25,97
qd 26,98
qf 132,120,80
q5 112,4,9216
qd 27,99
qf 132,124,84
s_add_u32 s48,s78,12288
buffer_load_dwordx4 v[64:67],v2,s[32:35],s48 offen offset:0 sc1
qd 28,100
qf 196,124,92
q5 116,4,10240
qd 29,101
qf 196,120,88
q0 4
qc 2,1024
qd 30,102
qf 200,96,88
q5 120,4,11264
qd 31,103
qf 200,100,92
qb 8,24,25
qb 9,26,27
qf 136,100,84
q5 124,4,12288
qb 10,28,29
qf 136,96,80
q0 4
qc 2,2048
qb 11,30,31
qf 140,104,80
q5 96,4,13312
qf 140,108,84
buffer_store_dwordx4 v[8:11],v2,s[32:35],s49 offen offset:0 sc1
qf 204,108,92
q5 100,4,14336
qf 204,104,88
q0 4
buffer_load_dwordx4 v[68:71],v2,s[32:35],s48 offen offset:1024 sc1
s_add_u32 s48,s79,12288
qf 208,112,88
q5 104,4,16384
qd 24,104
qf 208,116,92
qc 2,3072
s_add_u32 m0,s74,4096
qf 144,116,84
q5 108,4,17408
qd 25,105
qf 144,112,80
q0 4
qd 26,106
qd 27,107
qf 148,120,80
q5 112,4,18432
qd 28,108
qf 148,124,84
qc 3,0
qd 29,109
qf 212,124,92
q5 116,4,19456
qd 30,110
qf 212,120,88
q0 4
qd 31,111
qb 12,24,25
qf 216,96,88
q5 120,4,20480
qb 13,26,27
qf 216,100,92
buffer_load_dwordx4 v[72:75],v2,s[32:35],s48 offen offset:0 sc1
qb 14,28,29
qf 152,100,84
q5 124,4,21504
qb 15,30,31
qf 152,96,80
q0 4
qc 3,1024
qf 156,104,80
q5 96,4,22528
qf 156,108,84
buffer_store_dwordx4 v[12:15],v2,s[32:35],s49 offen offset:1024 sc1
qf 220,108,92
q5 100,4,24576
qd 24,112
qf 220,104,88
q0 4
qc 3,2048
s_add_u32 s72,s72,1
qf 224,112,88
q5 104,4,25600
v_readlane_b32 s73,v5,s72
qf 224,116,92
v_readlane_b32 s74,v6,s72
v_readlane_b32 s75,v7,s72
qf 160,116,84
q5 108,4,26624
qd 25,113
qf 160,112,80
q0 4
buffer_load_dwordx4 v[76:79],v2,s[32:35],s48 offen offset:1024 sc1
qd 26,114
qf 164,120,80
q5 112,4,27648
qd 27,115
qf 164,124,84
qd 28,116
qd 29,117
qf 228,124,92
q5 116,4,28672
qd 30,118
qf 228,120,88
q0 4
qd 31,119
qb 8,24,25
qf 232,96,88
q5 120,4,29696
qb 9,26,27
qf 232,100,92
qb 10,28,29
qb 11,30,31
qf 168,100,84
q5 124,4,30720
qf 168,96,80
s_waitcnt vmcnt(31) lgkmcnt(4)
s_barrier
v_add_u32 v4,s75,v2
qf 172,104,80
q5 96,4,0
qf 172,108,84
buffer_store_dwordx4 v[8:11],v2,s[32:35],s49 offen offset:2048 sc1
qd 24,120
qf 236,108,92
q5 100,4,1024
qd 25,121
qf 236,104,88
q0 4
qd 26,122
qd 27,123
qf 240,112,88
q5 104,4,2048
qd 28,124
qf 240,116,92
qd 29,125
qd 30,126
qf 176,116,84
q5 108,4,3072
qd 31,127
qf 176,112,80
q0 4
qb 12,24,25
qb 13,26,27
qf 180,120,80
q5 112,4,4096
qb 14,28,29
qf 180,124,84
qb 15,30,31
qf 244,124,92
q5 116,4,5120
qf 244,120,88
s_waitcnt vmcnt(31) lgkmcnt(4)
buffer_store_dwordx4 v[12:15],v2,s[32:35],s49 offen offset:3072 sc1
qf 192,96,40
s_mov_b32 m0,s74
q5 120,4,6144
qf 192,100,44
qc 2,0
qf 128,100,36
s_add_u32 s49,s90,49152
q5 124,4,8192
qd 24,192
qf 128,96,32
q0 4
qd 25,193
qd 26,194
qf 132,104,32
q5 96,4,9216
qd 27,195
qf 132,108,36
qd 28,196
qd 29,197
qf 196,108,44
q5 100,4,10240
qd 30,198
qf 196,104,40
q0 4
qc 2,1024
qd 31,199
qf 200,112,40
q5 104,4,11264
qb 8,24,25
qf 200,116,44
qb 9,26,27
qb 10,28,29
qf 136,116,36
q5 108,4,12288
qb 11,30,31
qf 136,112,32
q0 4
qc 2,2048
qf 140,120,32
q5 112,4,13312
qf 140,124,36
buffer_store_dwordx4 v[8:11],v2,s[32:35],s49 offen offset:0 sc1
qf 204,124,44
q5 116,4,14336
qf 204,120,40
q0 4
qf 208,96,40
q5 120,4,16384
qd 24,200
qf 208,100,44
qc 2,3072
s_add_u32 m0,s74,4096
qf 144,100,36
q5 124,4,17408
qd 25,201
qf 144,96,32
q0 4
qd 26,202
qd 27,203
qf 148,104,32
q5 96,4,18432
qd 28,204
qf 148,108,36
qc 3,0
qd 29,205
qf 212,108,44
q5 100,4,19456
qd 30,206
qf 212,104,40
q0 4
qd 31,207
qb 12,24,25
qf 216,112,40
q5 104,4,20480
qb 13,26,27
qf 216,116,44
qb 14,28,29
qb 15,30,31
qf 152,116,36
q5 108,4,21504
qf 152,112,32
q0 4
qc 3,1024
qf 156,120,32
q5 112,4,22528
qf 156,124,36
buffer_store_dwordx4 v[12:15],v2,s[32:35],s49 offen offset:1024 sc1
qf 220,124,44
q5 116,4,24576
qd 24,208
qf 220,120,40
q0 4
qc 3,2048
s_add_u32 s72,s72,1
qf 224,96,40
q5 120,4,25600
v_readlane_b32 s73,v5,s72
qf 224,100,44
LHH2_1kf:
s_cbranch_execz LHR2kf
v_readlane_b32 s74,v6,s72
v_readlane_b32 s75,v7,s72
qf 160,100,36
q5 124,4,26624
qd 25,209
qf 160,96,32
q0 4
qd 26,210
qd 27,211
qf 164,104,32
q5 96,4,27648
qd 28,212
qf 164,108,36
qd 29,213
qd 30,214
qf 228,108,44
q5 100,4,28672
qd 31,215
qf 228,104,40
q0 4
qb 8,24,25
qb 9,26,27
qf 232,112,40
q5 104,4,29696
qb 10,28,29
qf 232,116,44
qb 11,30,31
qf 168,116,36
q5 108,4,30720
qf 168,112,32
s_waitcnt vmcnt(27) lgkmcnt(4)
s_barrier
v_add_u32 v4,s75,v2
qf 172,120,32
q5 112,4,0
qf 172,124,36
buffer_store_dwordx4 v[8:11],v2,s[32:35],s49 offen offset:2048 sc1
qd 24,216
qf 236,124,44
q5 116,4,1024
qd 25,217
qf 236,120,40
q0 4
qd 26,218
qd 27,219
qf 240,96,40
q5 120,4,2048
qd 28,220
qf 240,100,44
qd 29,221
qd 30,222
qf 176,100,36
q5 124,4,3072
qd 31,223
qf 176,96,32
q0 4
qb 12,24,25
qb 13,26,27
qf 180,104,32
q5 96,4,4096
qb 14,28,29
qf 180,108,36
qb 15,30,31
qf 244,108,44
q5 100,4,5120
qf 244,104,40
s_waitcnt vmcnt(27) lgkmcnt(4)
buffer_store_dwordx4 v[12:15],v2,s[32:35],s49 offen offset:3072 sc1
qf 192,112,56
s_mov_b32 m0,s74
q5 104,4,6144
qf 192,116,60
s_lshl_b32 s93,s83,7
qc 2,0
s_add_u32 s93,s93,196608
qf 128,116,52
s_add_u32 s49,s90,57344
q5 108,4,8192
qd 24,224
qf 128,112,48
q0 4
qd 25,225
qd 26,226
qf 132,120,48
s_lshl_b32 s86,s83,14
q5 112,4,9216
qd 27,227
qf 132,124,52
s_add_u32 s86,s86,47325184
qd 28,228
qd 29,229
qf 196,124,60
s_lshl3_add_u32 s50,s82,s71
q5 116,4,10240
qd 30,230
qf 196,120,56
q0 4
qc 2,1024
qd 31,231
qf 200,96,56
s_mul_i32 s50,s50,114688
q5 120,4,11264
qb 8,24,25
qf 200,100,60
s_lshl_b32 s51,s31,12
qb 9,26,27
qb 10,28,29
qf 136,100,52
s_add_u32 s50,s50,s51
q5 124,4,12288
qb 11,30,31
qf 136,96,48
q0 4
qc 2,2048
s_add_u32 s80,s50,64626688
qf 140,104,48
s_lshl2_add_u32 s50,s82,s31
q5 96,4,13312
s_lshl3_add_u32 s50,s50,s71
qf 140,108,52
s_lshl_b32 s50,s50,7
buffer_store_dwordx4 v[8:11],v2,s[32:35],s49 offen offset:0 sc1
s_add_u32 s87,s50,327680
qf 204,108,60
q5 100,4,14336
qf 204,104,56
q0 4
qf 208,112,56
q5 104,4,16384
qd 24,232
qf 208,116,60
qc 2,3072
s_add_u32 m0,s74,4096
qf 144,116,52
q5 108,4,17408
qd 25,233
qf 144,112,48
q0 4
qd 26,234
qd 27,235
qf 148,120,48
q5 112,4,18432
qd 28,236
qf 148,124,52
qc 3,0
qd 29,237
qf 212,124,60
q5 116,4,19456
qd 30,238
qf 212,120,56
q0 4
qd 31,239
qb 12,24,25
qf 216,96,56
q5 120,4,20480
qb 13,26,27
qf 216,100,60
qb 14,28,29
qb 15,30,31
qf 152,100,52
q5 124,4,21504
qf 152,96,48
q0 4
qc 3,1024
qf 156,104,48
q5 96,4,22528
qf 156,108,52
buffer_store_dwordx4 v[12:15],v2,s[32:35],s49 offen offset:1024 sc1
qf 220,108,60
q5 100,4,24576
qd 24,240
qf 220,104,56
q0 4
qc 3,2048
s_add_u32 s72,s72,1
qf 224,112,56
q5 104,4,25600
v_readlane_b32 s73,v5,s72
qf 224,116,60
v_readlane_b32 s74,v6,s72
v_readlane_b32 s75,v7,s72
qf 160,116,52
q5 108,4,26624
qd 25,241
qf 160,112,48
q0 4
qd 26,242
qd 27,243
qf 164,120,48
q5 112,4,27648
qd 28,244
qf 164,124,52
qd 29,245
qd 30,246
qf 228,124,60
q5 116,4,28672
qd 31,247
qf 228,120,56
q0 4
qb 8,24,25
qb 9,26,27
qf 232,96,56
q5 120,4,29696
qb 10,28,29
qf 232,100,60
qb 11,30,31
qf 168,100,52
q5 124,4,30720
qf 168,96,48
s_waitcnt vmcnt(23) lgkmcnt(4)
s_barrier
v_add_u32 v4,s75,v2
qf 172,104,48
q5 96,4,0
qf 172,108,52
buffer_store_dwordx4 v[8:11],v2,s[32:35],s49 offen offset:2048 sc1
qd 24,248
qf 236,108,60
q5 100,4,1024
qd 25,249
qf 236,104,56
q0 4
qd 26,250
qd 27,251
qf 240,112,56
q5 104,4,2048
qd 28,252
qf 240,116,60
qd 29,253
qd 30,254
qf 176,116,52
q5 108,4,3072
qd 31,255
qf 176,112,48
q0 4
qb 12,24,25
qb 13,26,27
qf 180,120,48
q5 112,4,4096
qb 14,28,29
qf 180,124,52
qb 15,30,31
qf 244,124,60
q5 116,4,5120
qf 244,120,56
s_waitcnt vmcnt(23) lgkmcnt(4)
buffer_store_dwordx4 v[12:15],v2,s[32:35],s49 offen offset:3072 sc1
qf 192,96,72
s_mov_b32 m0,s74
q5 120,4,6144
qf 192,100,76
qc 2,0
qf 128,100,68
q5 124,4,8192
qf 128,96,64
q0 4
qf 132,104,64
q5 96,4,9216
qf 132,108,68
qf 196,108,76
q5 100,4,10240
qf 196,104,72
q0 4
qc 2,1024
qf 200,112,72
q5 104,4,11264
qf 200,116,76
qf 136,116,68
q5 108,4,12288
qf 136,112,64
q0 4
qc 2,2048
qf 140,120,64
q5 112,4,13312
v_mul_f32 v128,v128,abs(v128)
qf 140,124,68
v_mul_f32 v129,v129,abs(v129)
v_mul_f32 v130,v130,abs(v130)
qf 204,124,76
q5 116,4,14336
v_mul_f32 v131,v131,abs(v131)
qf 204,120,72
q0 4
v_mul_f32 v132,v132,abs(v132)
v_mul_f32 v133,v133,abs(v133)
qf 208,96,72
q5 120,4,16384
v_mul_f32 v134,v134,abs(v134)
qf 208,100,76
qc 2,3072
s_add_u32 m0,s74,4096
qf 144,100,68
q5 124,4,17408
v_mul_f32 v135,v135,abs(v135)
qf 144,96,64
q0 4
qb 32,128,129
qb 33,130,131
qf 148,104,64
q5 96,4,18432
qb 34,132,133
qf 148,108,68
qc 3,0
qb 35,134,135
qf 212,108,76
q5 100,4,19456
v_pk_max_i16 v32,v32,0
qf 212,104,72
q0 4
v_pk_max_i16 v33,v33,0
v_pk_max_i16 v34,v34,0
qf 216,112,72
q5 104,4,20480
v_pk_max_i16 v35,v35,0
qf 216,116,76
v_mul_f32 v136,v136,abs(v136)
v_mul_f32 v137,v137,abs(v137)
qf 152,116,68
q5 108,4,21504
v_mul_f32 v138,v138,abs(v138)
qf 152,112,64
q0 4
qc 3,1024
v_mul_f32 v139,v139,abs(v139)
qf 156,120,64
q5 112,4,22528
v_mul_f32 v140,v140,abs(v140)
qf 156,124,68
v_mul_f32 v141,v141,abs(v141)
v_mul_f32 v142,v142,abs(v142)
qf 220,124,76
q5 116,4,24576
v_mul_f32 v143,v143,abs(v143)
qf 220,120,72
q0 4
qc 3,2048
s_add_u32 s72,s72,1
qf 224,96,72
q5 120,4,25600
v_readlane_b32 s73,v5,s72
qf 224,100,76
v_readlane_b32 s74,v6,s72
v_readlane_b32 s75,v7,s72
qf 160,100,68
q5 124,4,26624
qb 36,136,137
qf 160,96,64
q0 4
qb 37,138,139
qb 38,140,141
qf 164,104,64
q5 96,4,27648
qb 39,142,143
qf 164,108,68
v_pk_max_i16 v36,v36,0
v_pk_max_i16 v37,v37,0
qf 228,108,76
q5 100,4,28672
v_pk_max_i16 v38,v38,0
qf 228,104,72
q0 4
v_pk_max_i16 v39,v39,0
v_mul_f32 v144,v144,abs(v144)
qf 232,112,72
q5 104,4,29696
v_mul_f32 v145,v145,abs(v145)
qf 232,116,76
v_mul_f32 v146,v146,abs(v146)
v_mul_f32 v147,v147,abs(v147)
qf 168,116,68
q5 108,4,30720
v_mul_f32 v148,v148,abs(v148)
qf 168,112,64
s_waitcnt vmcnt(20) lgkmcnt(4)
s_barrier
v_add_u32 v4,s75,v2
qf 172,120,64
qg 176,4,0
v_mul_f32 v149,v149,abs(v149)
qf 172,124,68
qg 180,4,1024
v_mul_f32 v150,v150,abs(v150)
qf 236,124,76
qg 184,4,2048
v_mul_f32 v151,v151,abs(v151)
qf 236,120,72
q0 5
qg 188,4,3072
qb 40,144,145
qf 240,96,72
qg 128,4,4096
qb 41,146,147
qf 240,100,76
qb 42,148,149
qb 43,150,151
qf 176,100,68
qg 132,4,5120
v_pk_max_i16 v40,v40,0
qf 176,96,64
q0 6
qg 136,4,6144
v_pk_max_i16 v41,v41,0
qf 180,104,64
qg 140,4,8192
v_pk_max_i16 v42,v42,0
qf 180,108,68
qg 144,4,9216
v_pk_max_i16 v43,v43,0
qf 244,108,76
qg 148,4,10240
v_mul_f32 v152,v152,abs(v152)
qf 244,104,72
v_mul_f32 v153,v153,abs(v153)
v_mul_f32 v154,v154,abs(v154)
v_mul_f32 v155,v155,abs(v155)
v_mul_f32 v156,v156,abs(v156)
v_mul_f32 v157,v157,abs(v157)
v_mul_f32 v158,v158,abs(v158)
v_mul_f32 v159,v159,abs(v159)
qb 44,152,153
qb 45,154,155
qb 46,156,157
qb 47,158,159
v_pk_max_i16 v44,v44,0
v_pk_max_i16 v45,v45,0
v_pk_max_i16 v46,v46,0
v_pk_max_i16 v47,v47,0
v_mul_f32 v160,v160,abs(v160)
v_mul_f32 v161,v161,abs(v161)
v_mul_f32 v162,v162,abs(v162)
v_mul_f32 v163,v163,abs(v163)
v_mul_f32 v164,v164,abs(v164)
v_mul_f32 v165,v165,abs(v165)
v_mul_f32 v166,v166,abs(v166)
v_mul_f32 v167,v167,abs(v167)
qb 48,160,161
qb 49,162,163
qb 50,164,165
qb 51,166,167
v_pk_max_i16 v48,v48,0
v_pk_max_i16 v49,v49,0
v_pk_max_i16 v50,v50,0
v_pk_max_i16 v51,v51,0
v_mul_f32 v168,v168,abs(v168)
v_mul_f32 v169,v169,abs(v169)
v_mul_f32 v170,v170,abs(v170)
v_mul_f32 v171,v171,abs(v171)
v_mul_f32 v172,v172,abs(v172)
v_mul_f32 v173,v173,abs(v173)
v_mul_f32 v174,v174,abs(v174)
v_mul_f32 v175,v175,abs(v175)
qb 52,168,169
qb 53,170,171
qb 54,172,173
qb 55,174,175
v_pk_max_i16 v52,v52,0
v_pk_max_i16 v53,v53,0
v_pk_max_i16 v54,v54,0
v_pk_max_i16 v55,v55,0
v_mul_f32 v176,v176,abs(v176)
v_mul_f32 v177,v177,abs(v177)
v_mul_f32 v178,v178,abs(v178)
v_mul_f32 v179,v179,abs(v179)
v_mul_f32 v180,v180,abs(v180)
v_mul_f32 v181,v181,abs(v181)
v_mul_f32 v182,v182,abs(v182)
v_mul_f32 v183,v183,abs(v183)
qb 56,176,177
qb 57,178,179
qb 58,180,181
qb 59,182,183
v_pk_max_i16 v56,v56,0
v_pk_max_i16 v57,v57,0
v_pk_max_i16 v58,v58,0
v_pk_max_i16 v59,v59,0
v_mul_f32 v224,v224,abs(v224)
v_mul_f32 v225,v225,abs(v225)
v_mul_f32 v226,v226,abs(v226)
v_mul_f32 v227,v227,abs(v227)
v_mul_f32 v228,v228,abs(v228)
v_mul_f32 v229,v229,abs(v229)
v_mul_f32 v230,v230,abs(v230)
v_mul_f32 v231,v231,abs(v231)
qb 80,224,225
qb 81,226,227
qb 82,228,229
qb 83,230,231
v_pk_max_i16 v80,v80,0
v_pk_max_i16 v81,v81,0
v_pk_max_i16 v82,v82,0
v_pk_max_i16 v83,v83,0
v_mul_f32 v232,v232,abs(v232)
v_mul_f32 v233,v233,abs(v233)
v_mul_f32 v234,v234,abs(v234)
v_mul_f32 v235,v235,abs(v235)
v_mul_f32 v236,v236,abs(v236)
v_mul_f32 v237,v237,abs(v237)
v_mul_f32 v238,v238,abs(v238)
v_mul_f32 v239,v239,abs(v239)
qb 84,232,233
qb 85,234,235
qb 86,236,237
qb 87,238,239
v_pk_max_i16 v84,v84,0
v_pk_max_i16 v85,v85,0
v_pk_max_i16 v86,v86,0
v_pk_max_i16 v87,v87,0
v_mul_f32 v240,v240,abs(v240)
v_mul_f32 v241,v241,abs(v241)
v_mul_f32 v242,v242,abs(v242)
v_mul_f32 v243,v243,abs(v243)
v_mul_f32 v244,v244,abs(v244)
v_mul_f32 v245,v245,abs(v245)
v_mul_f32 v246,v246,abs(v246)
v_mul_f32 v247,v247,abs(v247)
qb 88,240,241
)"
R"(qb 89,242,243
qb 90,244,245
qb 91,246,247
v_pk_max_i16 v88,v88,0
v_pk_max_i16 v89,v89,0
v_pk_max_i16 v90,v90,0
v_pk_max_i16 v91,v91,0
v_mul_f32 v192,v192,abs(v192)
v_mul_f32 v193,v193,abs(v193)
v_mul_f32 v194,v194,abs(v194)
v_mul_f32 v195,v195,abs(v195)
v_mul_f32 v196,v196,abs(v196)
v_mul_f32 v197,v197,abs(v197)
v_mul_f32 v198,v198,abs(v198)
v_mul_f32 v199,v199,abs(v199)
qb 64,192,193
qb 65,194,195
qb 66,196,197
qb 67,198,199
v_pk_max_i16 v64,v64,0
v_pk_max_i16 v65,v65,0
v_pk_max_i16 v66,v66,0
v_pk_max_i16 v67,v67,0
LHH2_2kf:
s_cbranch_execz LHR2kf
v_mul_f32 v200,v200,abs(v200)
v_mul_f32 v201,v201,abs(v201)
v_mul_f32 v202,v202,abs(v202)
v_mul_f32 v203,v203,abs(v203)
v_mul_f32 v204,v204,abs(v204)
v_mul_f32 v205,v205,abs(v205)
v_mul_f32 v206,v206,abs(v206)
v_mul_f32 v207,v207,abs(v207)
qb 68,200,201
qb 69,202,203
qb 70,204,205
qb 71,206,207
v_pk_max_i16 v68,v68,0
v_pk_max_i16 v69,v69,0
v_pk_max_i16 v70,v70,0
v_pk_max_i16 v71,v71,0
v_mul_f32 v208,v208,abs(v208)
v_mul_f32 v209,v209,abs(v209)
v_mul_f32 v210,v210,abs(v210)
v_mul_f32 v211,v211,abs(v211)
v_mul_f32 v212,v212,abs(v212)
v_mul_f32 v213,v213,abs(v213)
v_mul_f32 v214,v214,abs(v214)
v_mul_f32 v215,v215,abs(v215)
qb 72,208,209
qb 73,210,211
qb 74,212,213
qb 75,214,215
v_pk_max_i16 v72,v72,0
v_pk_max_i16 v73,v73,0
v_pk_max_i16 v74,v74,0
v_pk_max_i16 v75,v75,0
v_mul_f32 v216,v216,abs(v216)
v_mul_f32 v217,v217,abs(v217)
v_mul_f32 v218,v218,abs(v218)
v_mul_f32 v219,v219,abs(v219)
v_mul_f32 v220,v220,abs(v220)
v_mul_f32 v221,v221,abs(v221)
v_mul_f32 v222,v222,abs(v222)
v_mul_f32 v223,v223,abs(v223)
qb 76,216,217
qb 77,218,219
qb 78,220,221
qb 79,222,223
v_pk_max_i16 v76,v76,0
v_pk_max_i16 v77,v77,0
v_pk_max_i16 v78,v78,0
v_pk_max_i16 v79,v79,0
q0 8
qh 192,176,32
s_mov_b32 m0,s74
qg 152,4,11264
qi 192,180,36
q0 7
qc 2,0
qi 192,184,40
qg 156,4,12288
qi 192,188,44
q0 6
qi 192,128,48
qg 160,4,13312
qi 192,132,52
q0 6
qc 2,1024
qi 192,136,56
qg 164,4,14336
qh 196,176,64
qi 196,180,68
qg 168,4,16384
qi 196,184,72
qc 2,2048
qi 196,188,76
qg 172,4,17408
qi 196,128,80
qi 196,132,84
qg 176,4,18432
qi 196,136,88
q0 8
qc 2,3072
s_add_u32 m0,s74,4096
qh 200,140,32
qg 180,4,19456
qi 200,144,36
q0 7
qi 200,148,40
qg 184,4,20480
qi 200,152,44
q0 6
qc 3,0
qi 200,156,48
qg 188,4,21504
qi 200,160,52
q0 6
qi 200,164,56
qg 128,4,22528
qh 204,140,64
qc 3,1024
qi 204,144,68
qg 132,4,24576
qi 204,148,72
qi 204,152,76
qg 136,4,25600
qi 204,156,80
s_add_u32 s72,s72,1
qc 3,2048
v_readlane_b32 s73,v5,s72
qi 204,160,84
qg 140,4,26624
v_readlane_b32 s74,v6,s72
qi 204,164,88
q0 8
v_readlane_b32 s75,v7,s72
qb 224,192,193
qh 208,168,32
qg 144,4,27648
qb 225,194,195
qi 208,172,36
q0 7
qb 226,200,201
qb 227,202,203
qi 208,176,40
qg 148,4,28672
qj 64,224
qi 208,180,44
q0 6
qj 65,225
qj 66,226
qi 208,184,48
qg 152,4,29696
qj 67,227
qi 208,188,52
q0 6
qb 228,196,197
qb 229,198,199
qi 208,128,56
qg 156,4,30720
qb 230,204,205
qh 212,168,64
q3 14
s_barrier
v_add_u32 v4,s75,v2
qi 212,172,68
qg 160,4,0
qb 231,206,207
qi 212,176,72
qj 72,228
qj 73,229
qi 212,180,76
qg 164,4,1024
qj 74,230
qi 212,184,80
qj 75,231
v_mov_b32 v16,s81
qi 212,188,84
qg 168,4,2048
v_mov_b32 v17,0
qi 212,128,88
q0 8
buffer_store_dword v16,v17,s[32:35],s91 offen sc1
qh 216,132,32
qg 172,4,3072
qi 216,136,36
q0 7
qi 216,140,40
qg 176,4,4096
qi 216,144,44
q0 6
qi 216,148,48
qg 180,4,5120
qi 216,152,52
q0 6
qi 216,156,56
qg 184,4,6144
qh 220,132,64
qi 220,136,68
qg 188,4,8192
qi 220,140,72
qi 220,144,76
qg 128,4,9216
qi 220,148,80
qb 232,208,209
qb 233,210,211
qi 220,152,84
qg 132,4,10240
qb 234,216,217
qi 220,156,88
q0 8
qb 235,218,219
qj 68,232
qh 192,160,32
s_mov_b32 m0,s74
qg 136,4,11264
qj 69,233
qi 192,164,36
q0 7
qc 2,0
qj 70,234
qi 192,168,40
qg 140,4,12288
qj 71,235
qi 192,172,44
q0 6
qi 192,176,48
qg 144,4,13312
qb 236,212,213
qi 192,180,52
q0 6
qc 2,1024
qb 237,214,215
qi 192,184,56
qg 148,4,14336
qb 238,220,221
qh 196,160,64
qb 239,222,223
qj 76,236
qi 196,164,68
qg 152,4,16384
qj 77,237
qi 196,168,72
qc 2,2048
qj 78,238
qi 196,172,76
qg 156,4,17408
qj 79,239
qi 196,176,80
qi 196,180,84
qg 160,4,18432
qi 196,184,88
q0 8
qc 2,3072
s_add_u32 m0,s74,4096
qh 200,188,32
qg 164,4,19456
qi 200,128,36
q0 7
qi 200,132,40
qg 168,4,20480
qi 200,136,44
q0 6
qc 3,0
qi 200,140,48
qg 172,4,21504
qi 200,144,52
q0 6
qi 200,148,56
qg 176,4,22528
qh 204,188,64
qc 3,1024
qi 204,128,68
qg 180,4,24576
qi 204,132,72
qi 204,136,76
qg 184,4,25600
qi 204,140,80
s_add_u32 s72,s72,1
qc 3,2048
v_readlane_b32 s73,v5,s72
qi 204,144,84
qg 188,4,26624
v_readlane_b32 s74,v6,s72
qi 204,148,88
q0 8
v_readlane_b32 s75,v7,s72
qb 224,192,193
qh 208,152,32
qg 128,4,27648
qb 225,194,195
qi 208,156,36
q0 7
qb 226,200,201
qb 227,202,203
qi 208,160,40
qg 132,4,28672
qj 80,224
qi 208,164,44
q0 6
qj 81,225
qj 82,226
qi 208,168,48
qg 136,4,29696
qj 83,227
qi 208,172,52
q0 6
qb 228,196,197
qb 229,198,199
qi 208,176,56
qg 140,4,30720
qb 230,204,205
qh 212,152,64
q3 15
s_barrier
v_add_u32 v4,s75,v2
qi 212,156,68
qg 144,4,0
qb 231,206,207
qi 212,160,72
qj 88,228
qj 89,229
qi 212,164,76
qg 148,4,1024
qj 90,230
qi 212,168,80
qj 91,231
qi 212,172,84
qg 152,4,2048
qi 212,176,88
q0 8
qh 216,180,32
qg 156,4,3072
qi 216,184,36
q0 7
qi 216,188,40
qg 160,4,4096
qi 216,128,44
q0 6
qi 216,132,48
qg 164,4,5120
qi 216,136,52
q0 6
qi 216,140,56
qg 168,4,6144
qh 220,180,64
qi 220,184,68
qg 172,4,8192
qi 220,188,72
qi 220,128,76
qg 176,4,9216
qi 220,132,80
qb 232,208,209
qb 233,210,211
qi 220,136,84
qg 180,4,10240
qb 234,216,217
qi 220,140,88
q0 8
qb 235,218,219
qj 84,232
qh 192,144,32
s_mov_b32 m0,s74
qg 184,4,11264
qj 85,233
qi 192,148,36
q0 7
qc 2,0
qj 86,234
qi 192,152,40
qg 188,4,12288
qj 87,235
qi 192,156,44
q0 6
qi 192,160,48
qg 128,4,13312
qb 236,212,213
qi 192,164,52
q0 6
qc 2,1024
qb 237,214,215
qi 192,168,56
qg 132,4,14336
qb 238,220,221
qh 196,144,64
qb 239,222,223
qj 92,236
qi 196,148,68
qg 136,4,16384
qj 93,237
qi 196,152,72
qc 2,2048
qj 94,238
qi 196,156,76
qg 140,4,17408
qj 95,239
qi 196,160,80
qi 196,164,84
qg 144,4,18432
qi 196,168,88
q0 8
qc 2,3072
s_add_u32 m0,s74,4096
qh 200,172,32
qg 148,4,19456
qi 200,176,36
q0 7
qi 200,180,40
qg 152,4,20480
qi 200,184,44
q0 6
qc 3,0
qi 200,188,48
qg 156,4,21504
qi 200,128,52
q0 6
qi 200,132,56
qg 160,4,22528
qh 204,172,64
qc 3,1024
qi 204,176,68
qg 164,4,24576
qi 204,180,72
qi 204,184,76
qg 168,4,25600
qi 204,188,80
s_add_u32 s72,s72,1
qc 3,2048
v_readlane_b32 s73,v5,s72
qi 204,128,84
qg 172,4,26624
v_readlane_b32 s74,v6,s72
qi 204,132,88
q0 8
v_readlane_b32 s75,v7,s72
qb 224,192,193
qh 208,136,32
qg 176,4,27648
qb 225,194,195
qi 208,140,36
q0 7
qb 226,200,201
qb 227,202,203
qi 208,144,40
qg 180,4,28672
qj 96,224
qi 208,148,44
q0 6
qj 97,225
qj 98,226
qi 208,152,48
qg 184,4,29696
qj 99,227
qi 208,156,52
q0 6
qb 228,196,197
qb 229,198,199
qi 208,160,56
qg 188,4,30720
qb 230,204,205
qh 212,136,64
q3 15
s_barrier
v_add_u32 v4,s75,v2
qi 212,140,68
qg 128,4,0
qb 231,206,207
qi 212,144,72
qj 104,228
qj 105,229
qi 212,148,76
qg 132,4,1024
qj 106,230
qi 212,152,80
qj 107,231
v_mov_b32 v21,0
qi 212,156,84
qg 136,4,2048
qi 212,160,88
q0 8
buffer_load_dword v20,v21,s[32:35],s93 offen sc1
qh 216,164,32
qg 140,4,3072
qi 216,168,36
q0 7
qi 216,172,40
qg 144,4,4096
qi 216,176,44
q0 6
qi 216,180,48
qg 148,4,5120
qi 216,184,52
q0 6
qi 216,188,56
qg 152,4,6144
qh 220,164,64
qi 220,168,68
qg 156,4,8192
qi 220,172,72
qi 220,176,76
qg 160,4,9216
qi 220,180,80
qb 232,208,209
qb 233,210,211
qi 220,184,84
qg 164,4,10240
qb 234,216,217
qi 220,188,88
q0 8
qb 235,218,219
qj 100,232
qh 192,128,32
s_mov_b32 m0,s74
qg 168,4,11264
qj 101,233
qi 192,132,36
q0 7
qc 2,0
qj 102,234
qi 192,136,40
qg 172,4,12288
qj 103,235
qi 192,140,44
q0 6
qi 192,144,48
qg 176,4,13312
qb 236,212,213
qi 192,148,52
q0 6
qc 2,1024
qb 237,214,215
qi 192,152,56
qg 180,4,14336
qb 238,220,221
qh 196,128,64
qb 239,222,223
qj 108,236
qi 196,132,68
qg 184,4,16384
qj 109,237
qi 196,136,72
qc 2,2048
qj 110,238
qi 196,140,76
qg 188,4,17408
qj 111,239
qi 196,144,80
v_and_b32 v19,15,v1
v_lshlrev_b32 v19,7,v19
qi 196,148,84
qg 128,4,18432
qi 196,152,88
q0 8
qc 2,3072
s_add_u32 m0,s74,4096
qh 200,156,32
qg 132,4,19456
qi 200,160,36
q0 7
buffer_load_dword v18,v19,s[32:35],s92 offen sc1
qi 200,164,40
qg 136,4,20480
qi 200,168,44
q0 6
qc 3,0
qi 200,172,48
qg 140,4,21504
qi 200,176,52
s_waitcnt vmcnt(6) lgkmcnt(6)
qi 200,180,56
LHH2_3kf:
s_cbranch_execz LHR2kf
qg 144,4,22528
v_cmp_ne_u32 vcc,s81,v20
qh 204,156,64
qc 3,1024
s_cbranch_vccnz Lslow9kf_nb
Lback10kf_nb:
qi 204,160,68
qg 148,4,24576
qi 204,164,72
buffer_load_dwordx4 a[0:3],v2,s[32:35],s86 offen offset:0 sc1
qi 204,168,76
qg 152,4,25600
qi 204,172,80
s_add_u32 s72,s72,1
qc 3,2048
v_readlane_b32 s73,v5,s72
qi 204,176,84
qg 156,4,26624
v_readlane_b32 s74,v6,s72
qi 204,180,88
q0 8
v_readlane_b32 s75,v7,s72
qb 224,192,193
qh 208,184,32
qg 160,4,27648
qb 225,194,195
qi 208,188,36
q0 7
qb 226,200,201
qb 227,202,203
qi 208,128,40
qg 164,4,28672
qj 112,224
qi 208,132,44
q0 6
qj 113,225
qj 114,226
qi 208,136,48
qg 168,4,29696
qj 115,227
qi 208,140,52
q0 6
qb 228,196,197
qb 229,198,199
qi 208,144,56
qg 172,4,30720
qb 230,204,205
qh 212,184,64
q3 17
s_barrier
v_add_u32 v4,s75,v2
qi 212,188,68
qg 176,4,0
qb 231,206,207
qi 212,128,72
qj 120,228
qj 121,229
qi 212,132,76
qg 180,4,1024
qj 122,230
qi 212,136,80
buffer_load_dwordx4 a[4:7],v2,s[32:35],s86 offen offset:1024 sc1
qj 123,231
qi 212,140,84
qg 184,4,2048
qi 212,144,88
q0 8
qh 216,148,32
qg 188,4,3072
qi 216,152,36
q0 7
qi 216,156,40
qg 128,4,4096
qi 216,160,44
q0 6
buffer_load_dwordx4 a[8:11],v2,s[32:35],s86 offen offset:2048 sc1
qi 216,164,48
qg 132,4,5120
qi 216,168,52
q0 6
qi 216,172,56
qg 136,4,6144
qh 220,148,64
qi 220,152,68
qg 140,4,8192
qi 220,156,72
buffer_load_dwordx4 a[12:15],v2,s[32:35],s86 offen offset:3072 sc1
s_add_u32 s86,s86,4096
qi 220,160,76
qg 144,4,9216
qi 220,164,80
qb 232,208,209
qb 233,210,211
qi 220,168,84
qg 148,4,10240
qb 234,216,217
qi 220,172,88
q0 8
qb 235,218,219
qj 116,232
q3 7
v_cmp_ne_u32 vcc,s81,v18
s_nop 0
s_cbranch_vccnz Lslow7kf_nb
Lback8kf_nb:
qh 192,176,32
s_mov_b32 m0,s74
qg 152,4,11264
qj 117,233
qi 192,180,36
q0 7
qc 2,0
qj 118,234
qi 192,184,40
qg 156,4,12288
qj 119,235
qi 192,188,44
q0 6
buffer_load_dwordx4 a[16:19],v2,s[32:35],s86 offen offset:0 sc1
qi 192,128,48
qg 160,4,13312
qb 236,212,213
qi 192,132,52
q0 6
qc 2,1024
qb 237,214,215
qi 192,136,56
qg 164,4,14336
qb 238,220,221
qh 196,176,64
qc 2,2048
qb 239,222,223
qi 196,180,68
qg 168,4,16384
qj 124,236
qi 196,184,72
qj 125,237
qj 126,238
qi 196,188,76
qg 172,4,17408
qj 127,239
qi 196,128,80
qc 2,3072
s_add_u32 m0,s74,4096
qi 196,132,84
qg 176,4,18432
qi 196,136,88
q0 8
buffer_load_dwordx4 a[20:23],v2,s[32:35],s86 offen offset:1024 sc1
qh 200,140,32
qg 180,4,19456
qi 200,144,36
q0 7
qc 3,0
qi 200,148,40
qg 184,4,20480
qi 200,152,44
q0 6
qc 3,1024
qi 200,156,48
qg 188,4,21504
qi 200,160,52
q0 6
buffer_load_dwordx4 a[24:27],v2,s[32:35],s86 offen offset:2048 sc1
qi 200,164,56
qg 128,4,22528
qh 204,140,64
qc 3,2048
qi 204,144,68
qg 132,4,24576
qi 204,148,72
qi 204,152,76
qg 136,4,25600
qi 204,156,80
s_add_u32 s72,s72,1
qc 3,3072
v_readlane_b32 s73,v5,s72
qi 204,160,84
qg 140,4,26624
v_readlane_b32 s74,v6,s72
qi 204,164,88
q0 8
v_readlane_b32 s75,v7,s72
qb 224,192,193
qh 208,168,32
qg 144,4,27648
qb 225,194,195
qi 208,172,36
q0 7
qb 226,200,201
qb 227,202,203
qi 208,176,40
qg 148,4,28672
qj 192,224
qi 208,180,44
q0 6
qj 193,225
qj 194,226
qi 208,184,48
qg 152,4,29696
qj 195,227
qi 208,188,52
q0 6
qb 228,196,197
qb 229,198,199
qi 208,128,56
qg 156,4,30720
qb 230,204,205
qh 212,168,64
q3 24
s_barrier
v_add_u32 v4,s75,v2
qi 212,172,68
qg 160,4,0
qb 231,206,207
qi 212,176,72
qj 200,228
qj 201,229
qi 212,180,76
qg 164,4,1024
qj 202,230
qi 212,184,80
buffer_load_dwordx4 a[28:31],v2,s[32:35],s86 offen offset:3072 sc1
qj 203,231
qi 212,188,84
s_add_u32 s86,s86,4096
qg 168,4,2048
qi 212,128,88
q0 8
qh 216,132,32
qg 172,4,3072
qi 216,136,36
q0 7
qi 216,140,40
qg 176,4,4096
qi 216,144,44
q0 6
buffer_load_dwordx4 a[32:35],v2,s[32:35],s86 offen offset:0 sc1
qi 216,148,48
qg 180,4,5120
qi 216,152,52
q0 6
qi 216,156,56
qg 184,4,6144
qh 220,132,64
qi 220,136,68
qg 188,4,8192
qi 220,140,72
buffer_load_dwordx4 a[36:39],v2,s[32:35],s86 offen offset:1024 sc1
qi 220,144,76
qg 128,4,9216
qi 220,148,80
qb 232,208,209
qb 233,210,211
qi 220,152,84
qg 132,4,10240
qb 234,216,217
qi 220,156,88
q0 8
qb 235,218,219
qj 196,232
qh 192,160,32
s_mov_b32 m0,s74
qg 136,4,11264
qj 197,233
qi 192,164,36
q0 7
qc 2,0
qj 198,234
qi 192,168,40
qg 140,4,12288
qj 199,235
qi 192,172,44
q0 6
buffer_load_dwordx4 a[40:43],v2,s[32:35],s86 offen offset:2048 sc1
qi 192,176,48
qg 144,4,13312
qb 236,212,213
qi 192,180,52
q0 6
qc 2,1024
qb 237,214,215
qi 192,184,56
qg 148,4,14336
qb 238,220,221
qh 196,160,64
qc 2,2048
qb 239,222,223
qi 196,164,68
qg 152,4,16384
qj 204,236
qi 196,168,72
qj 205,237
qj 206,238
qi 196,172,76
qg 156,4,17408
qj 207,239
qi 196,176,80
qc 2,3072
s_add_u32 m0,s74,4096
qi 196,180,84
qg 160,4,18432
qi 196,184,88
q0 8
buffer_load_dwordx4 a[44:47],v2,s[32:35],s86 offen offset:3072 sc1
s_add_u32 s86,s86,4096
qh 200,188,32
qg 164,4,19456
qi 200,128,36
q0 7
qc 3,0
qi 200,132,40
qg 168,4,20480
qi 200,136,44
q0 6
qc 3,1024
qi 200,140,48
qg 172,4,21504
qi 200,144,52
q0 6
buffer_load_dwordx4 a[48:51],v2,s[32:35],s86 offen offset:0 sc1
qi 200,148,56
qg 176,4,22528
qh 204,188,64
qc 3,2048
qi 204,128,68
qg 180,4,24576
qi 204,132,72
qi 204,136,76
qg 184,4,25600
qi 204,140,80
s_add_u32 s72,s72,1
qc 3,3072
v_readlane_b32 s73,v5,s72
qi 204,144,84
qg 188,4,26624
v_readlane_b32 s74,v6,s72
qi 204,148,88
q0 8
v_readlane_b32 s75,v7,s72
qb 224,192,193
qh 208,152,32
qg 128,4,27648
qb 225,194,195
qi 208,156,36
q0 7
qb 226,200,201
qb 227,202,203
qi 208,160,40
qg 132,4,28672
qj 208,224
qi 208,164,44
q0 6
qj 209,225
qj 210,226
qi 208,168,48
qg 136,4,29696
qj 211,227
qi 208,172,52
q0 6
qb 228,196,197
qb 229,198,199
qi 208,176,56
qg 140,4,30720
qb 230,204,205
qh 212,152,64
q3 28
s_barrier
v_add_u32 v4,s75,v2
qi 212,156,68
qg 144,4,0
qb 231,206,207
qi 212,160,72
qj 216,228
qj 217,229
qi 212,164,76
qg 148,4,1024
qj 218,230
qi 212,168,80
buffer_load_dwordx4 a[52:55],v2,s[32:35],s86 offen offset:1024 sc1
qj 219,231
qi 212,172,84
qg 152,4,2048
qi 212,176,88
q0 8
qh 216,180,32
qg 156,4,3072
qi 216,184,36
q0 7
qi 216,188,40
qg 160,4,4096
qi 216,128,44
q0 6
buffer_load_dwordx4 a[56:59],v2,s[32:35],s86 offen offset:2048 sc1
qi 216,132,48
qg 164,4,5120
qi 216,136,52
q0 6
qi 216,140,56
qg 168,4,6144
qh 220,180,64
qi 220,184,68
qg 172,4,8192
qi 220,188,72
buffer_load_dwordx4 a[60:63],v2,s[32:35],s86 offen offset:3072 sc1
s_add_u32 s86,s86,4096
qi 220,128,76
qg 176,4,9216
qi 220,132,80
qb 232,208,209
qb 233,210,211
qi 220,136,84
qg 180,4,10240
qb 234,216,217
qi 220,140,88
q0 8
qb 235,218,219
qj 212,232
qh 192,144,32
s_mov_b32 m0,s74
qg 184,4,11264
qj 213,233
qi 192,148,36
q0 7
qc 2,0
qj 214,234
qi 192,152,40
qg 188,4,12288
qj 215,235
qi 192,156,44
q0 6
qi 192,160,48
qg 128,4,13312
qb 236,212,213
qi 192,164,52
q0 6
qc 2,1024
qb 237,214,215
qi 192,168,56
qg 132,4,14336
qb 238,220,221
qh 196,144,64
qc 2,2048
qb 239,222,223
qi 196,148,68
qg 136,4,16384
qj 220,236
qi 196,152,72
qj 221,237
qj 222,238
qi 196,156,76
qg 140,4,17408
qj 223,239
qi 196,160,80
qc 2,3072
s_add_u32 m0,s74,4096
qi 196,164,84
qg 144,4,18432
qi 196,168,88
q0 8
qh 200,172,32
qg 148,4,19456
qi 200,176,36
q0 7
qc 3,0
qi 200,180,40
qg 152,4,20480
qi 200,184,44
q0 6
qc 3,1024
qi 200,188,48
qg 156,4,21504
qi 200,128,52
q0 6
qi 200,132,56
qg 160,4,22528
qh 204,172,64
qc 3,2048
qi 204,176,68
qg 164,4,24576
qi 204,180,72
qi 204,184,76
qg 168,4,25600
qi 204,188,80
s_add_u32 s72,s72,1
qc 3,3072
v_readlane_b32 s73,v5,s72
qi 204,128,84
qg 172,4,26624
v_readlane_b32 s74,v6,s72
qi 204,132,88
q0 8
v_readlane_b32 s75,v7,s72
qb 224,192,193
qh 208,136,32
qg 176,4,27648
qb 225,194,195
qi 208,140,36
q0 7
qb 226,200,201
qb 227,202,203
qi 208,144,40
qg 180,4,28672
qj 224,224
qi 208,148,44
q0 6
qj 225,225
qj 226,226
qi 208,152,48
qg 184,4,29696
qj 227,227
qi 208,156,52
q0 6
qb 228,196,197
qb 229,198,199
qi 208,160,56
qg 188,4,30720
qb 230,204,205
qh 212,136,64
q3 25
s_barrier
v_add_u32 v4,s75,v2
qi 212,140,68
qg 128,4,0
qb 231,206,207
qi 212,144,72
qj 232,228
qj 233,229
qi 212,148,76
qg 132,4,1024
qj 234,230
qi 212,152,80
qj 235,231
qi 212,156,84
qg 136,4,2048
qi 212,160,88
q0 8
qh 216,164,32
qg 140,4,3072
qi 216,168,36
q0 7
qi 216,172,40
qg 144,4,4096
qi 216,176,44
q0 6
qi 216,180,48
qg 148,4,5120
qi 216,184,52
q0 6
qi 216,188,56
qg 152,4,6144
qh 220,164,64
qi 220,168,68
qg 156,4,7168
qi 220,172,72
LHH3_0kf:
s_cbranch_execz LHR3kf
qi 220,176,76
qg 160,4,8192
qi 220,180,80
qb 232,208,209
qb 233,210,211
qi 220,184,84
qg 164,4,9216
qb 234,216,217
qi 220,188,88
qb 235,218,219
qj 228,232
qj 229,233
qj 230,234
qj 231,235
s_nop 7
qb 236,212,213
qb 237,214,215
qb 238,220,221
qb 239,222,223
qj 236,236
qj 237,237
qj 238,238
qj 239,239
s_waitcnt vmcnt(8) lgkmcnt(8)
qk 36,128,32
s_mov_b32 m0,s74
qg 168,4,10240
v_mov_b32 v126,0xff800000
v_mov_b32 v127,0x49800000
ql 36,132,36
q0 7
qc 2,0
s_sub_u32 s82,15,s70
ql 36,136,40
s_lshr_b32 s52,s68,2
qg 172,4,11264
s_lshl_b32 s53,s82,2
ql 36,140,44
q0 6
s_add_u32 s53,s53,s52
s_lshl_b32 s94,s53,10
ql 36,144,48
s_add_u32 s94,s94,327680
qg 176,4,12288
s_lshl_b32 s95,s53,11
ql 36,148,52
q0 5
qc 2,1024
s_add_u32 s95,s95,393216
ql 36,152,56
s_lshl_b32 s54,s31,4
qg 180,4,13312
s_add_u32 s54,s54,s68
ql 36,156,60
s_mul_i32 s48,s82,917504
s_lshl_b32 s54,s54,10
s_add_u32 s48,s48,s54
qk 32,128,0
s_add_u32 s96,s48,64626688
qg 184,4,14336
s_mul_i32 s48,s82,1835008
ql 32,132,4
s_add_u32 s48,s48,s54
qc 2,2048
s_add_u32 s98,s48,79306752
ql 32,136,8
qg 188,4,15360
ql 32,140,12
s_cmp_eq_u32 s31,3
s_cselect_b32 s88,0,65536
s_add_u32 s97,s96,s88
ql 32,144,16
s_sub_u32 s49,7,s52
qg 128,4,16384
v_max3_f32 v121,v36,v37,v38
ql 32,148,20
s_lshl_b32 s50,s82,3
s_bitcmp1_b32 s68,1
s_cselect_b32 s49,s49,s52
ql 32,152,24
s_add_u32 s49,s49,s50
qg 132,4,17408
s_mul_i32 s89,s49,14336
ql 32,156,28
q0 8
qc 2,3072
s_add_u32 m0,s74,4096
qk 44,160,32
s_and_b32 s50,s68,1
qg 136,4,18432
s_lshl_b32 s50,s50,6
ql 44,164,36
q0 7
s_lshl_b32 s51,s31,7
s_add_u32 s90,s50,s51
ql 44,168,40
s_lshl_b32 s93,s83,7
qg 140,4,19456
s_add_u32 s93,s93,262144
ql 44,172,44
q0 6
qc 3,0
s_mul_i32 s86,s83,17408
ql 44,176,48
s_add_u32 s86,s86,55713792
qg 144,4,20480
s_sub_u32 s48,s93,65536
ql 44,180,52
q0 5
v_max_f32 v121,v121,v39
v_max3_f32 v120,v32,v33,v34
ql 44,184,56
qg 148,4,21504
v_max_f32 v120,v120,v35
ql 44,188,60
qc 3,1024
qk 40,160,0
qg 152,4,22528
ql 40,164,4
s_cmp_eq_u32 s3,0
s_cselect_b32 s93,s48,s93
s_cselect_b32 s86,1073741824,s86
ql 40,168,8
qg 156,4,23552
ql 40,172,12
qc 3,2048
ql 40,176,16
qg 160,4,24576
v_max3_f32 v121,v121,v44,v45
ql 40,180,20
v_max3_f32 v121,v121,v46,v47
ql 40,184,24
qg 164,4,25600
ql 40,188,28
q0 8
qc 3,3072
s_add_u32 s72,s72,1
qk 52,128,32
qg 168,4,26624
v_readlane_b32 s73,v5,s72
ql 52,132,36
q0 7
v_readlane_b32 s74,v6,s72
v_readlane_b32 s75,v7,s72
ql 52,136,40
qg 172,4,27648
ql 52,140,44
q0 6
ql 52,144,48
qg 176,4,28672
v_max3_f32 v120,v120,v40,v41
ql 52,148,52
q0 5
v_max3_f32 v120,v120,v42,v43
ql 52,152,56
qg 180,4,29696
ql 52,156,60
qk 48,128,0
qg 184,4,30720
ql 48,132,4
ql 48,136,8
qg 188,4,31744
ql 48,140,12
q3 19
s_barrier
v_add_u32 v4,s75,v2
ql 48,144,16
qg 128,4,0
v_max3_f32 v121,v121,v52,v53
ql 48,148,20
v_max3_f32 v121,v121,v54,v55
ql 48,152,24
qg 132,4,1024
ql 48,156,28
q0 8
qk 60,160,32
qg 136,4,2048
ql 60,164,36
q0 7
ql 60,168,40
qg 140,4,3072
ql 60,172,44
q0 6
ql 60,176,48
qg 144,4,4096
v_max3_f32 v120,v120,v48,v49
ql 60,180,52
q0 5
v_max3_f32 v120,v120,v50,v51
ql 60,184,56
qg 148,4,5120
ql 60,188,60
qk 56,160,0
qg 152,4,6144
ql 56,164,4
ql 56,168,8
qg 156,4,7168
ql 56,172,12
ql 56,176,16
qg 160,4,8192
v_max3_f32 v121,v121,v60,v61
ql 56,180,20
v_max3_f32 v121,v121,v62,v63
ql 56,184,24
qg 164,4,9216
ql 56,188,28
s_nop 7
v_max3_f32 v120,v120,v56,v57
v_max3_f32 v120,v120,v58,v59
v_mov_b32 v122,v120
v_mov_b32 v123,v121
s_nop 0
v_permlane32_swap_b32 v120,v122
v_permlane32_swap_b32 v121,v123
s_nop 0
v_max_f32 v120,v120,v122
v_max_f32 v121,v121,v123
v_mov_b32 v122,v120
v_mov_b32 v123,v121
s_nop 0
v_permlane16_swap_b32 v120,v122
v_permlane16_swap_b32 v121,v123
s_nop 0
v_max_f32 v120,v120,v122
v_max_f32 v121,v121,v123
v_mov_b32 v12,v120
v_mov_b32 v8,0
v_sub_f32 v112,0,v120
v_sub_f32 v113,0,v120
v_sub_f32 v114,0,v120
v_sub_f32 v115,0,v120
v_mov_b32 v13,v121
v_mov_b32 v9,0
v_sub_f32 v116,0,v121
v_sub_f32 v117,0,v121
v_sub_f32 v118,0,v121
v_sub_f32 v119,0,v121
v_pk_add_f32 v[32:33],v[32:33],v[112:113]
v_pk_add_f32 v[34:35],v[34:35],v[112:113]
v_pk_add_f32 v[36:37],v[36:37],v[116:117]
v_pk_add_f32 v[38:39],v[38:39],v[116:117]
v_pk_add_f32 v[40:41],v[40:41],v[112:113]
v_pk_add_f32 v[42:43],v[42:43],v[112:113]
v_pk_add_f32 v[44:45],v[44:45],v[116:117]
v_pk_add_f32 v[46:47],v[46:47],v[116:117]
v_pk_add_f32 v[48:49],v[48:49],v[112:113]
v_pk_add_f32 v[50:51],v[50:51],v[112:113]
v_pk_add_f32 v[52:53],v[52:53],v[116:117]
v_pk_add_f32 v[54:55],v[54:55],v[116:117]
v_pk_add_f32 v[56:57],v[56:57],v[112:113]
v_pk_add_f32 v[58:59],v[58:59],v[112:113]
v_pk_add_f32 v[60:61],v[60:61],v[116:117]
v_pk_add_f32 v[62:63],v[62:63],v[116:117]
qm 64,32
qm 65,33
qm 66,34
qm 67,35
qm 68,36
qm 69,37
qm 70,38
qm 71,39
qm 72,40
qm 73,41
qm 74,42
qm 75,43
qm 76,44
qm 77,45
qm 78,46
qm 79,47
qm 80,48
qm 81,49
qm 82,50
qm 83,51
qm 84,52
qm 85,53
qm 86,54
qm 87,55
qm 88,56
qm 89,57
qm 90,58
qm 91,59
qm 92,60
qm 93,61
qm 94,62
qm 95,63
v_pk_add_f32 v[120:121],v[64:65],v[66:67]
v_pk_add_f32 v[122:123],v[68:69],v[70:71]
v_pk_add_f32 v[120:121],v[120:121],v[72:73]
v_pk_add_f32 v[122:123],v[122:123],v[76:77]
v_pk_add_f32 v[120:121],v[120:121],v[74:75]
v_pk_add_f32 v[122:123],v[122:123],v[78:79]
v_pk_add_f32 v[120:121],v[120:121],v[80:81]
v_pk_add_f32 v[122:123],v[122:123],v[84:85]
v_pk_add_f32 v[120:121],v[120:121],v[82:83]
v_pk_add_f32 v[122:123],v[122:123],v[86:87]
v_pk_add_f32 v[120:121],v[120:121],v[88:89]
v_pk_add_f32 v[122:123],v[122:123],v[92:93]
v_pk_add_f32 v[120:121],v[120:121],v[90:91]
v_pk_add_f32 v[122:123],v[122:123],v[94:95]
q9 10,120,121
q9 11,122,123
q0 8
qn 32,128,0,112
s_mov_b32 m0,s74
qg 168,4,10240
q9 8,8,10
ql 32,132,4
q0 7
qc 2,0
q9 9,9,11
ql 32,136,8
qg 172,4,11264
qb 96,64,65
ql 32,140,12
q0 6
qb 97,66,67
qb 98,72,73
ql 32,144,16
qg 176,4,12288
qb 99,74,75
ql 32,148,20
q0 5
qc 2,1024
qb 100,68,69
ql 32,152,24
qg 180,4,13312
qb 101,70,71
ql 32,156,28
buffer_store_dwordx4 a[64:67],v2,s[32:35],s80 offen offset:0 sc1
qb 102,76,77
qn 36,128,32,116
qg 184,4,14336
qb 103,78,79
ql 36,132,36
qc 2,2048
qb 104,80,81
ql 36,136,40
qg 188,4,15360
qb 105,82,83
ql 36,140,44
qb 106,88,89
qb 107,90,91
ql 36,144,48
qg 128,4,16384
qb 108,84,85
ql 36,148,52
qb 109,86,87
qb 110,92,93
ql 36,152,56
qg 132,4,17408
qb 111,94,95
ql 36,156,60
q0 8
qc 2,3072
s_add_u32 m0,s74,4096
qn 40,160,0,112
qg 136,4,18432
ql 40,164,4
q0 7
qm 64,32
ql 40,168,8
qg 140,4,19456
ql 40,172,12
q0 6
qc 3,0
ql 40,176,16
qg 144,4,20480
v_and_b32 v16,15,v1
ql 40,180,20
q0 5
)"
R"(buffer_store_dwordx4 a[68:71],v2,s[32:35],s80 offen offset:1024 sc1
v_lshrrev_b32 v17,4,v1
ql 40,184,24
qg 148,4,21504
v_lshlrev_b32 v17,2,v17
ql 40,188,28
s_lshl_b32 s48,s31,4
qc 3,1024
v_sub_u32 v16,v16,v17
qn 44,160,32,116
s_sub_u32 s49,48,s48
qg 152,4,22528
v_add_u32 v14,s48,v16
ql 44,164,36
qm 65,33
ql 44,168,40
qg 156,4,23552
v_add_u32 v15,s49,v16
ql 44,172,44
qc 3,2048
q9 10,64,65
ql 44,176,48
qg 160,4,24576
ql 44,180,52
qm 66,34
ql 44,184,56
qg 164,4,25600
q9 10,10,66
ql 44,188,60
q0 8
qc 3,3072
s_add_u32 s72,s72,1
qn 48,128,0,112
qg 168,4,26624
v_readlane_b32 s73,v5,s72
ql 48,132,4
q0 7
qm 67,35
ql 48,136,8
qg 172,4,27648
v_readlane_b32 s74,v6,s72
ql 48,140,12
q0 6
buffer_store_dwordx4 a[72:75],v2,s[32:35],s80 offen offset:2048 sc1
q9 10,10,67
ql 48,144,16
qg 176,4,28672
v_readlane_b32 s75,v7,s72
ql 48,148,20
q0 5
qm 68,36
ql 48,152,24
qg 180,4,29696
ql 48,156,28
qm 69,37
qn 52,128,32,116
qg 184,4,30720
q9 11,68,69
ql 52,132,36
qm 70,38
ql 52,136,40
qg 188,4,31744
q9 11,11,70
ql 52,140,44
q3 19
s_barrier
v_add_u32 v4,s75,v2
ql 52,144,48
qg 128,4,0
ql 52,148,52
qm 71,39
ql 52,152,56
qg 132,4,1024
q9 11,11,71
ql 52,156,60
q0 8
qm 72,40
qn 56,160,0,112
qg 136,4,2048
q9 10,10,72
ql 56,164,4
q0 7
buffer_store_dwordx4 a[76:79],v2,s[32:35],s80 offen offset:3072 sc1
s_add_u32 s80,s80,16384
ql 56,168,8
qg 140,4,3072
ql 56,172,12
q0 6
qm 73,41
ql 56,176,16
qg 144,4,4096
q9 10,10,73
ql 56,180,20
q0 5
qm 74,42
ql 56,184,24
qg 148,4,5120
q9 10,10,74
ql 56,188,28
qm 75,43
qn 60,160,32,116
qg 152,4,6144
q9 10,10,75
ql 60,164,36
qm 76,44
ql 60,168,40
qg 156,4,7168
q9 11,11,76
ql 60,172,44
qm 77,45
ql 60,176,48
qg 160,4,8192
q9 11,11,77
ql 60,180,52
qm 78,46
ql 60,184,56
qg 164,4,9216
q9 11,11,78
ql 60,188,60
q0 8
buffer_store_dwordx4 a[80:83],v2,s[32:35],s80 offen offset:0 sc1
qh 128,128,96
s_mov_b32 m0,s74
qg 168,4,10240
qi 128,132,104
qc 2,0
qh 192,132,108
qg 172,4,11264
qi 192,128,100
q0 8
qm 79,47
qh 196,136,100
qg 176,4,12288
q9 11,11,79
qi 196,140,108
qc 2,1024
qh 132,140,104
qg 180,4,13312
qi 132,136,96
q0 8
qm 80,48
qh 136,144,96
qg 184,4,14336
q9 10,10,80
qi 136,148,104
qc 2,2048
qh 200,148,108
qg 188,4,15360
qi 200,144,100
q0 8
qm 81,49
qh 204,152,100
qg 128,4,16384
q9 10,10,81
qi 204,156,108
buffer_store_dwordx4 a[84:87],v2,s[32:35],s80 offen offset:1024 sc1
qh 140,156,104
qg 132,4,17408
qi 140,152,96
q0 8
qc 2,3072
s_add_u32 m0,s74,4096
qh 144,160,96
qg 136,4,18432
qi 144,164,104
qm 82,50
qh 208,164,108
qg 140,4,19456
q9 10,10,82
qi 208,160,100
q0 8
qc 3,0
qh 212,168,100
qg 144,4,20480
qi 212,172,108
qm 83,51
qh 148,172,104
qg 148,4,21504
q9 10,10,83
qi 148,168,96
q0 8
qc 3,1024
qh 152,176,96
qg 152,4,22528
qi 152,180,104
qm 84,52
qh 216,180,108
qg 156,4,23552
q9 11,11,84
qi 216,176,100
q0 8
qc 3,2048
qh 220,184,100
qg 160,4,24576
qi 220,188,108
buffer_store_dwordx4 a[88:91],v2,s[32:35],s80 offen offset:2048 sc1
qh 156,188,104
qg 164,4,25600
qi 156,184,96
q0 8
qc 3,3072
s_add_u32 s72,s72,1
qh 160,128,96
qg 168,4,26624
v_readlane_b32 s73,v5,s72
qi 160,132,104
qm 85,53
qh 224,132,108
qg 172,4,27648
v_readlane_b32 s74,v6,s72
qi 224,128,100
q0 8
qm 86,54
qh 228,136,100
qg 176,4,28672
v_readlane_b32 s75,v7,s72
qi 228,140,108
qm 87,55
qh 164,140,104
qg 180,4,29696
q9 11,11,85
qi 164,136,96
q0 8
qm 88,56
qh 168,144,96
qg 184,4,30720
q9 11,11,86
qi 168,148,104
qm 89,57
qh 232,148,108
qg 188,4,31744
q9 11,11,87
qi 232,144,100
s_waitcnt vmcnt(23) lgkmcnt(8)
s_barrier
v_add_u32 v4,s75,v2
qh 236,152,100
qg 128,4,0
q9 10,10,88
qi 236,156,108
qm 90,58
qh 172,156,104
qg 132,4,1024
q9 10,10,89
qi 172,152,96
q0 8
qm 91,59
qh 176,160,96
LHH3_1kf:
s_cbranch_execz LHR3kf
qg 136,4,2048
q9 10,10,90
qi 176,164,104
qm 92,60
qh 240,164,108
qg 140,4,3072
q9 10,10,91
qi 240,160,100
q0 8
qm 93,61
qh 244,168,100
qg 144,4,4096
q9 11,11,92
qi 244,172,108
qm 94,62
qh 180,172,104
qg 148,4,5120
q9 11,11,93
qi 180,168,96
q0 8
qm 95,63
qh 184,176,96
qg 152,4,6144
q9 11,11,94
qi 184,180,104
q9 11,11,95
qh 248,180,108
qg 156,4,7168
q9 120,10,11
qi 248,176,100
q0 8
v_cmp_nle_f32 vcc,v120,v127
qh 252,184,100
qg 160,4,8192
qi 252,188,108
qh 188,188,104
qg 164,4,9216
qi 188,184,96
s_cbranch_vccnz Lag13kf_rs_nb
Lag13kf_ok_nb:
q0 8
qn 32,128,0,112
s_mov_b32 m0,s74
qg 168,4,10240
q9 8,8,10
ql 32,132,4
q0 7
qc 2,0
q9 9,9,11
ql 32,136,8
qg 172,4,11264
qb 96,64,65
ql 32,140,12
q0 6
qb 97,66,67
qb 98,72,73
ql 32,144,16
qg 176,4,12288
qb 99,74,75
ql 32,148,20
q0 5
qc 2,1024
qb 100,68,69
ql 32,152,24
qg 180,4,13312
qb 101,70,71
ql 32,156,28
buffer_store_dwordx4 a[92:95],v2,s[32:35],s80 offen offset:3072 sc1
s_add_u32 s80,s80,16384
qn 36,128,32,116
qg 184,4,14336
qb 102,76,77
ql 36,132,36
qc 2,2048
qb 103,78,79
ql 36,136,40
qg 188,4,15360
qb 104,80,81
ql 36,140,44
qb 105,82,83
qb 106,88,89
ql 36,144,48
qg 128,4,16384
qb 107,90,91
ql 36,148,52
qb 108,84,85
qb 109,86,87
ql 36,152,56
qg 132,4,17408
qb 110,92,93
ql 36,156,60
q0 8
qc 2,3072
s_add_u32 m0,s74,4096
qn 40,160,0,112
qg 136,4,18432
qb 111,94,95
ql 40,164,4
q0 7
qm 64,32
ql 40,168,8
qg 140,4,19456
ql 40,172,12
q0 6
qc 3,0
ql 40,176,16
qg 144,4,20480
ql 40,180,20
q0 5
buffer_store_dwordx4 a[96:99],v2,s[32:35],s80 offen offset:0 sc1
ql 40,184,24
qg 148,4,21504
ql 40,188,28
qc 3,1024
qn 44,160,32,116
qg 152,4,22528
ql 44,164,36
qm 65,33
ql 44,168,40
qg 156,4,23552
q9 10,64,65
ql 44,172,44
qc 3,2048
ql 44,176,48
qg 160,4,24576
ql 44,180,52
qm 66,34
ql 44,184,56
qg 164,4,25600
q9 10,10,66
ql 44,188,60
q0 8
qc 3,3072
s_add_u32 s72,s72,1
qn 48,128,0,112
qg 168,4,26624
v_readlane_b32 s73,v5,s72
ql 48,132,4
q0 7
qm 67,35
ql 48,136,8
qg 172,4,27648
v_readlane_b32 s74,v6,s72
ql 48,140,12
q0 6
buffer_store_dwordx4 a[100:103],v2,s[32:35],s80 offen offset:1024 sc1
q9 10,10,67
ql 48,144,16
qg 176,4,28672
v_readlane_b32 s75,v7,s72
ql 48,148,20
q0 5
qm 68,36
ql 48,152,24
qg 180,4,29696
ql 48,156,28
qm 69,37
qn 52,128,32,116
qg 184,4,30720
q9 11,68,69
ql 52,132,36
qm 70,38
ql 52,136,40
qg 188,4,31744
q9 11,11,70
ql 52,140,44
q3 24
s_barrier
v_add_u32 v4,s75,v2
ql 52,144,48
qg 128,4,0
ql 52,148,52
qm 71,39
ql 52,152,56
qg 132,4,1024
q9 11,11,71
ql 52,156,60
q0 8
qm 72,40
qn 56,160,0,112
qg 136,4,2048
q9 10,10,72
ql 56,164,4
q0 7
buffer_store_dwordx4 a[104:107],v2,s[32:35],s80 offen offset:2048 sc1
ql 56,168,8
qg 140,4,3072
ql 56,172,12
q0 6
qm 73,41
ql 56,176,16
qg 144,4,4096
q9 10,10,73
ql 56,180,20
q0 5
qm 74,42
ql 56,184,24
qg 148,4,5120
q9 10,10,74
ql 56,188,28
qm 75,43
qn 60,160,32,116
qg 152,4,6144
q9 10,10,75
ql 60,164,36
qm 76,44
ql 60,168,40
qg 156,4,7168
q9 11,11,76
ql 60,172,44
qm 77,45
ql 60,176,48
qg 160,4,8192
q9 11,11,77
ql 60,180,52
qm 78,46
ql 60,184,56
qg 164,4,9216
q9 11,11,78
ql 60,188,60
q0 8
buffer_store_dwordx4 a[108:111],v2,s[32:35],s80 offen offset:3072 sc1
s_add_u32 s80,s80,16384
qi 128,128,96
s_mov_b32 m0,s74
qg 168,4,10240
qi 128,132,104
qc 2,0
qi 192,132,108
qg 172,4,11264
qi 192,128,100
q0 8
qm 79,47
qi 196,136,100
qg 176,4,12288
q9 11,11,79
qi 196,140,108
qc 2,1024
qi 132,140,104
qg 180,4,13312
qi 132,136,96
q0 8
qm 80,48
qi 136,144,96
qg 184,4,14336
q9 10,10,80
qi 136,148,104
qc 2,2048
qi 200,148,108
qg 188,4,15360
qi 200,144,100
q0 8
qm 81,49
qi 204,152,100
qg 128,4,16384
q9 10,10,81
qi 204,156,108
buffer_store_dwordx4 a[112:115],v2,s[32:35],s80 offen offset:0 sc1
qi 140,156,104
qg 132,4,17408
qi 140,152,96
q0 8
qc 2,3072
s_add_u32 m0,s74,4096
qi 144,160,96
qg 136,4,18432
qi 144,164,104
qm 82,50
qi 208,164,108
qg 140,4,19456
q9 10,10,82
qi 208,160,100
q0 8
qc 3,0
qi 212,168,100
qg 144,4,20480
qi 212,172,108
qm 83,51
qi 148,172,104
qg 148,4,21504
q9 10,10,83
qi 148,168,96
q0 8
qc 3,1024
qi 152,176,96
qg 152,4,22528
qi 152,180,104
qm 84,52
qi 216,180,108
qg 156,4,23552
q9 11,11,84
qi 216,176,100
q0 8
qc 3,2048
qi 220,184,100
qg 160,4,24576
qi 220,188,108
buffer_store_dwordx4 a[116:119],v2,s[32:35],s80 offen offset:1024 sc1
qi 156,188,104
qg 164,4,25600
qi 156,184,96
q0 8
qc 3,3072
s_add_u32 s72,s72,1
qi 160,128,96
qg 168,4,26624
v_readlane_b32 s73,v5,s72
qi 160,132,104
qm 85,53
qi 224,132,108
qg 172,4,27648
v_readlane_b32 s74,v6,s72
qi 224,128,100
q0 8
qm 86,54
qi 228,136,100
qg 176,4,28672
v_readlane_b32 s75,v7,s72
qi 228,140,108
qm 87,55
qi 164,140,104
qg 180,4,29696
q9 11,11,85
qi 164,136,96
q0 8
qm 88,56
qi 168,144,96
qg 184,4,30720
q9 11,11,86
qi 168,148,104
qm 89,57
qi 232,148,108
qg 188,4,31744
q9 11,11,87
qi 232,144,100
s_waitcnt vmcnt(23) lgkmcnt(8)
s_barrier
v_add_u32 v4,s75,v2
qi 236,152,100
qg 128,4,0
q9 10,10,88
qi 236,156,108
qm 90,58
qi 172,156,104
qg 132,4,1024
q9 10,10,89
qi 172,152,96
q0 8
qm 91,59
qi 176,160,96
qg 136,4,2048
q9 10,10,90
qi 176,164,104
qm 92,60
qi 240,164,108
qg 140,4,3072
q9 10,10,91
qi 240,160,100
q0 8
qm 93,61
qi 244,168,100
qg 144,4,4096
q9 11,11,92
qi 244,172,108
qm 94,62
qi 180,172,104
qg 148,4,5120
q9 11,11,93
qi 180,168,96
q0 8
qm 95,63
qi 184,176,96
qg 152,4,6144
q9 11,11,94
qi 184,180,104
q9 11,11,95
qi 248,180,108
qg 156,4,7168
q9 120,10,11
qi 248,176,100
q0 8
v_cmp_nle_f32 vcc,v120,v127
qi 252,184,100
qg 160,4,8192
qi 252,188,108
qi 188,188,104
qg 164,4,9216
qi 188,184,96
s_cbranch_vccnz Lag14kf_rs_nb
Lag14kf_ok_nb:
q0 8
qn 32,128,0,112
s_mov_b32 m0,s74
qg 168,4,10240
q9 8,8,10
ql 32,132,4
q0 7
qc 2,0
q9 9,9,11
ql 32,136,8
qg 172,4,11264
qb 96,64,65
ql 32,140,12
q0 6
qb 97,66,67
qb 98,72,73
ql 32,144,16
qg 176,4,12288
qb 99,74,75
ql 32,148,20
q0 5
qc 2,1024
qb 100,68,69
ql 32,152,24
qg 180,4,13312
qb 101,70,71
ql 32,156,28
buffer_store_dwordx4 a[120:123],v2,s[32:35],s80 offen offset:2048 sc1
qb 102,76,77
qn 36,128,32,116
qg 184,4,14336
qb 103,78,79
ql 36,132,36
qc 2,2048
qb 104,80,81
ql 36,136,40
qg 188,4,15360
qb 105,82,83
ql 36,140,44
qb 106,88,89
qb 107,90,91
ql 36,144,48
qg 128,4,16384
qb 108,84,85
ql 36,148,52
qb 109,86,87
qb 110,92,93
ql 36,152,56
qg 132,4,17408
qb 111,94,95
ql 36,156,60
q0 8
qc 2,3072
s_add_u32 m0,s74,4096
qn 40,160,0,112
qg 136,4,18432
ql 40,164,4
q0 7
qm 64,32
ql 40,168,8
qg 140,4,19456
ql 40,172,12
q0 6
qc 3,0
ql 40,176,16
qg 144,4,20480
ql 40,180,20
q0 5
buffer_store_dwordx4 a[124:127],v2,s[32:35],s80 offen offset:3072 sc1
s_add_u32 s80,s80,16384
ql 40,184,24
qg 148,4,21504
ql 40,188,28
qc 3,1024
qn 44,160,32,116
qg 152,4,22528
ql 44,164,36
qm 65,33
ql 44,168,40
qg 156,4,23552
q9 10,64,65
ql 44,172,44
qc 3,2048
ql 44,176,48
qg 160,4,24576
ql 44,180,52
qm 66,34
ql 44,184,56
qg 164,4,25600
q9 10,10,66
ql 44,188,60
q0 8
qc 3,3072
s_add_u32 s72,s72,1
qn 48,128,0,112
qg 168,4,26624
v_readlane_b32 s73,v5,s72
ql 48,132,4
q0 7
qm 67,35
ql 48,136,8
qg 172,4,27648
v_readlane_b32 s74,v6,s72
ql 48,140,12
q0 6
buffer_store_dwordx4 a[192:195],v2,s[32:35],s80 offen offset:0 sc1
q9 10,10,67
ql 48,144,16
qg 176,4,28672
v_readlane_b32 s75,v7,s72
ql 48,148,20
q0 5
qm 68,36
ql 48,152,24
qg 180,4,29696
ql 48,156,28
qm 69,37
qn 52,128,32,116
qg 184,4,30720
q9 11,68,69
ql 52,132,36
qm 70,38
ql 52,136,40
qg 188,4,31744
q9 11,11,70
ql 52,140,44
q3 24
s_barrier
v_add_u32 v4,s75,v2
ql 52,144,48
qg 128,4,0
ql 52,148,52
qm 71,39
ql 52,152,56
qg 132,4,1024
q9 11,11,71
ql 52,156,60
q0 8
qm 72,40
qn 56,160,0,112
qg 136,4,2048
q9 10,10,72
ql 56,164,4
q0 7
buffer_store_dwordx4 a[196:199],v2,s[32:35],s80 offen offset:1024 sc1
ql 56,168,8
qg 140,4,3072
ql 56,172,12
q0 6
qm 73,41
ql 56,176,16
qg 144,4,4096
q9 10,10,73
ql 56,180,20
q0 5
qm 74,42
ql 56,184,24
qg 148,4,5120
q9 10,10,74
ql 56,188,28
qm 75,43
qn 60,160,32,116
qg 152,4,6144
q9 10,10,75
ql 60,164,36
qm 76,44
ql 60,168,40
qg 156,4,7168
q9 11,11,76
ql 60,172,44
qm 77,45
ql 60,176,48
qg 160,4,8192
q9 11,11,77
ql 60,180,52
qm 78,46
ql 60,184,56
qg 164,4,9216
q9 11,11,78
ql 60,188,60
q0 8
buffer_store_dwordx4 a[200:203],v2,s[32:35],s80 offen offset:2048 sc1
qi 128,128,96
s_mov_b32 m0,s74
qg 168,4,10240
qi 128,132,104
qc 2,0
qi 192,132,108
qg 172,4,11264
qi 192,128,100
q0 8
qm 79,47
qi 196,136,100
qg 176,4,12288
q9 11,11,79
qi 196,140,108
qc 2,1024
qi 132,140,104
qg 180,4,13312
qi 132,136,96
q0 8
qm 80,48
qi 136,144,96
qg 184,4,14336
q9 10,10,80
qi 136,148,104
qc 2,2048
qi 200,148,108
qg 188,4,15360
qi 200,144,100
q0 8
qm 81,49
qi 204,152,100
qg 128,4,16384
q9 10,10,81
qi 204,156,108
LHH3_2kf:
s_cbranch_execz LHR3kf
buffer_store_dwordx4 a[204:207],v2,s[32:35],s80 offen offset:3072 sc1
s_add_u32 s80,s80,16384
qi 140,156,104
qg 132,4,17408
qi 140,152,96
q0 8
qc 2,3072
s_add_u32 m0,s74,4096
qi 144,160,96
qg 136,4,18432
qi 144,164,104
qm 82,50
qi 208,164,108
qg 140,4,19456
q9 10,10,82
qi 208,160,100
q0 8
qc 3,0
qi 212,168,100
qg 144,4,20480
qi 212,172,108
qm 83,51
qi 148,172,104
qg 148,4,21504
q9 10,10,83
qi 148,168,96
q0 8
qc 3,1024
qi 152,176,96
qg 152,4,22528
qi 152,180,104
qm 84,52
qi 216,180,108
qg 156,4,23552
q9 11,11,84
qi 216,176,100
q0 8
qc 3,2048
qi 220,184,100
qg 160,4,24576
qi 220,188,108
buffer_store_dwordx4 a[208:211],v2,s[32:35],s80 offen offset:0 sc1
qi 156,188,104
qg 164,4,25600
qi 156,184,96
q0 8
qc 3,3072
s_add_u32 s72,s72,1
qi 160,128,96
qg 168,4,26624
v_readlane_b32 s73,v5,s72
qi 160,132,104
qm 85,53
qi 224,132,108
qg 172,4,27648
v_readlane_b32 s74,v6,s72
qi 224,128,100
q0 8
qm 86,54
qi 228,136,100
qg 176,4,28672
v_readlane_b32 s75,v7,s72
qi 228,140,108
qm 87,55
qi 164,140,104
qg 180,4,29696
q9 11,11,85
qi 164,136,96
q0 8
qm 88,56
qi 168,144,96
qg 184,4,30720
q9 11,11,86
qi 168,148,104
qm 89,57
qi 232,148,108
qg 188,4,31744
q9 11,11,87
qi 232,144,100
s_waitcnt vmcnt(23) lgkmcnt(8)
s_barrier
v_add_u32 v4,s75,v2
qi 236,152,100
qg 128,4,0
q9 10,10,88
qi 236,156,108
qm 90,58
qi 172,156,104
qg 132,4,1024
q9 10,10,89
qi 172,152,96
q0 8
qm 91,59
qi 176,160,96
qg 136,4,2048
q9 10,10,90
qi 176,164,104
qm 92,60
qi 240,164,108
qg 140,4,3072
q9 10,10,91
qi 240,160,100
q0 8
qm 93,61
qi 244,168,100
qg 144,4,4096
q9 11,11,92
qi 244,172,108
qm 94,62
qi 180,172,104
qg 148,4,5120
q9 11,11,93
qi 180,168,96
q0 8
qm 95,63
qi 184,176,96
qg 152,4,6144
q9 11,11,94
qi 184,180,104
q9 11,11,95
qi 248,180,108
qg 156,4,7168
q9 120,10,11
qi 248,176,100
q0 8
v_cmp_nle_f32 vcc,v120,v127
qi 252,184,100
qg 160,4,8192
qi 252,188,108
qi 188,188,104
qg 164,4,9216
qi 188,184,96
s_cbranch_vccnz Lag15kf_rs_nb
Lag15kf_ok_nb:
q0 8
qn 32,128,0,112
s_mov_b32 m0,s74
qg 168,4,10240
q9 8,8,10
ql 32,132,4
q0 7
qc 2,0
q9 9,9,11
ql 32,136,8
qg 172,4,11264
qb 96,64,65
ql 32,140,12
q0 6
qb 97,66,67
qb 98,72,73
ql 32,144,16
qg 176,4,12288
qb 99,74,75
ql 32,148,20
q0 5
qc 2,1024
qb 100,68,69
ql 32,152,24
qg 180,4,13312
qb 101,70,71
ql 32,156,28
buffer_store_dwordx4 a[212:215],v2,s[32:35],s80 offen offset:1024 sc1
qb 102,76,77
qn 36,128,32,116
qg 184,4,14336
qb 103,78,79
ql 36,132,36
qc 2,2048
qb 104,80,81
ql 36,136,40
qg 188,4,15360
qb 105,82,83
ql 36,140,44
qb 106,88,89
qb 107,90,91
ql 36,144,48
qg 128,4,16384
qb 108,84,85
ql 36,148,52
qb 109,86,87
qb 110,92,93
ql 36,152,56
qg 132,4,17408
qb 111,94,95
ql 36,156,60
q0 8
qc 2,3072
s_add_u32 m0,s74,4096
qn 40,160,0,112
qg 136,4,18432
ql 40,164,4
q0 7
qm 64,32
ql 40,168,8
qg 140,4,19456
ql 40,172,12
q0 6
qc 3,0
ql 40,176,16
qg 144,4,20480
ql 40,180,20
q0 5
buffer_store_dwordx4 a[216:219],v2,s[32:35],s80 offen offset:2048 sc1
ql 40,184,24
qg 148,4,21504
ql 40,188,28
qc 3,1024
qn 44,160,32,116
qg 152,4,22528
ql 44,164,36
qm 65,33
ql 44,168,40
qg 156,4,23552
q9 10,64,65
ql 44,172,44
qc 3,2048
ql 44,176,48
qg 160,4,24576
ql 44,180,52
qm 66,34
ql 44,184,56
qg 164,4,25600
q9 10,10,66
ql 44,188,60
q0 8
qc 3,3072
s_add_u32 s72,s72,1
qn 48,128,0,112
qg 168,4,26624
v_readlane_b32 s73,v5,s72
ql 48,132,4
q0 7
qm 67,35
ql 48,136,8
qg 172,4,27648
v_readlane_b32 s74,v6,s72
ql 48,140,12
q0 6
buffer_store_dwordx4 a[220:223],v2,s[32:35],s80 offen offset:3072 sc1
s_add_u32 s80,s80,16384
ql 48,144,16
qg 176,4,28672
v_readlane_b32 s75,v7,s72
ql 48,148,20
q0 5
qm 68,36
ql 48,152,24
qg 180,4,29696
q9 10,10,67
ql 48,156,28
qm 69,37
qn 52,128,32,116
qg 184,4,30720
q9 11,68,69
ql 52,132,36
qm 70,38
ql 52,136,40
qg 188,4,31744
q9 11,11,70
ql 52,140,44
q3 24
s_barrier
v_add_u32 v4,s75,v2
ql 52,144,48
qg 128,4,0
ql 52,148,52
qm 71,39
ql 52,152,56
qg 132,4,1024
q9 11,11,71
ql 52,156,60
q0 8
qm 72,40
qn 56,160,0,112
qg 136,4,2048
q9 10,10,72
ql 56,164,4
q0 7
buffer_store_dwordx4 a[224:227],v2,s[32:35],s80 offen offset:0 sc1
ql 56,168,8
qg 140,4,3072
ql 56,172,12
q0 6
qm 73,41
ql 56,176,16
qg 144,4,4096
q9 10,10,73
ql 56,180,20
q0 5
qm 74,42
ql 56,184,24
qg 148,4,5120
q9 10,10,74
ql 56,188,28
qm 75,43
qn 60,160,32,116
qg 152,4,6144
q9 10,10,75
ql 60,164,36
qm 76,44
ql 60,168,40
qg 156,4,7168
q9 11,11,76
ql 60,172,44
qm 77,45
ql 60,176,48
qg 160,4,8192
q9 11,11,77
ql 60,180,52
qm 78,46
ql 60,184,56
qg 164,4,9216
q9 11,11,78
ql 60,188,60
q0 8
buffer_store_dwordx4 a[228:231],v2,s[32:35],s80 offen offset:1024 sc1
qi 128,128,96
s_mov_b32 m0,s74
qg 168,4,10240
qi 128,132,104
qc 2,0
qi 192,132,108
qg 172,4,11264
qi 192,128,100
q0 8
qm 79,47
qi 196,136,100
qg 176,4,12288
q9 11,11,79
qi 196,140,108
qc 2,1024
qi 132,140,104
qg 180,4,13312
qi 132,136,96
q0 8
qm 80,48
qi 136,144,96
qg 184,4,14336
q9 10,10,80
qi 136,148,104
qc 2,2048
qi 200,148,108
qg 188,4,15360
qi 200,144,100
q0 8
qm 81,49
qi 204,152,100
qg 128,4,16384
q9 10,10,81
qi 204,156,108
buffer_store_dwordx4 a[232:235],v2,s[32:35],s80 offen offset:2048 sc1
qi 140,156,104
qg 132,4,17408
qi 140,152,96
q0 8
qc 2,3072
s_add_u32 m0,s74,4096
qi 144,160,96
qg 136,4,18432
qi 144,164,104
qm 82,50
qi 208,164,108
qg 140,4,19456
q9 10,10,82
qi 208,160,100
q0 8
qc 3,0
qi 212,168,100
qg 144,4,20480
qi 212,172,108
qm 83,51
qi 148,172,104
qg 148,4,21504
q9 10,10,83
qi 148,168,96
q0 8
qc 3,1024
qi 152,176,96
qg 152,4,22528
qi 152,180,104
qm 84,52
qi 216,180,108
qg 156,4,23552
q9 11,11,84
qi 216,176,100
q0 8
qc 3,2048
qi 220,184,100
qg 160,4,24576
qi 220,188,108
buffer_store_dwordx4 a[236:239],v2,s[32:35],s80 offen offset:3072 sc1
s_add_u32 s80,s80,16384
qi 156,188,104
qg 164,4,25600
qi 156,184,96
q0 8
qc 3,3072
s_add_u32 s72,s72,1
qi 160,128,96
qg 168,4,26624
v_readlane_b32 s73,v5,s72
qi 160,132,104
qm 85,53
qi 224,132,108
qg 172,4,27648
v_readlane_b32 s74,v6,s72
qi 224,128,100
q0 8
qm 86,54
qi 228,136,100
qg 176,4,28672
v_readlane_b32 s75,v7,s72
qi 228,140,108
qm 87,55
qi 164,140,104
qg 180,4,29696
q9 11,11,85
qi 164,136,96
q0 8
qm 88,56
qi 168,144,96
qg 184,4,30720
q9 11,11,86
qi 168,148,104
qm 89,57
qi 232,148,108
qg 188,4,31744
q9 11,11,87
qi 232,144,100
s_waitcnt vmcnt(23) lgkmcnt(8)
s_barrier
v_add_u32 v4,s75,v2
qi 236,152,100
qg 128,4,0
q9 10,10,88
qi 236,156,108
qm 90,58
qi 172,156,104
qg 132,4,1024
q9 10,10,89
qi 172,152,96
q0 8
qm 91,59
qi 176,160,96
qg 136,4,2048
q9 10,10,90
qi 176,164,104
qm 92,60
qi 240,164,108
qg 140,4,3072
q9 10,10,91
qi 240,160,100
q0 8
qm 93,61
qi 244,168,100
qg 144,4,4096
q9 11,11,92
qi 244,172,108
qm 94,62
qi 180,172,104
qg 148,4,5120
q9 11,11,93
qi 180,168,96
q0 8
qm 95,63
qi 184,176,96
qg 152,4,6144
q9 11,11,94
qi 184,180,104
q9 11,11,95
qi 248,180,108
qg 156,4,7168
q9 120,10,11
qi 248,176,100
q0 8
v_cmp_nle_f32 vcc,v120,v127
qi 252,184,100
qg 160,4,8192
qi 252,188,108
qi 188,188,104
qg 164,4,9216
qi 188,184,96
s_cbranch_vccnz Lag16kf_rs_nb
Lag16kf_ok_nb:
s_mov_b32 s77,2
s_getpc_b64 s[10:11]
Lpc22kf:
s_add_u32 s10,s10,Lsg19kf-Lpc22kf
s_addc_u32 s11,s11,0
.p2align 6
Lal17kf:
q0 8
qn 32,128,0,112
s_mov_b32 m0,s74
qg 168,4,10240
q9 8,8,10
ql 32,132,4
q0 7
qc 2,0
q9 9,9,11
ql 32,136,8
qg 172,4,11264
qb 96,64,65
ql 32,140,12
q0 6
qb 97,66,67
qb 98,72,73
ql 32,144,16
qg 176,4,12288
qb 99,74,75
ql 32,148,20
q0 5
qc 2,1024
qb 100,68,69
ql 32,152,24
qg 180,4,13312
qb 101,70,71
ql 32,156,28
qb 102,76,77
qb 103,78,79
qn 36,128,32,116
qg 184,4,14336
qb 104,80,81
ql 36,132,36
qc 2,2048
qb 105,82,83
ql 36,136,40
qg 188,4,15360
qb 106,88,89
ql 36,140,44
qb 107,90,91
qb 108,84,85
ql 36,144,48
qg 128,4,16384
qb 109,86,87
ql 36,148,52
qb 110,92,93
qb 111,94,95
ql 36,152,56
qg 132,4,17408
ql 36,156,60
q0 8
qc 2,3072
s_add_u32 m0,s74,4096
qn 40,160,0,112
qg 136,4,18432
ql 40,164,4
q0 7
qm 64,32
ql 40,168,8
qg 140,4,19456
ql 40,172,12
q0 6
qc 3,0
ql 40,176,16
qg 144,4,20480
ql 40,180,20
q0 5
qm 65,33
ql 40,184,24
qg 148,4,21504
q9 10,64,65
ql 40,188,28
qc 3,1024
qn 44,160,32,116
qg 152,4,22528
ql 44,164,36
qm 66,34
ql 44,168,40
qg 156,4,23552
q9 10,10,66
ql 44,172,44
qc 3,2048
ql 44,176,48
qg 160,4,24576
ql 44,180,52
qm 67,35
ql 44,184,56
qg 164,4,25600
q9 10,10,67
ql 44,188,60
q0 8
qc 3,3072
s_add_u32 s72,s72,1
qn 48,128,0,112
qg 168,4,26624
v_readlane_b32 s73,v5,s72
ql 48,132,4
q0 7
qm 68,36
ql 48,136,8
qg 172,4,27648
v_readlane_b32 s74,v6,s72
ql 48,140,12
q0 6
qm 69,37
ql 48,144,16
qg 176,4,28672
v_readlane_b32 s75,v7,s72
ql 48,148,20
LHH3_3kf:
s_cbranch_execz LHR3kf
q0 5
qm 70,38
ql 48,152,24
qg 180,4,29696
q9 11,68,69
ql 48,156,28
qm 71,39
qn 52,128,32,116
qg 184,4,30720
q9 11,11,70
ql 52,132,36
qm 72,40
ql 52,136,40
qg 188,4,31744
q9 11,11,71
ql 52,140,44
q3 16
s_barrier
v_add_u32 v4,s75,v2
ql 52,144,48
qg 128,4,0
q9 10,10,72
ql 52,148,52
qm 73,41
ql 52,152,56
qg 132,4,1024
q9 10,10,73
ql 52,156,60
q0 8
qm 74,42
qn 56,160,0,112
qg 136,4,2048
q9 10,10,74
ql 56,164,4
q0 7
qm 75,43
ql 56,168,8
qg 140,4,3072
q9 10,10,75
ql 56,172,12
)"
R"(q0 6
qm 76,44
ql 56,176,16
qg 144,4,4096
q9 11,11,76
ql 56,180,20
q0 5
qm 77,45
ql 56,184,24
qg 148,4,5120
q9 11,11,77
ql 56,188,28
qn 60,160,32,116
qg 152,4,6144
ql 60,164,36
qm 78,46
ql 60,168,40
qg 156,4,7168
q9 11,11,78
ql 60,172,44
qm 79,47
ql 60,176,48
qg 160,4,8192
q9 11,11,79
ql 60,180,52
ql 60,184,56
qg 164,4,9216
ql 60,188,60
q0 8
qm 80,48
qi 128,128,96
s_mov_b32 m0,s74
qg 168,4,10240
q9 10,10,80
qi 128,132,104
qc 2,0
qi 192,132,108
qg 172,4,11264
qi 192,128,100
q0 8
qm 81,49
qi 196,136,100
qg 176,4,12288
q9 10,10,81
qi 196,140,108
qc 2,1024
qi 132,140,104
qg 180,4,13312
qi 132,136,96
q0 8
qm 82,50
qi 136,144,96
qg 184,4,14336
q9 10,10,82
qi 136,148,104
qc 2,2048
qi 200,148,108
qg 188,4,15360
qi 200,144,100
q0 8
qm 83,51
qi 204,152,100
qg 128,4,16384
q9 10,10,83
qi 204,156,108
qm 84,52
qi 140,156,104
qg 132,4,17408
q9 11,11,84
qi 140,152,96
q0 8
qc 2,3072
s_add_u32 m0,s74,4096
qi 144,160,96
qg 136,4,18432
qi 144,164,104
qm 85,53
qi 208,164,108
qg 140,4,19456
q9 11,11,85
qi 208,160,100
q0 8
qc 3,0
qi 212,168,100
qg 144,4,20480
qi 212,172,108
qm 86,54
qi 148,172,104
qg 148,4,21504
q9 11,11,86
qi 148,168,96
q0 8
qc 3,1024
qi 152,176,96
qg 152,4,22528
qi 152,180,104
qm 87,55
qi 216,180,108
qg 156,4,23552
q9 11,11,87
qi 216,176,100
q0 8
qc 3,2048
qi 220,184,100
qg 160,4,24576
qi 220,188,108
qm 88,56
qi 156,188,104
qg 164,4,25600
q9 10,10,88
qi 156,184,96
q0 8
qc 3,3072
s_add_u32 s72,s72,1
qi 160,128,96
qg 168,4,26624
v_readlane_b32 s73,v5,s72
qi 160,132,104
qm 89,57
qi 224,132,108
qg 172,4,27648
v_readlane_b32 s74,v6,s72
qi 224,128,100
q0 8
qm 90,58
qi 228,136,100
qg 176,4,28672
v_readlane_b32 s75,v7,s72
qi 228,140,108
qm 91,59
qi 164,140,104
qg 180,4,29696
q9 10,10,89
qi 164,136,96
q0 8
qm 92,60
qi 168,144,96
qg 184,4,30720
q9 10,10,90
qi 168,148,104
qm 93,61
qi 232,148,108
qg 188,4,31744
q9 10,10,91
qi 232,144,100
s_waitcnt vmcnt(16) lgkmcnt(8)
s_barrier
v_add_u32 v4,s75,v2
qi 236,152,100
qg 128,4,0
q9 11,11,92
qi 236,156,108
qm 94,62
qi 172,156,104
qg 132,4,1024
q9 11,11,93
qi 172,152,96
q0 8
qm 95,63
qi 176,160,96
qg 136,4,2048
q9 11,11,94
qi 176,164,104
q9 11,11,95
qi 240,164,108
qg 140,4,3072
q9 120,10,11
qi 240,160,100
q0 8
v_cmp_nle_f32 vcc,v120,v127
qi 244,168,100
qg 144,4,4096
qi 244,172,108
qi 180,172,104
qg 148,4,5120
qi 180,168,96
q0 8
qi 184,176,96
qg 152,4,6144
qi 184,180,104
qi 248,180,108
qg 156,4,7168
qi 248,176,100
q0 8
qi 252,184,100
qg 160,4,8192
qi 252,188,108
qi 188,188,104
qg 164,4,9216
s_sub_u32 s77,s77,1
s_cmp_lg_u32 s77,0
qi 188,184,96
s_cbranch_vccnz Lit23kf_rs_nb
Lit23kf_ok_nb:
s_cbranch_scc1 Lal17kf
s_setpc_b64 s[10:11]
Lsg19kf:
v_mov_b32 v124,s81
v_mov_b32 v125,0
buffer_store_dword v124,v125,s[32:35],s87 offen sc1
s_add_u32 s10,s10,Lsg20kf-Lsg19kf
s_addc_u32 s11,s11,0
s_mov_b32 s77,4
s_branch Lal17kf
Lsg20kf:
s_mov_b32 s44,s6
s_mov_b32 s45,s7
s_mov_b32 s46,LENDkf-LCODEkf
s_mov_b32 s47,0x20000
s_and_b32 s54,s68,1
s_lshl1_add_u32 s54,s70,s54
s_lshl2_add_u32 s54,s54,s31
s_lshl_b32 s54,s54,12
v_and_b32 v31,31,v1
v_lshlrev_b32 v31,7,v31
buffer_load_dword v31,v31,s[44:47],s54 offen
s_add_u32 s10,s10,Lsg21kf-Lsg20kf
s_addc_u32 s11,s11,0
s_sub_u32 s77,s84,14
s_branch Lal17kf
Lsg21kf:
s_nop 1
v_and_b32 v124,7,v1
v_lshlrev_b32 v124,7,v124
buffer_load_dword v125,v124,s[32:35],s94 offen sc1
q0 8
qn 32,128,0,112
s_mov_b32 m0,s74
qg 168,4,10240
q9 8,8,10
ql 32,132,4
q0 7
qc 2,0
q9 9,9,11
ql 32,136,8
qg 172,4,11264
qb 96,64,65
ql 32,140,12
q0 6
qb 97,66,67
qb 98,72,73
ql 32,144,16
qg 176,4,12288
qb 99,74,75
ql 32,148,20
q0 5
qc 2,1024
qb 100,68,69
ql 32,152,24
qg 180,4,13312
qb 101,70,71
ql 32,156,28
qb 102,76,77
qb 103,78,79
qn 36,128,32,116
qg 184,4,14336
qb 104,80,81
ql 36,132,36
qc 2,2048
qb 105,82,83
ql 36,136,40
qg 188,4,15360
qb 106,88,89
ql 36,140,44
qb 107,90,91
qb 108,84,85
ql 36,144,48
qg 128,4,16384
qb 109,86,87
ql 36,148,52
qb 110,92,93
qb 111,94,95
ql 36,152,56
qg 132,4,17408
ql 36,156,60
q0 8
qc 2,3072
s_add_u32 m0,s74,4096
qn 40,160,0,112
qg 136,4,18432
ql 40,164,4
q0 7
qm 64,32
ql 40,168,8
qg 140,4,19456
ql 40,172,12
q0 6
qc 3,0
ql 40,176,16
qg 144,4,20480
ql 40,180,20
q0 5
qm 65,33
ql 40,184,24
qg 148,4,21504
q9 10,64,65
ql 40,188,28
qc 3,1024
qn 44,160,32,116
qg 152,4,22528
ql 44,164,36
qm 66,34
ql 44,168,40
qg 156,4,23552
q9 10,10,66
ql 44,172,44
qc 3,2048
ql 44,176,48
qg 160,4,24576
ql 44,180,52
qm 67,35
ql 44,184,56
qg 164,4,25600
q9 10,10,67
ql 44,188,60
q0 8
qc 3,3072
s_add_u32 s72,s72,1
qn 48,128,0,112
qg 168,4,26624
v_readlane_b32 s73,v5,s72
ql 48,132,4
q0 7
qm 68,36
ql 48,136,8
qg 172,4,27648
v_readlane_b32 s74,v6,s72
ql 48,140,12
q0 6
qm 69,37
ql 48,144,16
qg 176,4,28672
v_readlane_b32 s75,v7,s72
ql 48,148,20
s_waitcnt vmcnt(8) lgkmcnt(5)
qm 70,38
ql 48,152,24
qg 180,4,29696
v_cmp_ne_u32 vcc,s81,v125
ql 48,156,28
s_cbranch_vccnz Lslow11kf_nb
Lback12kf_nb:
qm 71,39
qn 52,128,32,116
qg 184,4,30720
q9 11,68,69
ql 52,132,36
buffer_load_dwordx4 a[224:227],v2,s[32:35],s97 offen sc1
s_add_u32 s97,s97,114688
ql 52,136,40
qg 188,4,31744
q9 11,11,70
ql 52,140,44
q3 18
s_barrier
v_add_u32 v4,s75,v2
ql 52,144,48
qg 128,4,0
q9 11,11,71
ql 52,148,52
qm 72,40
ql 52,152,56
qg 132,4,1024
q9 10,10,72
ql 52,156,60
q0 8
qm 73,41
qn 56,160,0,112
qg 136,4,2048
q9 10,10,73
ql 56,164,4
q0 7
qm 74,42
ql 56,168,8
qg 140,4,3072
q9 10,10,74
ql 56,172,12
q0 6
buffer_load_dwordx4 a[228:231],v2,s[32:35],s97 offen sc1
s_add_u32 s97,s97,114688
ql 56,176,16
qg 144,4,4096
ql 56,180,20
q0 5
qm 75,43
ql 56,184,24
qg 148,4,5120
q9 10,10,75
ql 56,188,28
qm 76,44
qn 60,160,32,116
qg 152,4,6144
q9 11,11,76
ql 60,164,36
qm 77,45
ql 60,168,40
qg 156,4,7168
q9 11,11,77
ql 60,172,44
qm 78,46
ql 60,176,48
qg 160,4,8192
q9 11,11,78
ql 60,180,52
buffer_load_dwordx4 a[232:235],v2,s[32:35],s97 offen sc1
s_add_u32 s97,s97,114688
ql 60,184,56
qg 164,4,9216
ql 60,188,60
q0 8
qm 79,47
qi 128,128,96
s_mov_b32 m0,s74
qg 168,4,10240
q9 11,11,79
qi 128,132,104
qc 2,0
qi 192,132,108
qg 172,4,11264
qi 192,128,100
q0 8
qm 80,48
qi 196,136,100
qg 176,4,12288
q9 10,10,80
qi 196,140,108
qc 2,1024
qi 132,140,104
qg 180,4,13312
qi 132,136,96
q0 8
buffer_load_dwordx4 a[236:239],v2,s[32:35],s97 offen sc1
s_add_u32 s97,s97,114688
qi 136,144,96
qg 184,4,14336
qi 136,148,104
qc 2,2048
qi 200,148,108
qg 188,4,15360
qi 200,144,100
q0 8
qm 81,49
qi 204,152,100
qg 128,4,16384
q9 10,10,81
qi 204,156,108
qm 82,50
qi 140,156,104
qg 132,4,17408
q9 10,10,82
qi 140,152,96
q0 8
qc 2,3072
s_add_u32 m0,s74,4096
qi 144,160,96
qg 136,4,18432
qi 144,164,104
buffer_load_dwordx4 a[240:243],v2,s[32:35],s97 offen sc1
s_add_u32 s97,s97,114688
qi 208,164,108
qg 140,4,19456
qi 208,160,100
q0 8
qc 3,0
qi 212,168,100
qg 144,4,20480
qi 212,172,108
qm 83,51
qi 148,172,104
qg 148,4,21504
q9 10,10,83
qi 148,168,96
q0 8
qc 3,1024
qi 152,176,96
qg 152,4,22528
qi 152,180,104
qm 84,52
qi 216,180,108
qg 156,4,23552
q9 11,11,84
qi 216,176,100
q0 8
qc 3,2048
qi 220,184,100
qg 160,4,24576
qi 220,188,108
buffer_load_dwordx4 a[244:247],v2,s[32:35],s97 offen sc1
s_add_u32 s97,s97,114688
qi 156,188,104
qg 164,4,25600
qi 156,184,96
q0 8
qc 3,3072
s_add_u32 s72,s72,1
qi 160,128,96
qg 168,4,26624
v_readlane_b32 s73,v5,s72
qi 160,132,104
qm 85,53
qi 224,132,108
qg 172,4,27648
v_readlane_b32 s74,v6,s72
qi 224,128,100
q0 8
qm 86,54
qi 228,136,100
qg 176,4,28672
v_readlane_b32 s75,v7,s72
qi 228,140,108
qm 87,55
qi 164,140,104
qg 180,4,29696
q9 11,11,85
qi 164,136,96
q0 8
buffer_load_dwordx4 a[248:251],v2,s[32:35],s97 offen sc1
s_add_u32 s97,s97,114688
qi 168,144,96
qg 184,4,30720
q9 11,11,86
qi 168,148,104
qm 88,56
qi 232,148,108
qg 188,4,31744
q9 11,11,87
qi 232,144,100
s_waitcnt vmcnt(24) lgkmcnt(8)
s_barrier
v_add_u32 v4,s75,v2
qi 236,152,100
qg 128,4,0
q9 10,10,88
qi 236,156,108
qm 89,57
qi 172,156,104
qg 132,4,1024
q9 10,10,89
qi 172,152,96
q0 8
qm 90,58
qi 176,160,96
qg 136,4,2048
q9 10,10,90
qi 176,164,104
buffer_load_dwordx4 a[252:255],v2,s[32:35],s97 offen sc1
qi 240,164,108
qg 140,4,3072
qi 240,160,100
q0 8
qm 91,59
qi 244,168,100
qg 144,4,4096
q9 10,10,91
qi 244,172,108
qm 92,60
qi 180,172,104
qg 148,4,5120
q9 11,11,92
qi 180,168,96
q0 8
qm 93,61
qi 184,176,96
qg 152,4,6144
q9 11,11,93
qi 184,180,104
qm 94,62
qi 248,180,108
qg 156,4,7168
q9 11,11,94
qi 248,176,100
q0 8
qm 95,63
qi 252,184,100
qg 160,4,8192
q9 11,11,95
qi 252,188,108
q9 120,10,11
qi 188,188,104
qg 164,4,9216
v_cmp_nle_f32 vcc,v120,v127
qi 188,184,96
s_cbranch_vccnz Lai24kf_rs_nb
Lai24kf_ok_nb:
q0 8
v_cmp_gt_i32 s[56:57],0,v14
v_cmp_gt_i32 s[58:59],1,v14
v_cmp_gt_i32 s[60:61],2,v14
v_cmp_gt_i32 s[62:63],3,v14
v_cndmask_b32 v16,v112,v126,s[56:57]
LHH4_0kf:
s_cbranch_execz LHR4kf
v_cndmask_b32 v17,v112,v126,s[58:59]
v_cndmask_b32 v18,v112,v126,s[60:61]
v_cndmask_b32 v19,v112,v126,s[62:63]
qn 36,128,32,116
s_mov_b32 m0,s74
qg 168,4,10240
v_cmp_gt_i32 s[56:57],16,v14
ql 36,132,36
q0 7
qc 2,0
v_cmp_gt_i32 s[58:59],17,v14
ql 36,136,40
qg 172,4,11264
v_cmp_gt_i32 s[60:61],18,v14
ql 36,140,44
q0 6
q9 8,8,10
q9 9,9,11
ql 36,144,48
qg 176,4,12288
v_cmp_gt_i32 s[62:63],19,v14
ql 36,148,52
q0 5
qc 2,1024
v_cndmask_b32 v20,v112,v126,s[56:57]
ql 36,152,56
qg 180,4,13312
v_cndmask_b32 v21,v112,v126,s[58:59]
ql 36,156,60
qb 96,64,65
qb 97,66,67
qn 32,128,0,16
qg 184,4,14336
v_cndmask_b32 v22,v112,v126,s[60:61]
ql 32,132,4
qc 2,2048
v_cndmask_b32 v23,v112,v126,s[62:63]
ql 32,136,8
qg 188,4,15360
v_cmp_gt_i32 s[56:57],32,v14
ql 32,140,12
qb 98,72,73
qb 99,74,75
ql 32,144,16
qg 128,4,16384
v_cmp_gt_i32 s[58:59],33,v14
ql 32,148,20
qb 100,68,69
qb 101,70,71
ql 32,152,24
qg 132,4,17408
v_cmp_gt_i32 s[60:61],34,v14
ql 32,156,28
q0 8
qc 2,3072
s_add_u32 m0,s74,4096
qn 44,160,32,116
qg 136,4,18432
v_cmp_gt_i32 s[62:63],35,v14
ql 44,164,36
q0 7
qb 102,76,77
qb 103,78,79
ql 44,168,40
qg 140,4,19456
v_cndmask_b32 v24,v112,v126,s[56:57]
ql 44,172,44
q0 6
qc 3,0
v_cndmask_b32 v25,v112,v126,s[58:59]
ql 44,176,48
qg 144,4,20480
v_mov_b32 v121,0
ql 44,180,52
q0 5
buffer_load_dword v123,v121,s[32:35],s93 offen sc1
v_cndmask_b32 v26,v112,v126,s[60:61]
ql 44,184,56
qg 148,4,21504
v_cndmask_b32 v27,v112,v126,s[62:63]
ql 44,188,60
qc 3,1024
v_cmp_gt_i32 s[56:57],48,v14
qn 40,160,0,20
qg 152,4,22528
v_cmp_gt_i32 s[58:59],49,v14
ql 40,164,4
qb 104,80,81
qb 105,82,83
ql 40,168,8
qg 156,4,23552
v_cmp_gt_i32 s[60:61],50,v14
ql 40,172,12
qc 3,2048
v_cmp_gt_i32 s[62:63],51,v14
ql 40,176,16
qg 160,4,24576
v_cndmask_b32 v28,v112,v126,s[56:57]
ql 40,180,20
qb 106,88,89
qb 107,90,91
ql 40,184,24
qg 164,4,25600
v_cndmask_b32 v29,v112,v126,s[58:59]
ql 40,188,28
q0 8
qc 3,3072
s_add_u32 s72,s72,1
qn 52,128,32,116
qg 168,4,26624
v_readlane_b32 s73,v5,s72
ql 52,132,36
q0 7
v_cndmask_b32 v30,v112,v126,s[60:61]
v_cndmask_b32 v31,v112,v126,s[62:63]
ql 52,136,40
qg 172,4,27648
v_readlane_b32 s74,v6,s72
ql 52,140,44
q0 6
qb 108,84,85
qb 109,86,87
ql 52,144,48
qg 176,4,28672
v_readlane_b32 s75,v7,s72
ql 52,148,52
q0 5
qb 110,92,93
qb 111,94,95
ql 52,152,56
qg 180,4,29696
ql 52,156,60
qm 68,36
qn 48,128,0,24
qg 184,4,30720
ql 48,132,4
qm 69,37
ql 48,136,8
qg 188,4,31744
q9 11,68,69
ql 48,140,12
q3 25
s_barrier
v_add_u32 v4,s75,v2
ql 48,144,16
qg 128,4,0
ql 48,148,20
qm 70,38
ql 48,152,24
qg 132,4,1024
q9 11,11,70
ql 48,156,28
q0 8
qm 71,39
qn 60,160,32,116
qg 136,4,2048
q9 11,11,71
ql 60,164,36
q0 7
qm 64,32
ql 60,168,40
qg 140,4,3072
ql 60,172,44
q0 6
qm 65,33
ql 60,176,48
qg 144,4,4096
q9 10,64,65
ql 60,180,52
q0 5
qm 66,34
ql 60,184,56
qg 148,4,5120
q9 10,10,66
ql 60,188,60
qm 67,35
qn 56,160,0,28
qg 152,4,6144
q9 10,10,67
ql 56,164,4
qm 76,44
ql 56,168,8
qg 156,4,7168
q9 11,11,76
ql 56,172,12
qm 77,45
ql 56,176,16
qg 160,4,8192
q9 11,11,77
ql 56,180,20
qm 78,46
ql 56,184,24
qg 164,4,9216
q9 11,11,78
ql 56,188,28
q0 8
qm 79,47
qi 128,128,96
s_mov_b32 m0,s74
qg 168,4,10240
q3 3
qi 128,132,104
qc 2,0
v_cmp_ne_u32 vcc,s81,v123
qi 192,132,108
qg 172,4,11264
s_cbranch_vccnz Lslow26kf_nb
Lback27kf_nb:
qi 192,128,100
q0 8
s_add_u32 s50,s86,0
q9 11,11,79
qi 196,136,100
qg 176,4,12288
qi 196,140,108
qc 2,1024
qi 132,140,104
qg 180,4,13312
qi 132,136,96
q0 8
buffer_load_dwordx4 a[64:67],v2,s[32:35],s50 offen offset:0 sc1
s_add_u32 s50,s86,8192
qi 136,144,96
qg 184,4,14336
qi 136,148,104
qc 2,2048
qi 200,148,108
qg 188,4,15360
qi 200,144,100
q0 8
qm 72,40
qi 204,152,100
qg 128,4,16384
q9 10,10,72
qi 204,156,108
buffer_load_dwordx4 a[96:99],v2,s[32:35],s50 offen offset:0 sc1
s_add_u32 s50,s86,0
qi 140,156,104
qg 132,4,17408
qi 140,152,96
q0 8
qc 2,3072
s_add_u32 m0,s74,4096
qi 144,160,96
qg 136,4,18432
qi 144,164,104
qm 73,41
qi 208,164,108
qg 140,4,19456
q9 10,10,73
qi 208,160,100
q0 8
qc 3,0
qi 212,168,100
qg 144,4,20480
qi 212,172,108
buffer_load_dwordx4 a[68:71],v2,s[32:35],s50 offen offset:1024 sc1
s_add_u32 s50,s86,8192
qi 148,172,104
qg 148,4,21504
qi 148,168,96
q0 8
qc 3,1024
qi 152,176,96
qg 152,4,22528
qi 152,180,104
qm 74,42
qi 216,180,108
qg 156,4,23552
q9 10,10,74
qi 216,176,100
q0 8
qc 3,2048
qi 220,184,100
qg 160,4,24576
qi 220,188,108
buffer_load_dwordx4 a[100:103],v2,s[32:35],s50 offen offset:1024 sc1
s_add_u32 s50,s86,0
qi 156,188,104
qg 164,4,25600
qi 156,184,96
q0 8
qc 3,3072
s_add_u32 s72,s72,1
qi 160,128,96
qg 168,4,26624
v_readlane_b32 s73,v5,s72
qi 160,132,104
qm 75,43
qi 224,132,108
qg 172,4,27648
v_readlane_b32 s74,v6,s72
qi 224,128,100
q0 8
buffer_load_dwordx4 a[72:75],v2,s[32:35],s50 offen offset:2048 sc1
s_add_u32 s50,s86,8192
qi 228,136,100
qg 176,4,28672
v_readlane_b32 s75,v7,s72
qi 228,140,108
qm 84,52
qi 164,140,104
qg 180,4,29696
q9 10,10,75
qi 164,136,96
q0 8
qm 85,53
qi 168,144,96
qg 184,4,30720
q9 11,11,84
qi 168,148,104
buffer_load_dwordx4 a[104:107],v2,s[32:35],s50 offen offset:2048 sc1
s_add_u32 s50,s86,0
qi 232,148,108
qg 188,4,31744
q9 11,11,85
qi 232,144,100
s_waitcnt vmcnt(25) lgkmcnt(8)
s_barrier
v_add_u32 v4,s75,v2
qi 236,152,100
qg 128,4,0
qi 236,156,108
qm 86,54
qi 172,156,104
qg 132,4,1024
q9 11,11,86
qi 172,152,96
q0 8
buffer_load_dwordx4 a[76:79],v2,s[32:35],s50 offen offset:3072 sc1
s_add_u32 s50,s86,8192
qi 176,160,96
qg 136,4,2048
qi 176,164,104
qm 87,55
qi 240,164,108
qg 140,4,3072
q9 11,11,87
qi 240,160,100
q0 8
qm 80,48
qi 244,168,100
qg 144,4,4096
q9 10,10,80
qi 244,172,108
buffer_load_dwordx4 a[108:111],v2,s[32:35],s50 offen offset:3072 sc1
qi 180,172,104
qg 148,4,5120
qi 180,168,96
q0 8
qm 81,49
qi 184,176,96
qg 152,4,6144
q9 10,10,81
qi 184,180,104
qm 82,50
qi 248,180,108
qg 156,4,7168
q9 10,10,82
qi 248,176,100
q0 8
qm 83,51
qi 252,184,100
qg 160,4,8192
q9 10,10,83
qi 252,188,108
qm 92,60
qi 188,188,104
qg 164,4,9216
q9 11,11,92
qi 188,184,96
qm 93,61
qm 94,62
qm 95,63
qm 88,56
qm 89,57
qm 90,58
qm 91,59
q9 11,11,93
q9 11,11,94
q9 11,11,95
q9 10,10,88
q9 10,10,89
q9 10,10,90
q9 10,10,91
q9 120,10,11
v_cmp_nle_f32 vcc,v120,v127
s_nop 0
s_cbranch_vccnz Ld025kf_rs_nb
Ld025kf_ok_nb:
q0 8
v_cmp_gt_i32 s[56:57],0,v15
v_cmp_gt_i32 s[58:59],1,v15
v_cmp_gt_i32 s[60:61],2,v15
v_cmp_gt_i32 s[62:63],3,v15
v_cndmask_b32 v16,v116,v126,s[56:57]
v_cndmask_b32 v17,v116,v126,s[58:59]
v_cndmask_b32 v18,v116,v126,s[60:61]
v_cndmask_b32 v19,v116,v126,s[62:63]
v_cmp_gt_i32 s[56:57],16,v15
v_cmp_gt_i32 s[58:59],17,v15
v_cmp_gt_i32 s[60:61],18,v15
v_cmp_gt_i32 s[62:63],19,v15
v_cndmask_b32 v20,v116,v126,s[56:57]
v_cndmask_b32 v21,v116,v126,s[58:59]
v_cndmask_b32 v22,v116,v126,s[60:61]
v_cndmask_b32 v23,v116,v126,s[62:63]
qn 36,128,32,16
s_mov_b32 m0,s74
qg 168,4,10240
v_cmp_gt_i32 s[56:57],32,v15
ql 36,132,36
q0 7
qg 172,4,11264
v_cmp_gt_i32 s[58:59],33,v15
ql 36,136,40
qg 176,4,12288
v_cmp_gt_i32 s[60:61],34,v15
ql 36,140,44
q0 7
qg 180,4,13312
v_cmp_gt_i32 s[62:63],35,v15
ql 36,144,48
qg 184,4,14336
v_cndmask_b32 v24,v116,v126,s[56:57]
ql 36,148,52
q0 7
qg 188,4,15360
v_cndmask_b32 v25,v116,v126,s[58:59]
ql 36,152,56
qg 128,4,16384
v_cndmask_b32 v26,v116,v126,s[60:61]
ql 36,156,60
q0 7
qg 132,4,17408
v_cndmask_b32 v27,v116,v126,s[62:63]
qn 44,160,32,20
qg 136,4,18432
v_cmp_gt_i32 s[56:57],48,v15
ql 44,164,36
q0 7
qg 140,4,19456
v_cmp_gt_i32 s[58:59],49,v15
ql 44,168,40
qg 144,4,20480
v_cmp_gt_i32 s[60:61],50,v15
ql 44,172,44
q0 7
qg 148,4,21504
v_cmp_gt_i32 s[62:63],51,v15
ql 44,176,48
qg 152,4,22528
v_cndmask_b32 v28,v116,v126,s[56:57]
ql 44,180,52
q0 7
qg 156,4,23552
v_cndmask_b32 v29,v116,v126,s[58:59]
ql 44,184,56
qg 160,4,24576
v_cndmask_b32 v30,v116,v126,s[60:61]
ql 44,188,60
q0 7
qg 164,4,25600
v_cndmask_b32 v31,v116,v126,s[62:63]
qn 52,128,32,24
qg 168,4,26624
q9 8,8,10
ql 52,132,36
q0 7
qg 172,4,27648
q9 9,9,11
ql 52,136,40
qg 176,4,28672
qb 96,64,65
ql 52,140,44
q0 7
qg 180,4,29696
qb 97,66,67
ql 52,144,48
s_add_u32 s72,s72,1
qg 184,4,30720
qc 2,0
qc 2,1024
qc 2,2048
qc 2,3072
s_add_u32 m0,s74,4096
s_nop 0
qc 3,0
qc 3,1024
qc 3,2048
qc 3,3072
v_readlane_b32 s73,v5,s72
v_readlane_b32 s74,v6,s72
v_readlane_b32 s75,v7,s72
ql 52,148,52
q0 7
qg 188,4,31744
q3 24
s_barrier
v_add_u32 v4,s75,v2
ql 52,152,56
qg 128,4,0
qb 98,72,73
ql 52,156,60
q0 7
qg 132,4,1024
qb 99,74,75
qn 60,160,32,28
qg 136,4,2048
qb 100,68,69
ql 60,164,36
q0 7
qg 140,4,3072
qb 101,70,71
ql 60,168,40
qg 144,4,4096
qb 102,76,77
ql 60,172,44
q0 7
qg 148,4,5120
qb 103,78,79
ql 60,176,48
qg 152,4,6144
qb 104,80,81
ql 60,180,52
q0 7
qg 156,4,7168
qb 105,82,83
qb 106,88,89
qb 107,90,91
qb 108,84,85
qb 109,86,87
qb 110,92,93
qb 111,94,95
ql 60,184,56
qg 160,4,8192
v_mov_b32 v10,0
ql 60,188,60
q0 7
qg 164,4,9216
qi 128,128,96
s_mov_b32 m0,s74
qg 168,4,10240
s_add_u32 s50,s86,16384
qi 128,132,104
qc 2,0
qi 192,132,108
qg 172,4,11264
qi 192,128,100
q0 8
buffer_load_dwordx4 a[0:3],v2,s[32:35],s50 offen sc1
s_add_u32 s50,s86,4096
qi 196,136,100
qg 176,4,12288
qi 196,140,108
qc 2,1024
qi 132,140,104
qg 180,4,13312
qi 132,136,96
q0 8
qm 68,36
qi 136,144,96
qg 184,4,14336
qi 136,148,104
qc 2,2048
qi 200,148,108
qg 188,4,15360
qi 200,144,100
q0 8
buffer_load_dwordx4 a[80:83],v2,s[32:35],s50 offen offset:0 sc1
s_add_u32 s50,s86,12288
qi 204,152,100
qg 128,4,16384
qi 204,156,108
qm 69,37
qi 140,156,104
qg 132,4,17408
q9 11,68,69
qi 140,152,96
q0 8
qc 2,3072
s_add_u32 m0,s74,4096
qi 144,160,96
qg 136,4,18432
qi 144,164,104
LHH4_1kf:
s_cbranch_execz LHR4kf
buffer_load_dwordx4 a[112:115],v2,s[32:35],s50 offen offset:0 sc1
s_add_u32 s50,s86,4096
qi 208,164,108
qg 140,4,19456
qi 208,160,100
q0 8
qc 3,0
qi 212,168,100
qg 144,4,20480
qi 212,172,108
qm 70,38
qi 148,172,104
qg 148,4,21504
q9 11,11,70
qi 148,168,96
q0 8
qc 3,1024
qi 152,176,96
qg 152,4,22528
qi 152,180,104
buffer_load_dwordx4 a[84:87],v2,s[32:35],s50 offen offset:1024 sc1
s_add_u32 s50,s86,12288
qi 216,180,108
qg 156,4,23552
qi 216,176,100
q0 8
qc 3,2048
qi 220,184,100
qg 160,4,24576
qi 220,188,108
qm 71,39
qi 156,188,104
qg 164,4,25600
q9 11,11,71
qi 156,184,96
q0 8
qc 3,3072
s_add_u32 s72,s72,1
qi 160,128,96
qg 168,4,26624
v_readlane_b32 s73,v5,s72
qi 160,132,104
buffer_load_dwordx4 a[116:119],v2,s[32:35],s50 offen offset:1024 sc1
s_add_u32 s50,s86,4096
qi 224,132,108
qg 172,4,27648
v_readlane_b32 s74,v6,s72
qi 224,128,100
q0 8
qm 76,44
qi 228,136,100
qg 176,4,28672
v_readlane_b32 s75,v7,s72
qi 228,140,108
qm 77,45
qi 164,140,104
qg 180,4,29696
q9 11,11,76
qi 164,136,96
q0 8
buffer_load_dwordx4 a[88:91],v2,s[32:35],s50 offen offset:2048 sc1
s_add_u32 s50,s86,12288
qi 168,144,96
qg 184,4,30720
q9 11,11,77
qi 168,148,104
qm 78,46
qi 232,148,108
qg 188,4,31744
q9 11,11,78
qi 232,144,100
s_waitcnt vmcnt(26) lgkmcnt(8)
s_barrier
v_add_u32 v4,s75,v2
qi 236,152,100
qg 128,4,0
qi 236,156,108
buffer_load_dwordx4 a[120:123],v2,s[32:35],s50 offen offset:2048 sc1
s_add_u32 s50,s86,4096
qi 172,156,104
qg 132,4,1024
qi 172,152,96
q0 8
qm 79,47
qi 176,160,96
qg 136,4,2048
q9 11,11,79
qi 176,164,104
qm 84,52
qi 240,164,108
qg 140,4,3072
q9 11,11,84
qi 240,160,100
q0 8
buffer_load_dwordx4 a[92:95],v2,s[32:35],s50 offen offset:3072 sc1
s_add_u32 s50,s86,12288
qi 244,168,100
qg 144,4,4096
qi 244,172,108
qm 85,53
qi 180,172,104
qg 148,4,5120
q9 11,11,85
qi 180,168,96
q0 8
qm 86,54
qi 184,176,96
qg 152,4,6144
q9 11,11,86
qi 184,180,104
buffer_load_dwordx4 a[124:127],v2,s[32:35],s50 offen offset:3072 sc1
qi 248,180,108
qg 156,4,7168
qi 248,176,100
q0 8
qm 87,55
qi 252,184,100
qg 160,4,8192
q9 11,11,87
qi 252,188,108
qm 92,60
qi 188,188,104
qg 164,4,9216
q9 11,11,92
qi 188,184,96
qm 93,61
qm 94,62
qm 95,63
q9 11,11,93
q9 11,11,94
q9 11,11,95
v_cmp_nle_f32 vcc,v11,v127
s_nop 0
s_cbranch_vccnz Ld128kf_rs_nb
Ld128kf_ok_nb:
q9 9,9,11
v_bfe_u32 v120,v1,2,2
v_lshrrev_b32 v121,4,v1
v_cmp_eq_u32 vcc,v120,v121
v_and_b32 v120,1,v1
v_lshlrev_b32 v120,4,v120
v_mov_b32 v121,0x3f80
v_lshlrev_b32 v120,v120,v121
v_cndmask_b32 v120,0,v120,vcc
v_and_b32 v121,2,v1
v_cmp_eq_u32 vcc,0,v121
s_nop 1
v_cndmask_b32 v22,0,v120,vcc
v_cndmask_b32 v23,v120,0,vcc
v_mov_b32 v20,0
v_mov_b32 v21,0
v_mov_b32 v24,0
v_mov_b32 v25,0
q3 15
qd 112,0
qd 113,1
qd 114,2
qd 115,3
v_sub_f32 v26,v12,v112
v_min_f32 v26,0x42800000,v26
qm 26,26
v_sub_f32 v28,v13,v113
v_min_f32 v28,0x42800000,v28
qm 28,28
s_cmp_eq_u32 s3,0
s_cselect_b64 vcc,-1,0
v_cndmask_b32 v26,v26,1.0,vcc
v_mov_b32 v27,v26
v_cndmask_b32 v28,v28,1.0,vcc
v_mov_b32 v29,v28
v_pk_mul_f32 v[68:69],v[68:69],v[28:29]
v_pk_mul_f32 v[70:71],v[70:71],v[28:29]
v_pk_mul_f32 v[76:77],v[76:77],v[28:29]
v_pk_mul_f32 v[78:79],v[78:79],v[28:29]
v_pk_mul_f32 v[84:85],v[84:85],v[28:29]
v_pk_mul_f32 v[86:87],v[86:87],v[28:29]
v_pk_mul_f32 v[92:93],v[92:93],v[28:29]
v_pk_mul_f32 v[94:95],v[94:95],v[28:29]
qb 100,68,69
qb 101,70,71
qb 102,76,77
qb 103,78,79
qb 108,84,85
qb 109,86,87
qb 110,92,93
qb 111,94,95
v_pk_mul_f32 v[192:193],v[192:193],v[28:29]
v_pk_mul_f32 v[194:195],v[194:195],v[28:29]
v_pk_mul_f32 v[128:129],v[128:129],v[26:27]
v_pk_mul_f32 v[130:131],v[130:131],v[26:27]
v_pk_mul_f32 v[196:197],v[196:197],v[28:29]
v_pk_mul_f32 v[198:199],v[198:199],v[28:29]
v_pk_mul_f32 v[132:133],v[132:133],v[26:27]
v_pk_mul_f32 v[134:135],v[134:135],v[26:27]
v_pk_mul_f32 v[200:201],v[200:201],v[28:29]
v_pk_mul_f32 v[202:203],v[202:203],v[28:29]
v_pk_mul_f32 v[136:137],v[136:137],v[26:27]
v_pk_mul_f32 v[138:139],v[138:139],v[26:27]
v_pk_mul_f32 v[204:205],v[204:205],v[28:29]
v_pk_mul_f32 v[206:207],v[206:207],v[28:29]
v_pk_mul_f32 v[140:141],v[140:141],v[26:27]
v_pk_mul_f32 v[142:143],v[142:143],v[26:27]
v_pk_mul_f32 v[208:209],v[208:209],v[28:29]
v_pk_mul_f32 v[210:211],v[210:211],v[28:29]
v_pk_mul_f32 v[144:145],v[144:145],v[26:27]
v_pk_mul_f32 v[146:147],v[146:147],v[26:27]
v_pk_mul_f32 v[212:213],v[212:213],v[28:29]
v_pk_mul_f32 v[214:215],v[214:215],v[28:29]
v_pk_mul_f32 v[148:149],v[148:149],v[26:27]
v_pk_mul_f32 v[150:151],v[150:151],v[26:27]
v_pk_mul_f32 v[216:217],v[216:217],v[28:29]
v_pk_mul_f32 v[218:219],v[218:219],v[28:29]
v_pk_mul_f32 v[152:153],v[152:153],v[26:27]
v_pk_mul_f32 v[154:155],v[154:155],v[26:27]
v_pk_mul_f32 v[220:221],v[220:221],v[28:29]
v_pk_mul_f32 v[222:223],v[222:223],v[28:29]
v_pk_mul_f32 v[156:157],v[156:157],v[26:27]
v_pk_mul_f32 v[158:159],v[158:159],v[26:27]
v_pk_mul_f32 v[224:225],v[224:225],v[28:29]
v_pk_mul_f32 v[226:227],v[226:227],v[28:29]
v_pk_mul_f32 v[160:161],v[160:161],v[26:27]
v_pk_mul_f32 v[162:163],v[162:163],v[26:27]
v_pk_mul_f32 v[228:229],v[228:229],v[28:29]
v_pk_mul_f32 v[230:231],v[230:231],v[28:29]
v_pk_mul_f32 v[164:165],v[164:165],v[26:27]
v_pk_mul_f32 v[166:167],v[166:167],v[26:27]
v_pk_mul_f32 v[232:233],v[232:233],v[28:29]
v_pk_mul_f32 v[234:235],v[234:235],v[28:29]
v_pk_mul_f32 v[168:169],v[168:169],v[26:27]
v_pk_mul_f32 v[170:171],v[170:171],v[26:27]
v_pk_mul_f32 v[236:237],v[236:237],v[28:29]
v_pk_mul_f32 v[238:239],v[238:239],v[28:29]
v_pk_mul_f32 v[172:173],v[172:173],v[26:27]
v_pk_mul_f32 v[174:175],v[174:175],v[26:27]
v_pk_mul_f32 v[240:241],v[240:241],v[28:29]
v_pk_mul_f32 v[242:243],v[242:243],v[28:29]
v_pk_mul_f32 v[176:177],v[176:177],v[26:27]
v_pk_mul_f32 v[178:179],v[178:179],v[26:27]
v_pk_mul_f32 v[244:245],v[244:245],v[28:29]
v_pk_mul_f32 v[246:247],v[246:247],v[28:29]
v_pk_mul_f32 v[180:181],v[180:181],v[26:27]
v_pk_mul_f32 v[182:183],v[182:183],v[26:27]
v_pk_mul_f32 v[248:249],v[248:249],v[28:29]
v_pk_mul_f32 v[250:251],v[250:251],v[28:29]
v_pk_mul_f32 v[184:185],v[184:185],v[26:27]
v_pk_mul_f32 v[186:187],v[186:187],v[26:27]
v_pk_mul_f32 v[252:253],v[252:253],v[28:29]
v_pk_mul_f32 v[254:255],v[254:255],v[28:29]
v_pk_mul_f32 v[188:189],v[188:189],v[26:27]
v_pk_mul_f32 v[190:191],v[190:191],v[26:27]
q0 8
qi 192,128,100
s_mov_b32 m0,s74
qg 168,4,10240
v_mov_b32 v122,v8
qi 192,132,108
q3 36
qc 2,0
v_mov_b32 v123,v9
qo 192,22,96
qg 172,4,11264
qo 128,22,64
q0 8
v_permlane32_swap_b32 v8,v122
v_permlane32_swap_b32 v9,v123
qi 196,136,100
qg 176,4,12288
q9 8,8,122
qi 196,140,108
qc 2,1024
q9 9,9,123
qo 196,20,96
qg 180,4,13312
v_mov_b32 v122,v8
qo 132,20,64
q0 8
v_mov_b32 v123,v9
qi 200,144,100
qg 184,4,14336
qi 200,148,108
q3 32
qc 2,2048
v_permlane16_swap_b32 v8,v122
qo 200,22,100
qg 188,4,15360
v_permlane16_swap_b32 v9,v123
qo 136,22,68
q0 8
q9 8,8,122
q9 9,9,123
qi 204,152,100
qg 128,4,16384
v_fma_f32 v8,v8,v26,v114
qi 204,156,108
v_fma_f32 v9,v9,v28,v115
qb 64,192,193
qo 204,20,100
qg 132,4,17408
qb 65,194,195
qo 140,20,68
q0 8
qc 2,3072
s_add_u32 m0,s74,4096
qi 208,160,100
qg 136,4,18432
qb 66,196,197
qi 208,164,108
q3 31
qb 67,198,199
qb 32,128,129
qo 208,22,104
qg 140,4,19456
qb 33,130,131
qo 144,22,72
q0 8
qc 3,0
qb 34,132,133
qi 212,168,100
qg 144,4,20480
qb 35,134,135
qi 212,172,108
)"
R"(qb 68,200,201
qb 69,202,203
qo 212,20,104
qg 148,4,21504
qb 70,204,205
qo 148,20,72
q0 8
qc 3,1024
qb 71,206,207
qi 216,176,100
qg 152,4,22528
qb 36,136,137
qi 216,180,108
q3 31
qb 37,138,139
qb 38,140,141
qo 216,22,108
qg 156,4,23552
qb 39,142,143
qo 152,22,76
q0 8
qc 3,2048
qb 72,208,209
qi 220,184,100
qg 160,4,24576
qb 73,210,211
qi 220,188,108
qb 74,212,213
qb 75,214,215
qo 220,20,108
qg 164,4,25600
qb 40,144,145
qo 156,20,76
q0 8
qc 3,3072
s_add_u32 s72,s72,1
qi 224,128,100
qg 168,4,26624
v_readlane_b32 s73,v5,s72
qi 224,132,108
q3 18
v_readlane_b32 s74,v6,s72
v_readlane_b32 s75,v7,s72
qo 224,22,112
qg 172,4,27648
qb 41,146,147
qo 160,22,80
q0 8
qb 42,148,149
qb 43,150,151
qi 228,136,100
qg 176,4,28672
qb 76,216,217
qi 228,140,108
qb 77,218,219
qb 78,220,221
qo 228,20,112
qg 180,4,29696
qb 79,222,223
qo 164,20,80
q0 8
qb 44,152,153
qb 45,154,155
qi 232,144,100
qg 184,4,30720
qb 46,156,157
qi 232,148,108
q3 12
qb 47,158,159
qo 232,22,116
qg 188,4,31744
qo 168,22,84
s_waitcnt vmcnt(25) lgkmcnt(8)
s_barrier
v_add_u32 v4,s75,v2
qi 236,152,100
qg 128,4,0
qb 80,224,225
qi 236,156,108
qb 81,226,227
qb 82,228,229
qo 236,20,116
qg 132,4,1024
qb 83,230,231
qo 172,20,84
q0 8
qb 48,160,161
qb 49,162,163
qi 240,160,100
qg 136,4,2048
qb 50,164,165
qi 240,164,108
q3 10
qb 51,166,167
qo 240,22,120
qg 140,4,3072
qo 176,22,88
q0 8
qb 84,232,233
qb 85,234,235
qi 244,168,100
qg 144,4,4096
qb 86,236,237
qi 244,172,108
qb 87,238,239
qb 52,168,169
qo 244,20,120
qg 148,4,5120
qb 53,170,171
qo 180,20,88
q0 8
qb 54,172,173
qb 55,174,175
qi 248,176,100
qg 152,4,6144
qi 248,180,108
q3 8
qo 248,22,124
qg 156,4,7168
qo 184,22,92
q0 8
qb 88,240,241
qb 89,242,243
qi 252,184,100
qg 160,4,8192
qb 90,244,245
qi 252,188,108
qb 91,246,247
qb 56,176,177
qo 252,20,124
qg 164,4,9216
qb 57,178,179
qo 188,20,92
qb 58,180,181
v_rcp_f32 v8,v8
v_rcp_f32 v9,v9
qb 59,182,183
qb 92,248,249
qb 93,250,251
qb 94,252,253
qb 95,254,255
qb 60,184,185
qb 61,186,187
qb 62,188,189
qb 63,190,191
s_lshl4_add_u32 s48,s82,s68
s_mul_i32 s48,s48,114688
s_lshl_b32 s49,s31,12
s_add_u32 s48,s48,s49
s_add_u32 s80,s48,79306752
q0 8
qh 192,128,32
s_mov_b32 m0,s74
qg 168,4,10240
qi 192,132,36
q0 7
qc 2,0
qi 192,136,40
s_mov_b32 s44,s22
qg 172,4,11264
qi 192,140,44
q0 6
s_and_b32 s45,s23,0xffff
qi 192,144,48
s_mov_b32 s46,896
qg 176,4,12288
qi 192,148,52
q0 5
qc 2,1024
s_mov_b32 s47,0x20000
qi 192,152,56
s_mov_b32 s36,s4
qg 180,4,13312
qi 192,156,60
s_and_b32 s37,s5,0xffff
qh 196,128,64
s_mov_b32 s38,1835008
qg 184,4,14336
qi 196,132,68
s_mov_b32 s39,0x20000
qc 2,2048
qi 196,136,72
s_mov_b32 s40,s24
qg 188,4,15360
qi 196,140,76
s_and_b32 s41,s25,0xffff
qi 196,144,80
s_mov_b32 s42,1835008
qg 128,4,16384
qi 196,148,84
s_mov_b32 s43,0x20000
qi 196,152,88
s_lshl2_add_u32 s48,s82,s31
qg 132,4,17408
qi 196,156,92
q0 8
qc 2,3072
s_add_u32 m0,s74,4096
qh 200,160,32
s_lshl4_add_u32 s48,s48,s68
qg 136,4,18432
qi 200,164,36
q0 7
s_lshl_b32 s48,s48,7
qi 200,168,40
s_add_u32 s93,s48,393216
qg 140,4,19456
qi 200,172,44
q0 6
qc 3,0
s_add_u32 s4,s93,131072
qi 200,176,48
s_add_u32 s5,s93,262144
qg 144,4,20480
qi 200,180,52
q0 5
s_add_u32 s8,s93,393216
qi 200,184,56
qg 148,4,21504
v_mov_b32 v120,s81
qi 200,188,60
qc 3,1024
v_mov_b32 v121,0
qh 204,160,64
s_add_u32 s10,s95,131072
qg 152,4,22528
qi 204,164,68
s_add_u32 s48,s31,2
qi 204,168,72
s_and_b32 s48,s48,3
qg 156,4,23552
qi 204,172,76
s_lshl_b32 s48,s48,17
qc 3,2048
qi 204,176,80
s_add_u32 s11,s95,s48
qg 160,4,24576
qa 192,8,192
qi 204,180,84
v_bfe_u32 v22,v1,2,2
qa 193,8,193
qi 204,184,88
qg 164,4,25600
v_lshrrev_b32 v23,4,v1
qi 204,188,92
q0 8
qc 3,3072
s_add_u32 s72,s72,1
qh 208,128,32
qg 168,4,26624
v_readlane_b32 s73,v5,s72
qi 208,132,36
q0 7
v_readlane_b32 s74,v6,s72
v_readlane_b32 s75,v7,s72
qi 208,136,40
qg 172,4,27648
v_cmp_eq_u32 vcc,v22,v23
qi 208,140,44
q0 6
v_and_b32 v22,1,v1
qa 194,8,194
qi 208,144,48
LHH4_2kf:
s_cbranch_execz LHR4kf
qg 176,4,28672
v_lshlrev_b32 v22,4,v22
qi 208,148,52
q0 5
v_mov_b32 v23,0x3f80
qa 195,8,195
qi 208,152,56
qg 180,4,29696
v_lshlrev_b32 v22,v22,v23
qi 208,156,60
v_cndmask_b32 v22,0,v22,vcc
qa 200,8,200
qh 212,128,64
qg 184,4,30720
v_and_b32 v23,2,v1
qi 212,132,68
v_cmp_eq_u32 vcc,0,v23
qa 201,8,201
qi 212,136,72
qg 188,4,31744
v_cndmask_b32 v18,0,v22,vcc
qi 212,140,76
q3 21
s_barrier
v_add_u32 v4,s75,v2
qi 212,144,80
qg 128,4,0
v_cndmask_b32 v19,v22,0,vcc
qi 212,148,84
v_mov_b32 v16,0
qa 202,8,202
qi 212,152,88
qg 132,4,1024
v_mov_b32 v17,0
qi 212,156,92
q0 8
v_mov_b32 v20,0
qa 203,8,203
qh 216,160,32
qg 136,4,2048
v_mov_b32 v21,0
qi 216,164,36
q0 7
v_and_b32 v25,48,v1
qb 224,192,193
qi 216,168,40
qg 140,4,3072
v_add_u32 v25,s90,v25
qi 216,172,44
q0 6
v_and_b32 v24,15,v1
qb 225,194,195
qi 216,176,48
qg 144,4,4096
v_lshlrev_b32 v22,7,v24
qi 216,180,52
q0 5
v_mul_u32_u24 v24,896,v24
qb 226,200,201
qb 227,202,203
buffer_store_dwordx4 v[224:227],v2,s[32:35],s80 offen offset:0 sc1
qi 216,184,56
qg 148,4,5120
v_add3_u32 v24,v24,v25,s89
qi 216,188,60
s_lshr_b32 s55,s88,7
qa 196,9,196
qa 197,9,197
qh 220,160,64
qg 152,4,6144
qa 198,9,198
qi 220,164,68
qa 199,9,199
qa 204,9,204
qi 220,168,72
qg 156,4,7168
qa 205,9,205
qi 220,172,76
qa 206,9,206
qa 207,9,207
qi 220,176,80
qg 160,4,8192
qb 228,196,197
qi 220,180,84
qb 229,198,199
qb 230,204,205
qb 231,206,207
buffer_store_dwordx4 v[228:231],v2,s[32:35],s80 offen offset:2048 sc1
qi 220,184,88
qg 164,4,9216
qa 208,8,208
qi 220,188,92
q0 8
qa 209,8,209
qa 210,8,210
qh 192,128,32
s_mov_b32 m0,s74
qg 168,4,10240
qa 211,8,211
qi 192,132,36
q0 7
qc 2,0
qa 216,8,216
qi 192,136,40
qg 172,4,11264
qa 217,8,217
qi 192,140,44
q0 6
qa 218,8,218
qa 219,8,219
qi 192,144,48
qg 176,4,12288
qb 232,208,209
qi 192,148,52
q0 5
qc 2,1024
qb 233,210,211
qi 192,152,56
qg 180,4,13312
qb 234,216,217
qi 192,156,60
qb 235,218,219
qh 196,128,64
qg 184,4,14336
qi 196,132,68
qc 2,2048
qi 196,136,72
qg 188,4,15360
qi 196,140,76
buffer_store_dwordx4 v[232:235],v2,s[32:35],s80 offen offset:1024 sc1
qa 212,9,212
qi 196,144,80
qg 128,4,16384
qa 213,9,213
qi 196,148,84
qa 214,9,214
qa 215,9,215
qi 196,152,88
qg 132,4,17408
qa 220,9,220
qi 196,156,92
q0 8
qc 2,3072
s_add_u32 m0,s74,4096
qh 200,160,32
qg 136,4,18432
qa 221,9,221
qi 200,164,36
q0 7
qa 222,9,222
qa 223,9,223
qi 200,168,40
qg 140,4,19456
qb 236,212,213
qi 200,172,44
q0 6
qc 3,0
qb 237,214,215
qi 200,176,48
qg 144,4,20480
qb 238,220,221
qi 200,180,52
q0 5
qb 239,222,223
qi 200,184,56
qg 148,4,21504
qi 200,188,60
qc 3,1024
qh 204,160,64
qg 152,4,22528
qi 204,164,68
buffer_store_dwordx4 v[236:239],v2,s[32:35],s80 offen offset:3072 sc1
s_add_u32 s80,s80,16384
qi 204,168,72
qg 156,4,23552
qi 204,172,76
qc 3,2048
qi 204,176,80
qg 160,4,24576
qa 192,8,192
qi 204,180,84
qa 193,8,193
qa 194,8,194
qi 204,184,88
qg 164,4,25600
qa 195,8,195
qi 204,188,92
q0 8
qc 3,3072
s_add_u32 s72,s72,1
qh 208,128,32
qg 168,4,26624
v_readlane_b32 s73,v5,s72
qi 208,132,36
q0 7
v_readlane_b32 s74,v6,s72
v_readlane_b32 s75,v7,s72
qi 208,136,40
qg 172,4,27648
qa 200,8,200
qi 208,140,44
q0 6
qa 201,8,201
qa 202,8,202
qi 208,144,48
qg 176,4,28672
qa 203,8,203
qi 208,148,52
q0 5
qb 224,192,193
qb 225,194,195
qi 208,152,56
qg 180,4,29696
qb 226,200,201
qi 208,156,60
qb 227,202,203
qh 212,128,64
qg 184,4,30720
qi 212,132,68
buffer_store_dwordx4 v[224:227],v2,s[32:35],s80 offen offset:0 sc1
qa 196,9,196
qi 212,136,72
qg 188,4,31744
qa 197,9,197
qi 212,140,76
q3 21
s_barrier
v_add_u32 v4,s75,v2
qi 212,144,80
qg 128,4,0
qa 198,9,198
qi 212,148,84
qa 199,9,199
qa 204,9,204
qi 212,152,88
qg 132,4,1024
qa 205,9,205
qi 212,156,92
q0 8
qa 206,9,206
qa 207,9,207
qh 216,160,32
qg 136,4,2048
qb 228,196,197
qi 216,164,36
q0 7
qb 229,198,199
qb 230,204,205
qi 216,168,40
qg 140,4,3072
qb 231,206,207
qi 216,172,44
q0 6
buffer_store_dwordx4 v[228:231],v2,s[32:35],s80 offen offset:2048 sc1
qi 216,176,48
qg 144,4,4096
qi 216,180,52
q0 5
qi 216,184,56
qg 148,4,5120
qi 216,188,60
qh 220,160,64
qg 152,4,6144
qi 220,164,68
qi 220,168,72
qg 156,4,7168
qi 220,172,76
qi 220,176,80
qg 160,4,8192
qa 208,8,208
qi 220,180,84
qa 209,8,209
qa 210,8,210
qi 220,184,88
qg 164,4,9216
qa 211,8,211
qi 220,188,92
q0 8
qa 216,8,216
qa 217,8,217
qh 192,128,32
s_mov_b32 m0,s74
qg 168,4,10240
qa 218,8,218
qi 192,132,36
q0 7
qc 2,0
qa 219,8,219
qi 192,136,40
qg 172,4,11264
qb 232,208,209
qi 192,140,44
q0 6
qb 233,210,211
qb 234,216,217
qi 192,144,48
qg 176,4,12288
qb 235,218,219
qi 192,148,52
q0 5
qc 2,1024
qi 192,152,56
qg 180,4,13312
qi 192,156,60
buffer_store_dwordx4 v[232:235],v2,s[32:35],s80 offen offset:1024 sc1
qa 212,9,212
qh 196,128,64
qg 184,4,14336
qa 213,9,213
qi 196,132,68
qc 2,2048
qa 214,9,214
qi 196,136,72
qg 188,4,15360
qa 215,9,215
qi 196,140,76
qa 220,9,220
qa 221,9,221
qi 196,144,80
qg 128,4,16384
qa 222,9,222
qi 196,148,84
qa 223,9,223
qb 236,212,213
qi 196,152,88
qg 132,4,17408
qb 237,214,215
qi 196,156,92
q0 8
qc 2,3072
s_add_u32 m0,s74,4096
qh 200,160,32
qg 136,4,18432
qb 238,220,221
qi 200,164,36
q0 7
qb 239,222,223
qi 200,168,40
qg 140,4,19456
qi 200,172,44
q0 6
qc 3,0
qi 200,176,48
qg 144,4,20480
qi 200,180,52
q0 5
buffer_store_dwordx4 v[236:239],v2,s[32:35],s80 offen offset:3072 sc1
s_add_u32 s80,s80,16384
qi 200,184,56
qg 148,4,21504
qi 200,188,60
qc 3,1024
qh 204,160,64
qg 152,4,22528
qi 204,164,68
qi 204,168,72
qg 156,4,23552
qi 204,172,76
qc 3,2048
qi 204,176,80
qg 160,4,24576
qa 192,8,192
qi 204,180,84
qa 193,8,193
qa 194,8,194
qi 204,184,88
qg 164,4,25600
qa 195,8,195
qi 204,188,92
q0 8
qc 3,3072
s_add_u32 s72,s72,1
qh 208,128,32
qg 168,4,26624
v_readlane_b32 s73,v5,s72
qi 208,132,36
q0 7
v_readlane_b32 s74,v6,s72
v_readlane_b32 s75,v7,s72
qi 208,136,40
qg 172,4,27648
qa 200,8,200
qi 208,140,44
q0 6
qa 201,8,201
qa 202,8,202
qi 208,144,48
qg 176,4,28672
qa 203,8,203
qi 208,148,52
q0 5
qb 224,192,193
qb 225,194,195
qi 208,152,56
qg 180,4,29696
qb 226,200,201
qi 208,156,60
qb 227,202,203
qh 212,128,64
qg 184,4,30720
qi 212,132,68
buffer_store_dwordx4 v[224:227],v2,s[32:35],s80 offen offset:0 sc1
qa 196,9,196
qi 212,136,72
qg 188,4,31744
qa 197,9,197
qi 212,140,76
q3 25
s_barrier
v_add_u32 v4,s75,v2
qi 212,144,80
qg 128,4,0
qa 198,9,198
qi 212,148,84
qa 199,9,199
qa 204,9,204
qi 212,152,88
qg 132,4,1024
qa 205,9,205
qi 212,156,92
q0 8
qa 206,9,206
qa 207,9,207
qh 216,160,32
qg 136,4,2048
qb 228,196,197
qi 216,164,36
q0 7
qb 229,198,199
qb 230,204,205
qi 216,168,40
qg 140,4,3072
qb 231,206,207
qi 216,172,44
q0 6
buffer_store_dwordx4 v[228:231],v2,s[32:35],s80 offen offset:2048 sc1
qi 216,176,48
qg 144,4,4096
qi 216,180,52
q0 5
qi 216,184,56
qg 148,4,5120
qi 216,188,60
qh 220,160,64
qg 152,4,6144
qi 220,164,68
qi 220,168,72
qg 156,4,7168
qi 220,172,76
qi 220,176,80
qg 160,4,8192
qa 208,8,208
qi 220,180,84
qa 209,8,209
qa 210,8,210
qi 220,184,88
qg 164,4,9216
qa 211,8,211
qi 220,188,92
q0 8
qa 216,8,216
qa 217,8,217
qh 192,128,32
s_mov_b32 m0,s74
qg 168,4,10240
qa 218,8,218
qi 192,132,36
q0 7
qc 2,0
qa 219,8,219
qi 192,136,40
qg 172,4,11264
qb 232,208,209
qi 192,140,44
q0 6
qb 233,210,211
qb 234,216,217
qi 192,144,48
qg 176,4,12288
qb 235,218,219
qi 192,148,52
q0 5
qc 2,1024
qi 192,152,56
qg 180,4,13312
qi 192,156,60
buffer_store_dwordx4 v[232:235],v2,s[32:35],s80 offen offset:1024 sc1
qa 212,9,212
qh 196,128,64
qg 184,4,14336
qa 213,9,213
qi 196,132,68
qc 2,2048
qa 214,9,214
qi 196,136,72
qg 188,4,15360
qa 215,9,215
qi 196,140,76
qa 220,9,220
qa 221,9,221
qi 196,144,80
qg 128,4,16384
qa 222,9,222
qi 196,148,84
qa 223,9,223
qb 236,212,213
qi 196,152,88
qg 132,4,17408
qb 237,214,215
qi 196,156,92
q0 8
qc 2,3072
s_add_u32 m0,s74,4096
qh 200,160,32
qg 136,4,18432
qb 238,220,221
qi 200,164,36
q0 7
qb 239,222,223
qi 200,168,40
qg 140,4,19456
qi 200,172,44
q0 6
qc 3,0
qi 200,176,48
qg 144,4,20480
qi 200,180,52
q0 5
buffer_store_dwordx4 v[236:239],v2,s[32:35],s80 offen offset:3072 sc1
s_add_u32 s80,s80,16384
qi 200,184,56
qg 148,4,21504
qi 200,188,60
qc 3,1024
qh 204,160,64
qg 152,4,22528
qi 204,164,68
qi 204,168,72
qg 156,4,23552
qi 204,172,76
qc 3,2048
qi 204,176,80
qg 160,4,24576
qa 192,8,192
qi 204,180,84
qa 193,8,193
qa 194,8,194
qi 204,184,88
qg 164,4,25600
qa 195,8,195
qi 204,188,92
q0 8
qc 3,3072
s_add_u32 s72,s72,1
qh 208,128,32
qg 168,4,26624
v_readlane_b32 s73,v5,s72
qi 208,132,36
q0 7
v_readlane_b32 s74,v6,s72
v_readlane_b32 s75,v7,s72
qi 208,136,40
qg 172,4,27648
qa 200,8,200
qi 208,140,44
q0 6
qa 201,8,201
qa 202,8,202
qi 208,144,48
qg 176,4,28672
qa 203,8,203
qi 208,148,52
q0 5
qb 224,192,193
qb 225,194,195
qi 208,152,56
qg 180,4,29696
qb 226,200,201
qi 208,156,60
qb 227,202,203
qh 212,128,64
qg 184,4,30720
qi 212,132,68
buffer_store_dwordx4 v[224:227],v2,s[32:35],s80 offen offset:0 sc1
qa 196,9,196
qi 212,136,72
qg 188,4,31744
qa 197,9,197
qi 212,140,76
q3 25
s_barrier
v_add_u32 v4,s75,v2
qi 212,144,80
qg 128,4,0
qa 198,9,198
qi 212,148,84
qa 199,9,199
qa 204,9,204
qi 212,152,88
qg 132,4,1024
qa 205,9,205
qi 212,156,92
q0 8
qa 206,9,206
qa 207,9,207
qh 216,160,32
qg 136,4,2048
qb 228,196,197
qi 216,164,36
LHH4_3kf:
s_cbranch_execz LHR4kf
q0 7
qb 229,198,199
qb 230,204,205
qi 216,168,40
qg 140,4,3072
qb 231,206,207
qi 216,172,44
q0 6
buffer_store_dwordx4 v[228:231],v2,s[32:35],s80 offen offset:2048 sc1
qi 216,176,48
qg 144,4,4096
qi 216,180,52
q0 5
qi 216,184,56
qg 148,4,5120
qi 216,188,60
qh 220,160,64
qg 152,4,6144
qi 220,164,68
qi 220,168,72
qg 156,4,7168
qi 220,172,76
buffer_load_dwordx4 v[180:183],v25,s[44:47],s55 offen
qi 220,176,80
qg 160,4,8192
qa 208,8,208
qi 220,180,84
qa 209,8,209
qa 210,8,210
qi 220,184,88
qg 164,4,9216
qa 211,8,211
qi 220,188,92
q0 8
qa 216,8,216
qa 217,8,217
qh 192,128,32
s_add_u32 s72,s72,1
qg 168,4,10240
v_readlane_b32 s73,v5,s72
qi 192,132,36
q0 7
v_readlane_b32 s74,v6,s72
v_readlane_b32 s75,v7,s72
qi 192,136,40
qg 172,4,11264
qa 218,8,218
qi 192,140,44
q0 6
qa 219,8,219
qb 232,208,209
qi 192,144,48
qg 176,4,12288
qb 233,210,211
qi 192,148,52
q0 5
qb 234,216,217
qb 235,218,219
qi 192,152,56
qg 180,4,13312
qi 192,156,60
buffer_store_dwordx4 v[232:235],v2,s[32:35],s80 offen offset:1024 sc1
qa 212,9,212
qh 196,128,64
qg 184,4,14336
qa 213,9,213
qi 196,132,68
qa 214,9,214
qa 215,9,215
qi 196,136,72
qg 188,4,15360
qa 220,9,220
qi 196,140,76
qa 221,9,221
qa 222,9,222
qi 196,144,80
qg 128,4,16384
qa 223,9,223
qi 196,148,84
qb 236,212,213
qb 237,214,215
qi 196,152,88
qg 132,4,17408
qb 238,220,221
qi 196,156,92
q0 8
qb 239,222,223
qh 200,160,32
qg 136,4,18432
qi 200,164,36
q0 7
buffer_load_dwordx4 v[100:103],v24,s[36:39],s55 offen
qi 200,168,40
qg 140,4,19456
qi 200,172,44
q0 6
buffer_store_dwordx4 v[236:239],v2,s[32:35],s80 offen offset:3072 sc1
s_add_u32 s80,s80,16384
qi 200,176,48
qg 144,4,20480
qi 200,180,52
q0 5
qi 200,184,56
qg 148,4,21504
qi 200,188,60
qh 204,160,64
qg 152,4,22528
qi 204,164,68
qi 204,168,72
qg 156,4,23552
qi 204,172,76
qi 204,176,80
qg 160,4,24576
qa 192,8,192
qi 204,180,84
qa 193,8,193
qa 194,8,194
qi 204,184,88
qg 164,4,25600
qa 195,8,195
qi 204,188,92
q0 8
qa 200,8,200
qa 201,8,201
qh 208,128,32
qg 168,4,26624
qa 202,8,202
qi 208,132,36
q0 7
qa 203,8,203
qb 224,192,193
qi 208,136,40
qg 172,4,27648
v_add_u32 v3,s88,v2
qi 208,140,44
q0 6
qb 225,194,195
qb 226,200,201
qi 208,144,48
qg 176,4,28672
qb 227,202,203
qi 208,148,52
q0 5
buffer_store_dwordx4 v[224:227],v2,s[32:35],s80 offen offset:0 sc1
qa 196,9,196
qi 208,152,56
qg 180,4,29696
qa 197,9,197
qi 208,156,60
qa 198,9,198
qa 199,9,199
qh 212,128,64
qg 184,4,30720
qa 204,9,204
qi 212,132,68
qa 205,9,205
qa 206,9,206
qi 212,136,72
qg 188,4,31744
qa 207,9,207
qi 212,140,76
q3 19
s_barrier
v_add_u32 v4,s75,v2
qi 212,144,80
qg 128,4,0
qb 228,196,197
qi 212,148,84
qb 229,198,199
qb 230,204,205
qi 212,152,88
qg 132,4,1024
qb 231,206,207
qi 212,156,92
q0 8
buffer_store_dwordx4 v[228:231],v2,s[32:35],s80 offen offset:2048 sc1
qh 216,160,32
qg 136,4,2048
qi 216,164,36
q0 7
qi 216,168,40
qg 140,4,3072
qi 216,172,44
q0 6
qi 216,176,48
qg 144,4,4096
qi 216,180,52
q0 5
qi 216,184,56
qg 148,4,5120
qi 216,188,60
qh 220,160,64
qg 152,4,6144
qi 220,164,68
qi 220,168,72
qg 156,4,7168
qi 220,172,76
qi 220,176,80
qg 160,4,8192
qa 208,8,208
qi 220,180,84
qa 209,8,209
qa 210,8,210
qi 220,184,88
qg 164,4,9216
qa 211,8,211
qi 220,188,92
q0 8
qa 216,8,216
qa 217,8,217
qh 192,128,32
s_add_u32 s72,s72,1
qg 168,4,10240
v_readlane_b32 s73,v5,s72
qi 192,132,36
q0 7
v_readlane_b32 s74,v6,s72
v_readlane_b32 s75,v7,s72
qi 192,136,40
qg 172,4,11264
qa 218,8,218
qi 192,140,44
q0 6
qa 219,8,219
qb 232,208,209
qi 192,144,48
qg 176,4,12288
qb 233,210,211
qi 192,148,52
q0 5
qb 234,216,217
qb 235,218,219
qi 192,152,56
qg 180,4,13312
qi 192,156,60
buffer_store_dwordx4 v[232:235],v2,s[32:35],s80 offen offset:1024 sc1
qa 212,9,212
qh 196,128,64
qg 184,4,14336
qa 213,9,213
qi 196,132,68
qa 214,9,214
qa 215,9,215
qi 196,136,72
qg 188,4,15360
qa 220,9,220
qi 196,140,76
qa 221,9,221
qa 222,9,222
qi 196,144,80
qg 128,4,16384
qa 223,9,223
qi 196,148,84
qb 236,212,213
qb 237,214,215
qi 196,152,88
qg 132,4,17408
qb 238,220,221
qi 196,156,92
q0 8
qb 239,222,223
qh 200,160,32
qg 136,4,18432
qi 200,164,36
q0 7
buffer_store_dwordx4 v[236:239],v2,s[32:35],s80 offen offset:3072 sc1
s_add_u32 s80,s80,16384
qi 200,168,40
qg 140,4,19456
qi 200,172,44
q0 6
qi 200,176,48
qg 144,4,20480
qi 200,180,52
q0 5
qi 200,184,56
qg 148,4,21504
qi 200,188,60
qh 204,160,64
qg 152,4,22528
qi 204,164,68
q3 4
buffer_store_dword v120,v121,s[32:35],s4 offen sc1
qi 204,168,72
qg 156,4,23552
qi 204,172,76
qi 204,176,80
qg 160,4,24576
qa 192,8,192
qi 204,180,84
qa 193,8,193
qa 194,8,194
qi 204,184,88
qg 164,4,25600
qa 195,8,195
qi 204,188,92
q0 8
qa 200,8,200
qa 201,8,201
qh 208,128,32
qg 168,4,26624
qa 202,8,202
qi 208,132,36
q0 7
qa 203,8,203
qb 224,192,193
qi 208,136,40
qg 172,4,27648
qb 225,194,195
qi 208,140,44
q0 6
qb 226,200,201
qb 227,202,203
qi 208,144,48
qg 176,4,28672
qi 208,148,52
q0 5
buffer_store_dwordx4 v[224:227],v2,s[32:35],s80 offen offset:0 sc1
qa 196,9,196
qi 208,152,56
qg 180,4,29696
qa 197,9,197
qi 208,156,60
qa 198,9,198
qa 199,9,199
qh 212,128,64
qg 184,4,30720
qa 204,9,204
qi 212,132,68
qa 205,9,205
qa 206,9,206
qi 212,136,72
qg 188,4,31744
qa 207,9,207
qi 212,140,76
q3 12
s_barrier
v_add_u32 v4,s75,v2
qi 212,144,80
qg 128,4,0
qb 228,196,197
qi 212,148,84
qb 229,198,199
qb 230,204,205
qi 212,152,88
qg 132,4,1024
qb 231,206,207
qi 212,156,92
q0 8
buffer_store_dwordx4 v[228:231],v2,s[32:35],s80 offen offset:2048 sc1
qh 216,160,32
qg 136,4,2048
qi 216,164,36
q0 7
qi 216,168,40
qg 140,4,3072
qi 216,172,44
q0 6
qi 216,176,48
qg 144,4,4096
qi 216,180,52
q0 5
qi 216,184,56
qg 148,4,5120
qi 216,188,60
qh 220,160,64
qg 152,4,6144
qi 220,164,68
qi 220,168,72
qg 156,4,7168
qi 220,172,76
qi 220,176,80
qg 160,4,8192
qa 208,8,208
qi 220,180,84
qa 209,8,209
qa 210,8,210
qi 220,184,88
qg 164,4,9216
qa 211,8,211
qi 220,188,92
q0 8
qa 216,8,216
qa 217,8,217
qh 192,128,32
s_add_u32 s72,s72,1
qg 168,4,10240
v_readlane_b32 s73,v5,s72
qi 192,132,36
q0 7
v_readlane_b32 s74,v6,s72
v_readlane_b32 s75,v7,s72
qi 192,136,40
qg 172,4,11264
qa 218,8,218
qi 192,140,44
q0 6
qa 219,8,219
qb 232,208,209
qi 192,144,48
qg 176,4,12288
qb 233,210,211
qi 192,148,52
q0 5
qb 234,216,217
qb 235,218,219
qi 192,152,56
qg 180,4,13312
qi 192,156,60
buffer_store_dwordx4 v[232:235],v2,s[32:35],s80 offen offset:1024 sc1
qa 212,9,212
qh 196,128,64
qg 184,4,14336
qa 213,9,213
qi 196,132,68
qa 214,9,214
qa 215,9,215
qi 196,136,72
qg 188,4,15360
qa 220,9,220
qi 196,140,76
qa 221,9,221
qa 222,9,222
qi 196,144,80
qg 128,4,16384
qa 223,9,223
qi 196,148,84
qb 236,212,213
qb 237,214,215
qi 196,152,88
qg 132,4,17408
qb 238,220,221
qi 196,156,92
q0 8
qb 239,222,223
qh 200,160,32
qg 136,4,18432
qi 200,164,36
q0 7
buffer_store_dwordx4 v[236:239],v2,s[32:35],s80 offen offset:3072 sc1
s_add_u32 s80,s80,16384
qi 200,168,40
qg 140,4,19456
qi 200,172,44
q0 6
qi 200,176,48
qg 144,4,20480
qi 200,180,52
q0 5
qi 200,184,56
qg 148,4,21504
qi 200,188,60
qh 204,160,64
qg 152,4,22528
qi 204,164,68
qi 204,168,72
qg 156,4,23552
qi 204,172,76
qi 204,176,80
qg 160,4,24576
qa 192,8,192
qi 204,180,84
qa 193,8,193
qa 194,8,194
qi 204,184,88
qg 164,4,25600
qa 195,8,195
qi 204,188,92
q0 8
qa 200,8,200
qa 201,8,201
qh 208,128,32
qg 168,4,26624
qa 202,8,202
qi 208,132,36
q0 7
qa 203,8,203
qb 224,192,193
qi 208,136,40
qg 172,4,27648
qb 225,194,195
qi 208,140,44
q0 6
qb 226,200,201
qb 227,202,203
qi 208,144,48
qg 176,4,28672
qi 208,148,52
q0 5
buffer_store_dwordx4 v[224:227],v2,s[32:35],s80 offen offset:0 sc1
qa 196,9,196
qi 208,152,56
qg 180,4,29696
qa 197,9,197
qi 208,156,60
qa 198,9,198
qa 199,9,199
qh 212,128,64
qg 184,4,30720
qa 204,9,204
qi 212,132,68
qa 205,9,205
qa 206,9,206
qi 212,136,72
qg 188,4,31744
qa 207,9,207
qi 212,140,76
qb 228,196,197
qb 229,198,199
qi 212,144,80
qb 230,204,205
qb 231,206,207
qi 212,148,84
buffer_store_dwordx4 v[228:231],v2,s[32:35],s80 offen offset:2048 sc1
qi 212,152,88
qi 212,156,92
q0 6
qh 216,160,32
qi 216,164,36
q0 4
qi 216,168,40
qi 216,172,44
q0 2
qi 216,176,48
qi 216,180,52
q0 0
qi 216,184,56
qi 216,188,60
qh 220,160,64
qi 220,164,68
qi 220,168,72
qi 220,172,76
qi 220,176,80
qa 208,8,208
qa 209,8,209
qi 220,180,84
qa 210,8,210
qa 211,8,211
qi 220,184,88
qa 216,8,216
qa 217,8,217
qi 220,188,92
qa 218,8,218
qa 219,8,219
qb 232,208,209
qb 233,210,211
qb 234,216,217
qb 235,218,219
buffer_store_dwordx4 v[232:235],v2,s[32:35],s80 offen offset:1024 sc1
s_nop 7
qa 212,9,212
qa 213,9,213
qa 214,9,214
qa 215,9,215
qa 220,9,220
qa 221,9,221
qa 222,9,222
qa 223,9,223
qb 236,212,213
qb 237,214,215
qb 238,220,221
qb 239,222,223
buffer_store_dwordx4 v[236:239],v2,s[32:35],s80 offen offset:3072 sc1
s_add_u32 s80,s80,16384
q3 9
buffer_store_dword v120,v121,s[32:35],s5 offen sc1
q3 5
buffer_store_dword v120,v121,s[32:35],s8 offen sc1
q3 16
qp 112,18,224
qo 112,18,228
qo 112,18,232
qo 112,18,236
qo 112,18,240
qo 112,18,244
qo 112,18,248
qo 112,18,252
qq 136,18,180
qp 116,16,224
qo 116,16,228
qo 116,16,232
qo 116,16,236
qo 116,16,240
qo 116,16,244
qo 116,16,248
qo 116,16,252
qq 140,16,180
qq 152,18,100
qq 156,16,100
s_nop 5
v_fmamk_f32 v112,v112,0x42f5fdf0,v136
v_fmamk_f32 v113,v113,0x42f5fdf0,v137
v_fmamk_f32 v114,v114,0x42f5fdf0,v138
v_fmamk_f32 v115,v115,0x42f5fdf0,v139
v_fmamk_f32 v116,v116,0x42f5fdf0,v140
v_fmamk_f32 v117,v117,0x42f5fdf0,v141
v_fmamk_f32 v118,v118,0x42f5fdf0,v142
v_fmamk_f32 v119,v119,0x42f5fdf0,v143
v_mul_f32 v136,0xbfb8aa3b,v112
v_mul_f32 v137,0xbfb8aa3b,v113
v_mul_f32 v138,0xbfb8aa3b,v114
v_mul_f32 v139,0xbfb8aa3b,v115
v_mul_f32 v140,0xbfb8aa3b,v116
v_mul_f32 v141,0xbfb8aa3b,v117
LHH5_0kf:
s_cbranch_execz LHR5kf
v_mul_f32 v142,0xbfb8aa3b,v118
v_mul_f32 v143,0xbfb8aa3b,v119
qm 136,136
qm 137,137
qm 138,138
qm 139,139
qm 140,140
qm 141,141
qm 142,142
qm 143,143
v_add_f32 v136,1.0,v136
v_add_f32 v137,1.0,v137
v_add_f32 v138,1.0,v138
v_add_f32 v139,1.0,v139
v_add_f32 v140,1.0,v140
v_add_f32 v141,1.0,v141
v_add_f32 v142,1.0,v142
v_add_f32 v143,1.0,v143
)"
R"(v_rcp_f32 v136,v136
v_rcp_f32 v137,v137
v_rcp_f32 v138,v138
v_rcp_f32 v139,v139
v_rcp_f32 v140,v140
v_rcp_f32 v141,v141
v_rcp_f32 v142,v142
v_rcp_f32 v143,v143
qa 112,112,136
qa 113,113,137
qa 114,114,138
qa 115,115,139
qa 116,116,140
qa 117,117,141
qa 118,118,142
qa 119,119,143
q3 2
buffer_store_dword v120,v121,s[32:35],s93 offen sc1
s_cmp_eq_u32 s31,3
s_cbranch_scc1 Le29kf
s_mov_b32 s85,200000
Lpoll30kf:
buffer_load_dword v23,v22,s[32:35],s11 offen sc1
q3 0
v_cmp_ne_u32 vcc,s81,v23
s_nop 0
s_cbranch_vccz Lpdone31kf
s_sleep 2
s_sub_u32 s85,s85,1
s_cmp_lg_u32 s85,0
s_cbranch_scc1 Lpoll30kf
Lpdone31kf:
s_mov_b32 s48,s98
buffer_load_dwordx4 a[64:67],v3,s[32:35],s48 offen sc1
s_add_u32 s48,s48,114688
buffer_load_dwordx4 a[68:71],v3,s[32:35],s48 offen sc1
s_add_u32 s48,s48,114688
buffer_load_dwordx4 a[72:75],v3,s[32:35],s48 offen sc1
s_add_u32 s48,s48,114688
buffer_load_dwordx4 a[76:79],v3,s[32:35],s48 offen sc1
s_add_u32 s48,s48,114688
buffer_load_dwordx4 a[80:83],v3,s[32:35],s48 offen sc1
s_add_u32 s48,s48,114688
buffer_load_dwordx4 a[84:87],v3,s[32:35],s48 offen sc1
s_add_u32 s48,s48,114688
buffer_load_dwordx4 a[88:91],v3,s[32:35],s48 offen sc1
s_add_u32 s48,s48,114688
buffer_load_dwordx4 a[92:95],v3,s[32:35],s48 offen sc1
s_add_u32 s48,s48,114688
buffer_load_dwordx4 a[96:99],v3,s[32:35],s48 offen sc1
s_add_u32 s48,s48,114688
buffer_load_dwordx4 a[100:103],v3,s[32:35],s48 offen sc1
s_add_u32 s48,s48,114688
buffer_load_dwordx4 a[104:107],v3,s[32:35],s48 offen sc1
s_add_u32 s48,s48,114688
buffer_load_dwordx4 a[108:111],v3,s[32:35],s48 offen sc1
s_add_u32 s48,s48,114688
buffer_load_dwordx4 a[112:115],v3,s[32:35],s48 offen sc1
s_add_u32 s48,s48,114688
buffer_load_dwordx4 a[116:119],v3,s[32:35],s48 offen sc1
s_add_u32 s48,s48,114688
buffer_load_dwordx4 a[120:123],v3,s[32:35],s48 offen sc1
s_add_u32 s48,s48,114688
buffer_load_dwordx4 a[124:127],v3,s[32:35],s48 offen sc1
q3 15
qp 168,18,64
qp 172,16,64
q3 14
qo 168,18,68
qo 172,16,68
q3 13
qo 168,18,72
qo 172,16,72
q3 12
qo 168,18,76
qo 172,16,76
q3 11
qo 168,18,80
qo 172,16,80
q3 10
qo 168,18,84
qo 172,16,84
q3 9
qo 168,18,88
qo 172,16,88
q3 8
qo 168,18,92
qo 172,16,92
q3 7
qo 168,18,96
qo 172,16,96
q3 6
qo 168,18,100
qo 172,16,100
q3 5
qo 168,18,104
qo 172,16,104
q3 4
qo 168,18,108
qo 172,16,108
q3 3
qo 168,18,112
qo 172,16,112
q3 2
qo 168,18,116
qo 172,16,116
q3 1
qo 168,18,120
qo 172,16,120
q3 0
qo 168,18,124
qo 172,16,124
s_nop 7
v_fma_f32 v168,v168,v112,v152
v_fma_f32 v169,v169,v113,v153
v_fma_f32 v170,v170,v114,v154
v_fma_f32 v171,v171,v115,v155
v_fma_f32 v172,v172,v116,v156
v_fma_f32 v173,v173,v117,v157
v_fma_f32 v174,v174,v118,v158
v_fma_f32 v175,v175,v119,v159
qb 168,168,169
qb 169,170,171
qb 170,172,173
qb 171,174,175
buffer_store_dwordx4 v[168:171],v24,s[40:43],s55 offen
Le29kf:
s_branch Lfin3kf
RESCkf:
s_nop 7
v_max3_f32 v120,v32,v33,v34
v_max3_f32 v120,v120,v35,v40
v_max3_f32 v120,v120,v41,v42
v_max3_f32 v120,v120,v43,v48
v_max3_f32 v120,v120,v49,v50
v_max3_f32 v120,v120,v51,v56
v_max3_f32 v120,v120,v57,v58
v_max_f32 v120,v120,v59
v_mov_b32 v122,v120
s_nop 1
v_permlane32_swap_b32 v120,v122
s_nop 1
v_max_f32 v120,v120,v122
v_mov_b32 v122,v120
s_nop 1
v_permlane16_swap_b32 v120,v122
s_nop 1
v_max_f32 v120,v120,v122
v_max3_f32 v121,v36,v37,v38
v_max3_f32 v121,v121,v39,v44
v_max3_f32 v121,v121,v45,v46
v_max3_f32 v121,v121,v47,v52
v_max3_f32 v121,v121,v53,v54
v_max3_f32 v121,v121,v55,v60
v_max3_f32 v121,v121,v61,v62
v_max_f32 v121,v121,v63
v_mov_b32 v122,v121
s_nop 1
v_permlane32_swap_b32 v121,v122
s_nop 1
v_max_f32 v121,v121,v122
v_mov_b32 v122,v121
s_nop 1
v_permlane16_swap_b32 v121,v122
s_nop 1
v_max_f32 v121,v121,v122
v_max_f32 v120,0,v120
v_exp_f32 v124,-v120
s_nop 0
v_mov_b32 v125,v124
qa 8,8,124
v_pk_mul_f32 v[128:129],v[128:129],v[124:125]
v_pk_mul_f32 v[130:131],v[130:131],v[124:125]
v_pk_mul_f32 v[132:133],v[132:133],v[124:125]
v_pk_mul_f32 v[134:135],v[134:135],v[124:125]
v_pk_mul_f32 v[136:137],v[136:137],v[124:125]
v_pk_mul_f32 v[138:139],v[138:139],v[124:125]
v_pk_mul_f32 v[140:141],v[140:141],v[124:125]
v_pk_mul_f32 v[142:143],v[142:143],v[124:125]
v_pk_mul_f32 v[144:145],v[144:145],v[124:125]
v_pk_mul_f32 v[146:147],v[146:147],v[124:125]
v_pk_mul_f32 v[148:149],v[148:149],v[124:125]
v_pk_mul_f32 v[150:151],v[150:151],v[124:125]
v_pk_mul_f32 v[152:153],v[152:153],v[124:125]
v_pk_mul_f32 v[154:155],v[154:155],v[124:125]
v_pk_mul_f32 v[156:157],v[156:157],v[124:125]
v_pk_mul_f32 v[158:159],v[158:159],v[124:125]
v_pk_mul_f32 v[160:161],v[160:161],v[124:125]
v_pk_mul_f32 v[162:163],v[162:163],v[124:125]
v_pk_mul_f32 v[164:165],v[164:165],v[124:125]
v_pk_mul_f32 v[166:167],v[166:167],v[124:125]
v_pk_mul_f32 v[168:169],v[168:169],v[124:125]
v_pk_mul_f32 v[170:171],v[170:171],v[124:125]
v_pk_mul_f32 v[172:173],v[172:173],v[124:125]
v_pk_mul_f32 v[174:175],v[174:175],v[124:125]
v_pk_mul_f32 v[176:177],v[176:177],v[124:125]
v_pk_mul_f32 v[178:179],v[178:179],v[124:125]
v_pk_mul_f32 v[180:181],v[180:181],v[124:125]
v_pk_mul_f32 v[182:183],v[182:183],v[124:125]
v_pk_mul_f32 v[184:185],v[184:185],v[124:125]
v_pk_mul_f32 v[186:187],v[186:187],v[124:125]
v_pk_mul_f32 v[188:189],v[188:189],v[124:125]
v_pk_mul_f32 v[190:191],v[190:191],v[124:125]
v_max_f32 v121,0,v121
v_exp_f32 v124,-v121
s_nop 0
v_mov_b32 v125,v124
qa 9,9,124
v_pk_mul_f32 v[192:193],v[192:193],v[124:125]
v_pk_mul_f32 v[194:195],v[194:195],v[124:125]
v_pk_mul_f32 v[196:197],v[196:197],v[124:125]
v_pk_mul_f32 v[198:199],v[198:199],v[124:125]
v_pk_mul_f32 v[200:201],v[200:201],v[124:125]
v_pk_mul_f32 v[202:203],v[202:203],v[124:125]
v_pk_mul_f32 v[204:205],v[204:205],v[124:125]
v_pk_mul_f32 v[206:207],v[206:207],v[124:125]
v_pk_mul_f32 v[208:209],v[208:209],v[124:125]
v_pk_mul_f32 v[210:211],v[210:211],v[124:125]
v_pk_mul_f32 v[212:213],v[212:213],v[124:125]
v_pk_mul_f32 v[214:215],v[214:215],v[124:125]
v_pk_mul_f32 v[216:217],v[216:217],v[124:125]
v_pk_mul_f32 v[218:219],v[218:219],v[124:125]
v_pk_mul_f32 v[220:221],v[220:221],v[124:125]
v_pk_mul_f32 v[222:223],v[222:223],v[124:125]
v_pk_mul_f32 v[224:225],v[224:225],v[124:125]
v_pk_mul_f32 v[226:227],v[226:227],v[124:125]
v_pk_mul_f32 v[228:229],v[228:229],v[124:125]
v_pk_mul_f32 v[230:231],v[230:231],v[124:125]
v_pk_mul_f32 v[232:233],v[232:233],v[124:125]
v_pk_mul_f32 v[234:235],v[234:235],v[124:125]
v_pk_mul_f32 v[236:237],v[236:237],v[124:125]
v_pk_mul_f32 v[238:239],v[238:239],v[124:125]
v_pk_mul_f32 v[240:241],v[240:241],v[124:125]
v_pk_mul_f32 v[242:243],v[242:243],v[124:125]
v_pk_mul_f32 v[244:245],v[244:245],v[124:125]
v_pk_mul_f32 v[246:247],v[246:247],v[124:125]
v_pk_mul_f32 v[248:249],v[248:249],v[124:125]
v_pk_mul_f32 v[250:251],v[250:251],v[124:125]
v_pk_mul_f32 v[252:253],v[252:253],v[124:125]
v_pk_mul_f32 v[254:255],v[254:255],v[124:125]
q9 12,12,120
v_sub_f32 v112,0,v12
v_sub_f32 v113,0,v12
v_sub_f32 v114,0,v12
v_sub_f32 v115,0,v12
v_sub_f32 v32,v32,v120
v_sub_f32 v33,v33,v120
v_sub_f32 v34,v34,v120
v_sub_f32 v35,v35,v120
v_sub_f32 v40,v40,v120
v_sub_f32 v41,v41,v120
v_sub_f32 v42,v42,v120
v_sub_f32 v43,v43,v120
v_sub_f32 v48,v48,v120
v_sub_f32 v49,v49,v120
v_sub_f32 v50,v50,v120
v_sub_f32 v51,v51,v120
v_sub_f32 v56,v56,v120
v_sub_f32 v57,v57,v120
v_sub_f32 v58,v58,v120
v_sub_f32 v59,v59,v120
qm 64,32
qm 65,33
qm 66,34
qm 67,35
qm 72,40
qm 73,41
qm 74,42
qm 75,43
qm 80,48
qm 81,49
qm 82,50
qm 83,51
qm 88,56
qm 89,57
qm 90,58
qm 91,59
q9 10,64,65
q9 10,10,66
q9 10,10,67
q9 10,10,72
q9 10,10,73
q9 10,10,74
q9 10,10,75
q9 10,10,80
q9 10,10,81
q9 10,10,82
q9 10,10,83
q9 10,10,88
q9 10,10,89
q9 10,10,90
q9 10,10,91
q9 13,13,121
v_sub_f32 v116,0,v13
v_sub_f32 v117,0,v13
v_sub_f32 v118,0,v13
v_sub_f32 v119,0,v13
v_sub_f32 v36,v36,v121
v_sub_f32 v37,v37,v121
v_sub_f32 v38,v38,v121
v_sub_f32 v39,v39,v121
v_sub_f32 v44,v44,v121
v_sub_f32 v45,v45,v121
v_sub_f32 v46,v46,v121
v_sub_f32 v47,v47,v121
v_sub_f32 v52,v52,v121
v_sub_f32 v53,v53,v121
v_sub_f32 v54,v54,v121
v_sub_f32 v55,v55,v121
v_sub_f32 v60,v60,v121
v_sub_f32 v61,v61,v121
v_sub_f32 v62,v62,v121
v_sub_f32 v63,v63,v121
qm 68,36
qm 69,37
qm 70,38
qm 71,39
qm 76,44
qm 77,45
qm 78,46
qm 79,47
qm 84,52
qm 85,53
qm 86,54
qm 87,55
qm 92,60
qm 93,61
qm 94,62
qm 95,63
q9 11,68,69
q9 11,11,70
q9 11,11,71
q9 11,11,76
q9 11,11,77
q9 11,11,78
q9 11,11,79
q9 11,11,84
q9 11,11,85
q9 11,11,86
q9 11,11,87
q9 11,11,92
q9 11,11,93
q9 11,11,94
q9 11,11,95
s_nop 1
s_setpc_b64 s[64:65]
Lslow5kf_nb:
s_mov_b32 s85,200000
Lsl32kf:
s_sleep 2
buffer_load_dwordx4 v[80:83],v2,s[32:35],s93 offen sc1
q3 0
v_xor_b32 v80,s81,v80
v_xor_b32 v81,s81,v81
v_xor_b32 v82,s81,v82
v_xor_b32 v83,s81,v83
v_or3_b32 v80,v80,v81,v82
v_or_b32 v80,v80,v83
v_cmp_ne_u32 vcc,0,v80
s_nop 0
s_cbranch_vccz Lback6kf_nb
s_sub_u32 s85,s85,1
s_cmp_lg_u32 s85,0
s_cbranch_scc1 Lsl32kf
s_branch Lback6kf_nb
Lslow7kf_nb:
s_mov_b32 s85,200000
Lsl33kf:
s_sleep 2
buffer_load_dword v18,v19,s[32:35],s92 offen sc1
q3 0
v_cmp_ne_u32 vcc,s81,v18
s_nop 0
s_cbranch_vccz Lback8kf_nb
s_sub_u32 s85,s85,1
s_cmp_lg_u32 s85,0
s_cbranch_scc1 Lsl33kf
s_branch Lback8kf_nb
Lslow9kf_nb:
s_mov_b32 s85,200000
Lsl34kf:
s_sleep 2
buffer_load_dword v20,v21,s[32:35],s93 offen sc1
q3 0
v_cmp_ne_u32 vcc,s81,v20
s_nop 0
s_cbranch_vccz Lback10kf_nb
s_sub_u32 s85,s85,1
s_cmp_lg_u32 s85,0
s_cbranch_scc1 Lsl34kf
s_branch Lback10kf_nb
Lslow11kf_nb:
s_mov_b32 s85,200000
Lsl35kf:
s_sleep 2
buffer_load_dword v125,v124,s[32:35],s94 offen sc1
q3 0
v_cmp_ne_u32 vcc,s81,v125
s_nop 0
s_cbranch_vccz Lback12kf_nb
s_sub_u32 s85,s85,1
s_cmp_lg_u32 s85,0
s_cbranch_scc1 Lsl35kf
s_branch Lback12kf_nb
Lslow26kf_nb:
s_mov_b32 s85,200000
Lsl36kf:
s_sleep 2
buffer_load_dword v123,v121,s[32:35],s93 offen sc1
q3 0
v_cmp_ne_u32 vcc,s81,v123
s_nop 0
s_cbranch_vccz Lback27kf_nb
s_sub_u32 s85,s85,1
s_cmp_lg_u32 s85,0
s_cbranch_scc1 Lsl36kf
s_branch Lback27kf_nb
Lag13kf_rs_nb:
s_swappc_b64 s[64:65],s[66:67]
s_branch Lag13kf_ok_nb
Lag14kf_rs_nb:
s_swappc_b64 s[64:65],s[66:67]
s_branch Lag14kf_ok_nb
Lag15kf_rs_nb:
s_swappc_b64 s[64:65],s[66:67]
s_branch Lag15kf_ok_nb
Lag16kf_rs_nb:
s_swappc_b64 s[64:65],s[66:67]
s_branch Lag16kf_ok_nb
Lit23kf_rs_nb:
s_swappc_b64 s[64:65],s[66:67]
s_branch Lit23kf_ok_nb
Lai24kf_rs_nb:
s_swappc_b64 s[64:65],s[66:67]
s_branch Lai24kf_ok_nb
Ld025kf_rs_nb:
s_swappc_b64 s[64:65],s[66:67]
s_branch Ld025kf_ok_nb
Ld128kf_rs_nb:
s_swappc_b64 s[64:65],s[66:67]
v_mov_b32 v10,0
s_branch Ld128kf_ok_nb
LroleB2kf:
LHH6_0kf:
s_cbranch_execz LHRPkf
q0 0
q6 0,96,128
s_add_u32 m0,s88,65536
s_xor_b32 s86,s12,128
s_add_u32 s86,s86,s64
q7 0,100,132
q5 104,16,2048
q6 4,96,136
q5 108,17,2048
q7 4,100,140
q2 8,0
q6 8,96,144
q7 8,100,148
q2 9,1024
q6 12,96,152
q7 12,100,156
q6 16,96,160
q2 10,2048
q7 16,100,164
q6 20,96,168
q7 20,100,172
s_xor_b32 s87,s13,1048576
q2 11,3072
s_add_u32 s87,s87,s65
q6 24,96,176
s_add_u32 m0,s89,65536
q7 24,100,180
q6 28,96,184
q1 12,0
q7 28,100,188
q0 0
q6 60,104,184
q3 12
s_barrier
q7 60,108,188
q5 112,16,4096
q6 56,104,176
q5 116,17,4096
q7 56,108,180
q4 192,20,16384
q6 52,104,168
q4 194,21,16640
q7 52,108,172
s_mov_b64 exec,0
s_cmp_eq_u32 s31,0
s_cbranch_scc1 LHH7_0kf
s_cmp_eq_u32 s31,1
s_cbranch_scc1 LHH7_1kf
s_cmp_eq_u32 s31,2
s_cbranch_scc1 LHH7_2kf
s_branch LHH7_3kf
LHR7kf:
s_mov_b64 exec,-1
q4 196,20,20480
q6 48,104,160
q4 198,21,20736
q7 48,108,164
q4 200,21,16384
q6 44,104,152
q4 202,20,16640
q7 44,108,156
q4 204,21,20480
q6 40,104,144
q4 206,20,20736
q7 40,108,148
q1 13,1024
q6 36,104,136
q1 14,2048
q7 36,108,140
s_xor_b32 s86,s12,131200
q1 15,3072
s_add_u32 s86,s86,s64
q6 32,104,128
s_add_u32 m0,s88,98304
v_mov_b32 v72,32768
q7 32,108,132
q0 8
q2 8,0
q6 64,112,128
q2 9,1024
q7 64,116,132
q5 120,16,6144
q6 68,112,136
q5 124,17,6144
q7 68,116,140
q4 208,20,16896
q6 72,112,144
q4 210,21,17152
q7 72,116,148
q4 212,20,20992
q6 76,112,152
q4 214,21,21248
q7 76,116,156
q4 216,21,16896
q6 80,112,160
q4 218,20,17152
q7 80,116,164
q4 220,21,20992
q6 84,112,168
q4 222,20,21248
q7 84,116,172
q2 10,2048
q6 88,112,176
s_xor_b32 s87,s13,1048704
q2 11,3072
s_add_u32 s87,s87,s65
q7 88,116,180
s_add_u32 m0,s89,98304
buffer_load_dword v79,v72,s[32:35],0 offen sc1
s_lshl_b32 s48,s30,2
q6 92,112,184
s_add_u32 s48,s48,s31
q1 12,0
q1 13,1024
q1 14,2048
q1 15,3072
q7 92,116,188
q0 8
s_lshl_b32 s48,s48,1
v_lshrrev_b32 v72,5,v1
q6 124,120,184
s_waitcnt vmcnt(17) lgkmcnt(0)
s_barrier
v_add_u32 v72,s48,v72
q7 124,124,188
q5 96,16,32768
v_and_b32 v73,31,v1
q6 120,120,176
q5 100,17,32768
v_cmp_gt_u32 s[96:97],28,v73
q7 120,124,180
q4 224,20,49152
v_mul_u32_u24 v76,896,v72
q6 116,120,168
s_xor_b32 s86,s12,256
q4 226,21,49408
s_add_u32 s86,s86,s64
q7 116,124,172
s_add_u32 m0,s88,0
q4 228,20,53248
v_lshl_add_u32 v76,v73,4,v76
q6 112,120,160
q4 230,21,53504
v_mov_b32 v75,1073741824
q7 112,124,164
s_mov_b32 s44,s4
q2 8,0
v_cndmask_b32 v76,v75,v76,s[96:97]
q6 108,120,152
s_mov_b32 s47,0x20000
s_mov_b32 s46,1835008
s_and_b32 s45,s5,0xffff
q7 108,124,156
buffer_load_dwordx4 v[32:35],v76,s[44:47],0 offen
q6 104,120,144
s_lshl_b32 s49,s31,1
buffer_load_dwordx4 v[36:39],v76,s[44:47],0 offen offset:448
v_lshrrev_b32 v77,5,v1
q7 104,124,148
v_add3_u32 v77,v77,v73,s49
v_and_b32 v72,7,v77
q6 100,120,136
v_lshlrev_b32 v72,4,v72
v_lshl_add_u32 v72,v73,7,v72
q7 100,124,140
v_add_u32 v30,135168,v72
v_add_u32 v73,28,v73
q6 96,120,128
q2 9,1024
v_add_u32 v77,4,v77
q7 96,124,132
q0 4
v_and_b32 v77,7,v77
v_lshlrev_b32 v77,4,v77
q6 128,96,128
v_lshrrev_b32 v72,5,v73
v_and_b32 v73,31,v73
q7 128,100,132
q5 104,16,34816
v_lshl_add_u32 v77,v72,13,v77
q6 132,96,136
q5 108,17,34816
v_lshl_add_u32 v77,v73,7,v77
q7 132,100,140
q4 232,21,49152
v_add_u32 v31,135168,v77
q6 136,96,144
q4 234,20,49408
v_cndmask_b32 v30,v31,v30,s[96:97]
q7 136,100,148
q4 236,21,53248
v_lshrrev_b32 v72,5,v1
q6 140,96,152
q4 238,20,53504
v_lshlrev_b32 v72,10,v72
q7 140,100,156
q2 10,2048
v_bfe_u32 v73,v1,3,2
q6 144,96,160
v_lshl_add_u32 v72,v73,8,v72
v_lshrrev_b32 v73,3,v1
q7 144,100,164
v_sub_u32 v73,v1,v73
v_and_b32 v73,7,v73
q6 148,96,168
s_lshr_b32 s49,s30,1
s_mul_i32 s49,s49,14336
v_lshl_add_u32 v72,v73,4,v72
q7 148,100,172
s_and_b32 s50,s30,1
s_lshl_b32 s50,s50,7
s_add_u32 s49,s49,s50
q6 152,96,176
s_xor_b32 s87,s13,1572864
q2 11,3072
s_add_u32 s87,s87,s65
q7 152,100,180
s_lshl_b32 s50,s31,12
s_add_u32 s49,s49,s50
s_add_u32 m0,s89,0
q6 156,96,184
s_lshr_b32 s48,s30,3
s_lshl_b32 s48,s48,2
v_add_u32 v77,s49,v72
q7 156,100,188
q0 4
s_lshl_b32 s49,s71,9
s_add_u32 s48,s48,s31
q6 188,104,184
s_cmp_eq_u32 s48,127
s_cselect_b32 s49,s49,536870912
v_lshl_add_u32 v78,v1,3,s49
q7 188,108,188
LHH6_1kf:
s_cbranch_execz LHRPkf
s_mov_b32 s44,s18
q5 112,16,36864
s_and_b32 s45,s19,0xffff
q6 184,104,176
s_mov_b32 s46,3584
q5 116,17,36864
s_mov_b32 s47,0x20000
q7 184,108,180
q4 240,20,49664
q6 180,104,168
q4 242,21,49920
q7 180,108,172
q4 244,20,53760
q6 176,104,160
q4 246,21,54016
q7 176,108,164
q1 12,0
q6 172,104,152
buffer_load_dwordx2 v[80:81],v78,s[44:47],0 offen
v_lshlrev_b32 v78,1,v78
q7 172,108,156
v_bfe_u32 v86,v1,2,3
v_lshrrev_b32 v72,5,v1
q6 168,104,144
v_and_b32 v73,3,v1
v_lshlrev_b32 v73,4,v73
q7 168,108,148
s_lshl_b32 s48,s31,13
q1 13,1024
v_lshl_add_u32 v87,v72,6,v73
q6 164,104,136
s_add_u32 s48,s48,131072
v_lshrrev_b32 v72,4,v1
v_lshlrev_b32 v72,10,v72
q7 164,108,140
v_bfe_u32 v73,v1,2,2
v_lshl_add_u32 v72,v73,6,v72
q6 160,104,128
s_mov_b64 exec,0
s_cmp_eq_u32 s31,0
s_cbranch_scc1 LHH8_0kf
s_cmp_eq_u32 s31,1
s_cbranch_scc1 LHH8_1kf
s_cmp_eq_u32 s31,2
s_cbranch_scc1 LHH8_2kf
s_branch LHH8_3kf
LHR8kf:
s_mov_b64 exec,-1
v_and_b32 v73,3,v1
v_lshl_add_u32 v72,v73,3,v72
q7 160,108,132
q0 4
v_add_u32 v84,s48,v72
v_lshrrev_b32 v72,5,v1
q6 192,112,128
v_lshlrev_b32 v72,10,v72
v_lshl_add_u32 v72,v86,6,v72
q7 192,116,132
q5 120,16,38912
v_lshl_add_u32 v72,v73,4,v72
q6 196,112,136
s_lshl_b32 s82,s31,13
q5 124,17,38912
v_add_u32 v85,s48,v72
q7 196,116,140
s_add_u32 s82,s82,131072
q4 248,21,49664
s_lshr_b32 s48,s30,3
q6 200,112,144
s_lshl2_add_u32 s48,s48,s31
q4 250,20,49920
q7 200,116,148
q4 252,21,53760
q6 204,112,152
q4 254,20,54016
q7 204,116,156
q1 14,2048
q6 208,112,160
s_cmp_ge_u32 s48,56
s_cselect_b32 s49,1,0
q7 208,116,164
s_mul_i32 s50,s49,56
q1 15,3072
q6 212,112,168
s_sub_u32 s50,s48,s50
q7 212,116,172
s_lshl_b32 s51,s71,1
q6 216,112,176
s_add_u32 s51,s51,s49
q7 216,116,180
s_mov_b32 s95,0
q6 220,112,184
s_mov_b32 s44,s8
q7 220,116,188
q0 4
s_and_b32 s45,s9,0xffff
q6 252,120,184
s_waitcnt vmcnt(20) lgkmcnt(0)
s_barrier
s_mov_b32 s46,3670016
q7 252,124,188
s_mov_b32 s47,0x20000
q5 96,18,0
q6 248,120,176
s_mov_b32 s94,8192
q5 100,19,0
q7 248,124,180
s_mov_b32 s52,2760704
q6 244,120,168
s_add_u32 m0,s88,32768
s_xor_b32 s86,s12,131328
s_add_u32 s86,s86,s64
q7 244,124,172
s_mul_i32 s53,s51,229376
q6 240,120,160
s_add_u32 s52,s52,s53
q7 240,124,164
s_lshr_b32 s53,s50,2
q2 8,0
q6 236,120,152
s_and_b32 s54,s50,3
q7 236,124,156
s_mul_i32 s98,s53,s94
q6 232,120,144
s_lshl_b32 s98,s98,5
q7 232,124,148
s_lshl_b32 s55,s51,9
q6 228,120,136
s_add_u32 s98,s98,s55
q7 228,124,140
s_lshl_b32 s55,s54,7
q6 224,120,128
s_add_u32 s98,s98,s55
q2 9,1024
q7 224,124,132
q0 0
s_lshl_b32 s54,s54,13
q7 0,96,192
s_add_u32 s52,s52,s54
q7 0,100,196
s_and_b32 s54,s53,1
q5 104,18,2048
q7 4,96,200
s_lshl_b32 s54,s54,10
q5 108,19,2048
q7 4,100,204
s_add_u32 s52,s52,s54
q4 128,22,16384
q7 8,96,208
s_lshr_b32 s54,s53,1
q4 130,23,16640
q7 8,100,212
s_lshl_b32 s54,s54,15
q4 132,22,20480
q7 12,96,216
s_add_u32 s91,s52,s54
q4 134,23,20736
q7 12,100,220
q4 136,23,16384
q7 16,96,224
q4 138,22,16640
q7 16,100,228
q2 10,2048
q7 20,96,232
s_cmp_ge_u32 s48,112
s_cselect_b32 s98,1073741824,s98
s_cselect_b32 s91,1073741824,s91
q7 20,100,236
s_lshl_b32 s48,s94,3
s_sub_u32 s48,s48,1024
v_mad_u32_u24 v92,v86,s94,v87
q7 24,96,240
s_xor_b32 s87,s13,1572992
q2 11,3072
s_add_u32 s87,s87,s65
q7 24,100,244
s_add_u32 m0,s89,32768
v_add_u32 v93,s48,v92
v_add_u32 v94,s48,v93
q7 28,96,248
s_mov_b32 s76,m0
s_add_u32 m0,s82,0
v_add_u32 v95,s48,v94
q7 28,100,252
q0 6
q8 92,0
s_mov_b32 m0,s76
q7 60,104,248
q7 60,108,252
q5 112,18,4096
q7 56,104,240
q5 116,19,4096
q7 56,108,244
q4 140,23,20480
q7 52,104,232
q4 142,22,20736
q7 52,108,236
q4 144,22,16896
q7 48,104,224
q4 146,23,17152
q7 48,108,228
q4 148,22,20992
q7 44,104,216
q4 150,23,21248
q7 44,108,220
q1 12,0
q7 40,104,208
q7 40,108,212
q1 13,1024
q7 36,104,200
q7 36,108,204
q7 32,104,192
q7 32,108,196
q0 6
q7 64,112,192
q7 64,116,196
q5 120,18,6144
q7 68,112,200
q5 124,19,6144
q7 68,116,204
q4 152,23,16896
q7 72,112,208
q4 154,22,17152
q7 72,116,212
q4 156,23,20992
q7 76,112,216
q4 158,22,21248
q7 76,116,220
q1 14,2048
q7 80,112,224
q7 80,116,228
q1 15,3072
q7 84,112,232
q7 84,116,236
q7 88,112,240
s_mov_b32 s76,m0
s_add_u32 m0,s82,0
q7 88,116,244
q8 93,1024
s_mov_b32 m0,s76
q7 92,112,248
q7 92,116,252
q0 4
q7 124,120,248
s_waitcnt vmcnt(21) lgkmcnt(0)
s_barrier
q7 124,124,252
q5 96,18,32768
q7 120,120,240
q5 100,19,32768
q7 120,124,244
q4 160,22,49152
q7 116,120,232
s_xor_b32 s86,s12,384
q4 162,23,49408
s_add_u32 s86,s86,s64
q7 116,124,236
s_add_u32 m0,s88,65536
q4 164,22,53248
q7 112,120,224
q4 166,23,53504
q7 112,124,228
q2 8,0
q7 108,120,216
q7 108,124,220
q7 104,120,208
q7 104,124,212
q7 100,120,200
q7 100,124,204
q7 96,120,192
q2 9,1024
q7 96,124,196
q0 4
q7 128,96,192
q7 128,100,196
q5 104,18,34816
q7 132,96,200
q5 108,19,34816
q7 132,100,204
q4 168,23,49152
q7 136,96,208
q4 170,22,49408
q7 136,100,212
q4 172,23,53248
q7 140,96,216
q4 174,22,53504
q7 140,100,220
q2 10,2048
q7 144,96,224
q7 144,100,228
q7 148,96,232
s_mov_b32 s76,m0
s_add_u32 m0,s82,0
q7 148,100,236
q8 94,2048
s_mov_b32 m0,s76
q7 152,96,240
s_xor_b32 s87,s13,2097152
q2 11,3072
s_add_u32 s87,s87,s65
q7 152,100,244
s_add_u32 m0,s89,65536
q7 156,96,248
q7 156,100,252
q0 4
q7 188,104,248
q7 188,108,252
q5 112,18,36864
q7 184,104,240
q5 116,19,36864
q7 184,108,244
q4 176,22,49664
q7 180,104,232
q4 178,23,49920
q7 180,108,236
q4 180,22,53760
q7 176,104,224
q4 182,23,54016
q7 176,108,228
q1 12,0
q7 172,104,216
q7 172,108,220
q7 168,104,208
q7 168,108,212
q1 13,1024
q7 164,104,200
q3 20
v_readfirstlane_b32 s99,v79
q7 164,108,204
s_add_u32 s99,s99,1
q7 160,104,192
s_mul_i32 s81,s99,0x9e3779b1
q7 160,108,196
q0 4
s_xor_b32 s81,s81,0x85ebca6b
q7 192,112,192
v_lshlrev_b32 v82,16,v81
v_and_b32 v83,0xffff0000,v81
q7 192,116,196
q5 120,18,38912
v_and_b32 v81,0xffff0000,v80
q7 196,112,200
q5 124,19,38912
v_lshlrev_b32 v80,16,v80
q7 196,116,204
q4 184,23,49664
v_mul_f32 v80,0x3db8aa3b,v80
q7 200,112,208
q4 186,22,49920
v_mul_f32 v81,0x3db8aa3b,v81
q7 200,116,212
q4 188,23,53760
v_mul_f32 v82,0x3db8aa3b,v82
q7 204,112,216
q4 190,22,54016
v_mul_f32 v83,0x3db8aa3b,v83
q7 204,116,220
q1 14,2048
q7 208,112,224
s_mov_b32 s77,917504
buffer_store_dwordx4 v[80:83],v78,s[32:35],s77 offen sc1
v_lshlrev_b32 v40,16,v32
q7 208,116,228
q1 15,3072
v_and_b32 v41,0xffff0000,v32
q7 212,112,232
s_mov_b32 s76,m0
s_add_u32 m0,s82,0
v_lshlrev_b32 v42,16,v33
q7 212,116,236
q8 95,3072
s_mov_b32 m0,s76
q7 216,112,240
s_lshr_b32 s48,s30,3
v_and_b32 v43,0xffff0000,v33
v_lshlrev_b32 v44,16,v34
q7 216,116,244
s_lshl2_add_u32 s48,s48,s31
v_and_b32 v45,0xffff0000,v34
v_lshlrev_b32 v46,16,v35
q7 220,112,248
s_cmp_ge_u32 s48,56
s_cselect_b32 s49,1,0
v_and_b32 v47,0xffff0000,v35
q7 220,116,252
q0 4
s_mul_i32 s50,s49,56
v_lshlrev_b32 v48,16,v36
q7 252,120,248
s_waitcnt vmcnt(21) lgkmcnt(0)
s_barrier
s_sub_u32 s50,s48,s50
q7 252,124,252
s_lshl_b32 s51,s71,1
q5 96,16,0
v_and_b32 v49,0xffff0000,v36
q7 248,120,240
s_add_u32 s51,s51,s49
q5 100,17,0
v_lshlrev_b32 v50,16,v37
q7 248,124,244
s_mov_b32 s95,1
v_and_b32 v51,0xffff0000,v37
v_lshlrev_b32 v52,16,v38
q7 244,120,232
s_add_u32 m0,s88,98304
s_xor_b32 s86,s12,131456
s_add_u32 s86,s86,s64
q7 244,124,236
s_mov_b32 s44,s14
v_and_b32 v53,0xffff0000,v38
v_lshlrev_b32 v54,16,v39
q7 240,120,224
s_and_b32 s45,s15,0xffff
v_and_b32 v55,0xffff0000,v39
q9 72,40,41
q7 240,124,228
s_mov_b32 s46,3670016
q2 8,0
q9 72,72,42
q7 236,120,216
s_mov_b32 s47,0x20000
q9 72,72,43
q9 72,72,44
q7 236,124,220
s_mov_b32 s94,896
q9 72,72,45
q9 72,72,46
q7 232,120,208
s_mov_b32 s52,6430720
q9 72,72,47
q9 72,72,48
q7 232,124,212
s_mul_i32 s53,s51,229376
q9 72,72,49
q9 72,72,50
q7 228,120,200
s_add_u32 s52,s52,s53
q9 72,72,51
q9 72,72,52
q7 228,124,204
s_and_b32 s53,s50,7
q9 72,72,53
q9 72,72,54
q7 224,120,192
s_lshr_b32 s54,s50,3
q2 9,1024
q9 72,72,55
q7 224,124,196
q0 0
s_lshl_b32 s55,s51,3
q7 0,96,128
s_add_u32 s55,s55,s53
v_add_f32_dpp v72,v72,v72 row_ror:8 row_mask:0xf bank_mask:0xf
q7 0,100,132
s_mul_i32 s98,s55,28672
q5 104,16,2048
q7 4,96,136
s_lshl_b32 s55,s54,7
q5 108,17,2048
v_add_f32_dpp v72,v72,v72 row_ror:4 row_mask:0xf bank_mask:0xf
q7 4,100,140
s_add_u32 s98,s98,s55
q4 192,20,16384
q7 8,96,144
s_lshl_b32 s54,s54,15
q4 194,21,16640
v_add_f32_dpp v72,v72,v72 row_ror:2 row_mask:0xf bank_mask:0xf
q7 8,100,148
s_add_u32 s52,s52,s54
q4 196,20,20480
q7 12,96,152
s_lshl_b32 s53,s53,10
q4 198,21,20736
v_add_f32_dpp v72,v72,v72 row_ror:1 row_mask:0xf bank_mask:0xf
q7 12,100,156
s_add_u32 s92,s52,s53
q4 200,21,16384
q7 16,96,160
q4 202,20,16640
v_mov_b32 v73,v72
q7 16,100,164
q2 10,2048
q7 20,96,168
s_cmp_ge_u32 s48,112
s_cselect_b32 s98,1073741824,s98
s_cselect_b32 s92,1073741824,s92
q7 20,100,172
s_lshl_b32 s48,s94,3
s_sub_u32 s48,s48,1024
v_mad_u32_u24 v92,v86,s94,v87
q7 24,96,176
s_xor_b32 s87,s13,2097280
q2 11,3072
s_add_u32 s87,s87,s65
q7 24,100,180
s_add_u32 m0,s89,98304
v_add_u32 v93,s48,v92
v_add_u32 v94,s48,v93
q7 28,96,184
s_mov_b32 s76,m0
s_add_u32 m0,s82,4096
v_add_u32 v95,s48,v94
q7 28,100,188
q0 6
q8 92,0
s_mov_b32 m0,s76
q7 60,104,184
v_permlane16_swap_b32 v72,v73
q7 60,108,188
q5 112,16,4096
q7 56,104,176
q5 116,17,4096
q9 72,72,73
q7 56,108,180
q4 204,21,20480
v_mul_f32 v74,0x3b124925,v72
q7 52,104,168
q4 206,20,20736
v_sub_f32 v56,v40,v74
q7 52,108,172
q4 208,20,16896
v_sub_f32 v57,v41,v74
q7 48,104,160
q4 210,21,17152
v_sub_f32 v58,v42,v74
q7 48,108,164
q4 212,20,20992
v_sub_f32 v59,v43,v74
q7 44,104,152
q4 214,21,21248
v_sub_f32 v60,v44,v74
q7 44,108,156
q1 12,0
v_sub_f32 v61,v45,v74
q7 40,104,144
v_sub_f32 v62,v46,v74
v_sub_f32 v63,v47,v74
q7 40,108,148
q1 13,1024
v_sub_f32 v64,v48,v74
q7 36,104,136
v_sub_f32 v65,v49,v74
v_sub_f32 v66,v50,v74
q7 36,108,140
v_sub_f32 v67,v51,v74
v_sub_f32 v68,v52,v74
q7 32,104,128
v_sub_f32 v69,v53,v74
v_sub_f32 v70,v54,v74
q7 32,108,132
q0 6
v_sub_f32 v71,v55,v74
qa 72,56,56
q7 64,112,128
v_fmac_f32 v72,v57,v57
v_fmac_f32 v72,v58,v58
q7 64,116,132
q5 120,16,6144
v_fmac_f32 v72,v59,v59
q7 68,112,136
q5 124,17,6144
v_fmac_f32 v72,v60,v60
q7 68,116,140
q4 216,21,16896
v_fmac_f32 v72,v61,v61
q7 72,112,144
q4 218,20,17152
v_fmac_f32 v72,v62,v62
q7 72,116,148
q4 220,21,20992
v_fmac_f32 v72,v63,v63
q7 76,112,152
q4 222,20,21248
v_fmac_f32 v72,v64,v64
q7 76,116,156
q1 14,2048
v_fmac_f32 v72,v65,v65
q7 80,112,160
v_fmac_f32 v72,v66,v66
v_fmac_f32 v72,v67,v67
q7 80,116,164
q1 15,3072
v_fmac_f32 v72,v68,v68
q7 84,112,168
v_fmac_f32 v72,v69,v69
v_fmac_f32 v72,v70,v70
q7 84,116,172
v_fmac_f32 v72,v71,v71
v_cndmask_b32 v72,0,v72,s[96:97]
q7 88,112,176
s_mov_b32 s76,m0
s_add_u32 m0,s82,4096
q7 88,116,180
q8 93,1024
s_mov_b32 m0,s76
q7 92,112,184
v_add_f32_dpp v72,v72,v72 row_ror:8 row_mask:0xf bank_mask:0xf
q7 92,116,188
q0 4
q7 124,120,184
s_waitcnt vmcnt(22) lgkmcnt(0)
s_barrier
v_add_f32_dpp v72,v72,v72 row_ror:4 row_mask:0xf bank_mask:0xf
q7 124,124,188
q5 96,16,32768
q7 120,120,176
q5 100,17,32768
v_add_f32_dpp v72,v72,v72 row_ror:2 row_mask:0xf bank_mask:0xf
q7 120,124,180
LHH6_2kf:
s_cbranch_execz LHRPkf
q4 224,20,49152
q7 116,120,168
s_xor_b32 s86,s12,512
q4 226,21,49408
s_add_u32 s86,s86,s64
q7 116,124,172
s_add_u32 m0,s88,0
q4 228,20,53248
v_add_f32_dpp v72,v72,v72 row_ror:1 row_mask:0xf bank_mask:0xf
q7 112,120,160
q4 230,21,53504
q7 112,124,164
q2 8,0
v_mov_b32 v73,v72
q7 108,120,152
q7 108,124,156
v_permlane16_swap_b32 v72,v73
q7 104,120,144
q7 104,124,148
q9 72,72,73
v_mul_f32 v72,0x3b124925,v72
q7 100,120,136
qb 72,72,72
v_and_b32 v72,0xffff0000,v72
q7 100,124,140
v_add_f32 v72,0x3727c5ac,v72
qb 72,72,72
q7 96,120,128
q2 9,1024
v_and_b32 v72,0xffff0000,v72
q7 96,124,132
q0 4
v_sqrt_f32 v72,v72
q7 128,96,128
qb 72,72,72
v_and_b32 v72,0xffff0000,v72
q7 128,100,132
q5 104,16,34816
q7 132,96,136
q5 108,17,34816
q7 132,100,140
q4 232,21,49152
q7 136,96,144
q4 234,20,49408
q7 136,100,148
q4 236,21,53248
q7 140,96,152
q4 238,20,53504
q7 140,100,156
q2 10,2048
q7 144,96,160
v_rcp_f32 v75,v72
q7 144,100,164
qb 74,74,74
v_and_b32 v74,0xffff0000,v74
q7 148,96,168
s_mov_b32 s76,m0
s_add_u32 m0,s82,4096
v_sub_f32 v40,v40,v74
q7 148,100,172
q8 94,2048
s_mov_b32 m0,s76
q7 152,96,176
s_xor_b32 s87,s13,2621440
q2 11,3072
s_add_u32 s87,s87,s65
q7 152,100,180
s_add_u32 m0,s89,0
v_sub_f32 v41,v41,v74
v_sub_f32 v42,v42,v74
q7 156,96,184
v_sub_f32 v43,v43,v74
v_sub_f32 v44,v44,v74
q7 156,100,188
q0 4
v_sub_f32 v45,v45,v74
v_sub_f32 v46,v46,v74
q7 188,104,184
v_sub_f32 v47,v47,v74
v_sub_f32 v48,v48,v74
q7 188,108,188
q5 112,16,36864
v_sub_f32 v49,v49,v74
q7 184,104,176
q5 116,17,36864
v_sub_f32 v50,v50,v74
q7 184,108,180
q4 240,20,49664
v_sub_f32 v51,v51,v74
q7 180,104,168
q4 242,21,49920
v_sub_f32 v52,v52,v74
q7 180,108,172
q4 244,20,53760
v_sub_f32 v53,v53,v74
q7 176,104,160
q4 246,21,54016
v_sub_f32 v54,v54,v74
q7 176,108,164
q1 12,0
v_sub_f32 v55,v55,v74
q7 172,104,152
v_mul_f32 v75,0x3db8aa3b,v75
qa 56,40,75
q7 172,108,156
qa 57,41,75
qa 58,42,75
q7 168,104,144
qa 59,43,75
qa 60,44,75
q7 168,108,148
q1 13,1024
qa 61,45,75
q7 164,104,136
qa 62,46,75
qa 63,47,75
q7 164,108,140
qa 64,48,75
qa 65,49,75
q7 160,104,128
qa 66,50,75
qa 67,51,75
q7 160,108,132
q0 4
qa 68,52,75
qa 69,53,75
q7 192,112,128
qa 70,54,75
qa 71,55,75
q7 192,116,132
q5 120,16,38912
qb 32,56,57
q7 196,112,136
q5 124,17,38912
qb 33,58,59
q7 196,116,140
q4 248,21,49664
qb 34,60,61
q7 200,112,144
q4 250,20,49920
qb 35,62,63
q7 200,116,148
q4 252,21,53760
qb 36,64,65
q7 204,112,152
q4 254,20,54016
qb 37,66,67
q7 204,116,156
q1 14,2048
qb 38,68,69
q7 208,112,160
qb 39,70,71
q7 208,116,164
q1 15,3072
q7 212,112,168
s_mov_b32 s76,m0
s_add_u32 m0,s82,4096
q7 212,116,172
q8 95,3072
s_mov_b32 m0,s76
q7 216,112,176
q3 20
q4 40,84,0
q7 216,116,180
q4 42,84,256
q7 220,112,184
q4 44,84,32
q7 220,116,188
q0 7
q4 46,84,288
q7 252,120,184
s_waitcnt vmcnt(21) lgkmcnt(0)
s_barrier
q7 252,124,188
q5 96,18,0
q7 248,120,176
q5 100,19,0
q7 248,124,180
q4 48,84,512
q7 244,120,168
s_add_u32 m0,s88,32768
s_xor_b32 s86,s12,131584
s_add_u32 s86,s86,s64
q7 244,124,172
q4 50,84,768
q7 240,120,160
q4 52,84,544
q7 240,124,164
q2 8,0
q7 236,120,152
q4 54,84,800
q7 236,124,156
q7 232,120,144
q7 232,124,148
q7 228,120,136
q7 228,124,140
q7 224,120,128
q2 9,1024
q7 224,124,132
q0 4
q7 0,96,192
buffer_store_dwordx4 v[40:43],v2,s[32:35],s91 offen sc1
s_add_u32 s91,s91,2048
q7 0,100,196
q5 104,18,2048
q7 4,96,200
q5 108,19,2048
q7 4,100,204
)"
R"(q4 128,22,16384
q7 8,96,208
q4 130,23,16640
q7 8,100,212
q4 132,22,20480
q7 12,96,216
q4 134,23,20736
q7 12,100,220
q4 136,23,16384
q7 16,96,224
q4 138,22,16640
q7 16,100,228
q2 10,2048
q7 20,96,232
q7 20,100,236
q7 24,96,240
s_xor_b32 s87,s13,2621568
q2 11,3072
s_add_u32 s87,s87,s65
q7 24,100,244
s_add_u32 m0,s89,32768
q7 28,96,248
q7 28,100,252
q0 6
q7 60,104,248
q7 60,108,252
q5 112,18,4096
q7 56,104,240
q5 116,19,4096
q7 56,108,244
q4 140,23,20480
q7 52,104,232
q4 142,22,20736
q7 52,108,236
q4 144,22,16896
q7 48,104,224
s_lshr_b32 s48,s30,3
q4 146,23,17152
q7 48,108,228
s_lshl2_add_u32 s48,s48,s31
q4 148,22,20992
q7 44,104,216
q4 150,23,21248
q7 44,108,220
q1 12,0
q7 40,104,208
s_cmp_ge_u32 s48,49
s_cselect_b32 s95,1,0
s_cselect_b32 s54,s20,s16
s_cselect_b32 s55,s21,s17
s_mov_b32 s94,3584
s_cselect_b32 s94,896,s94
q7 40,108,212
s_mov_b32 s44,s54
q1 13,1024
q7 36,104,200
s_and_b32 s45,s55,0xffff
buffer_store_dwordx4 v[44:47],v2,s[32:35],s91 offen sc1
s_add_u32 s91,s91,2048
q7 36,108,204
s_mov_b32 s46,1605632
q7 32,104,192
s_mov_b32 s47,0x20000
q7 32,108,196
q0 6
s_mul_i32 s49,s71,49
q7 64,112,192
s_add_u32 s49,s49,s48
q7 64,116,196
s_mul_i32 s50,s49,2341
q5 120,18,6144
q7 68,112,200
s_lshr_b32 s50,s50,15
q5 124,19,6144
q7 68,116,204
s_mul_i32 s51,s50,14
q4 152,23,16896
q7 72,112,208
s_sub_u32 s51,s49,s51
q4 154,22,17152
q7 72,116,212
s_mul_i32 s56,s51,114688
q4 156,23,20992
q7 76,112,216
s_lshl_b32 s52,s50,7
q4 158,22,21248
q7 76,116,220
s_add_u32 s56,s56,s52
q1 14,2048
q7 80,112,224
s_lshr_b32 s52,s51,1
q7 80,116,228
s_lshl_b32 s52,s52,15
q1 15,3072
q7 84,112,232
s_add_u32 s52,s52,10100736
q7 84,116,236
s_and_b32 s51,s51,1
q7 88,112,240
s_lshl2_add_u32 s53,s50,0
q7 88,116,244
s_mul_i32 s54,s53,2341
q7 92,112,248
s_lshr_b32 s54,s54,15
q7 92,116,252
q0 4
s_mul_i32 s55,s54,14
q7 124,120,248
s_waitcnt vmcnt(21) lgkmcnt(0)
s_barrier
s_sub_u32 s53,s53,s55
q7 124,124,252
s_lshl1_add_u32 s53,s53,s51
q5 96,18,32768
q7 120,120,240
s_mul_i32 s55,s53,37
q5 100,19,32768
q7 120,124,244
s_lshr_b32 s55,s55,8
q4 160,22,49152
q7 116,120,232
s_xor_b32 s86,s12,640
q4 162,23,49408
s_add_u32 s86,s86,s64
q7 116,124,236
s_add_u32 m0,s88,65536
q4 164,22,53248
s_add_u32 s53,s53,s55
q7 112,120,224
s_lshl_b32 s53,s53,10
q4 166,23,53504
q7 112,124,228
s_mul_i32 s54,s54,229376
q2 8,0
q7 108,120,216
s_add_u32 s53,s53,s54
buffer_store_dwordx4 v[48:51],v2,s[32:35],s91 offen sc1
s_add_u32 s91,s91,2048
q7 108,124,220
s_add_u32 s93,s53,s52
q7 104,120,208
s_lshl2_add_u32 s53,s50,1
q7 104,124,212
s_mul_i32 s54,s53,2341
q7 100,120,200
s_lshr_b32 s54,s54,15
q7 100,124,204
s_mul_i32 s55,s54,14
q7 96,120,192
s_sub_u32 s53,s53,s55
q2 9,1024
q7 96,124,196
q0 4
s_lshl1_add_u32 s53,s53,s51
q7 128,96,192
s_mul_i32 s55,s53,37
q7 128,100,196
s_lshr_b32 s55,s55,8
q5 104,18,34816
q7 132,96,200
s_add_u32 s53,s53,s55
q5 108,19,34816
q7 132,100,204
s_lshl_b32 s53,s53,10
q4 168,23,49152
q7 136,96,208
s_mul_i32 s54,s54,229376
q4 170,22,49408
q7 136,100,212
s_add_u32 s53,s53,s54
q4 172,23,53248
q7 140,96,216
s_add_u32 s78,s53,s52
q4 174,22,53504
q7 140,100,220
s_lshl2_add_u32 s53,s50,2
q2 10,2048
q7 144,96,224
s_mul_i32 s54,s53,2341
q7 144,100,228
s_lshr_b32 s54,s54,15
q7 148,96,232
s_mul_i32 s55,s54,14
q7 148,100,236
s_sub_u32 s53,s53,s55
q7 152,96,240
s_xor_b32 s87,s13,3145728
q2 11,3072
s_add_u32 s87,s87,s65
q7 152,100,244
s_lshl1_add_u32 s53,s53,s51
s_add_u32 m0,s89,65536
q7 156,96,248
s_mul_i32 s55,s53,37
q7 156,100,252
q0 4
s_lshr_b32 s55,s55,8
q7 188,104,248
s_add_u32 s53,s53,s55
q7 188,108,252
s_lshl_b32 s53,s53,10
q5 112,18,36864
q7 184,104,240
s_mul_i32 s54,s54,229376
q5 116,19,36864
q7 184,108,244
s_add_u32 s53,s53,s54
q4 176,22,49664
q7 180,104,232
s_add_u32 s79,s53,s52
q4 178,23,49920
q7 180,108,236
s_lshl2_add_u32 s53,s50,3
q4 180,22,53760
q7 176,104,224
s_mul_i32 s54,s53,2341
q4 182,23,54016
q7 176,108,228
s_lshr_b32 s54,s54,15
q1 12,0
q7 172,104,216
s_mul_i32 s55,s54,14
buffer_store_dwordx4 v[52:55],v2,s[32:35],s91 offen sc1
q7 172,108,220
s_sub_u32 s53,s53,s55
q7 168,104,208
s_lshl1_add_u32 s53,s53,s51
q7 168,108,212
s_mul_i32 s55,s53,37
q1 13,1024
q7 164,104,200
s_lshr_b32 s55,s55,8
q7 164,108,204
s_add_u32 s53,s53,s55
q7 160,104,192
s_lshl_b32 s53,s53,10
q7 160,108,196
q0 4
s_mul_i32 s54,s54,229376
q7 192,112,192
s_add_u32 s53,s53,s54
q7 192,116,196
s_add_u32 s80,s53,s52
q5 120,18,38912
q7 196,112,200
s_sub_u32 s49,s48,49
q5 124,19,38912
q7 196,116,204
s_mul_i32 s50,s49,37
q4 184,23,49664
q7 200,112,208
s_lshr_b32 s50,s50,8
q4 186,22,49920
q7 200,116,212
s_mul_i32 s51,s50,7
q4 188,23,53760
q7 204,112,216
s_sub_u32 s51,s49,s51
q4 190,22,54016
q7 204,116,220
s_mul_i32 s52,s71,7
q1 14,2048
q7 208,112,224
s_add_u32 s52,s52,s51
q7 208,116,228
s_mul_i32 s52,s52,28672
q1 15,3072
q7 212,112,232
s_lshl_b32 s53,s50,7
q7 212,116,236
s_add_u32 s52,s52,s53
q7 216,112,240
s_mul_i32 s53,s71,229376
q7 216,116,244
s_lshl_b32 s50,s50,15
q7 220,112,248
s_add_u32 s53,s53,s50
q7 220,116,252
q0 4
s_lshl_b32 s51,s51,10
q7 252,120,248
s_waitcnt vmcnt(21) lgkmcnt(0)
s_barrier
s_add_u32 s53,s53,s51
q7 252,124,252
s_add_u32 s53,s53,11935744
q5 96,16,0
q7 248,120,240
q5 100,17,0
q7 248,124,244
s_cmp_ge_u32 s48,98
s_cselect_b32 s52,1073741824,s52
s_cselect_b32 s53,1073741824,s53
q7 244,120,232
s_add_u32 m0,s88,98304
s_xor_b32 s86,s12,131712
s_add_u32 s86,s86,s64
q7 244,124,236
s_add_u32 s54,s53,8192
q7 240,120,224
s_add_u32 s55,s53,16384
q7 240,124,228
s_add_u32 s51,s53,24576
q2 8,0
q7 236,120,216
s_cmp_eq_u32 s95,0
s_cselect_b32 s98,s56,s52
s_cselect_b32 s93,s93,s53
s_cselect_b32 s78,s78,s54
q7 236,124,220
s_cmp_eq_u32 s95,0
s_cselect_b32 s79,s79,s55
s_cselect_b32 s80,s80,s51
q7 232,120,208
s_lshl_b32 s48,s94,3
s_sub_u32 s48,s48,1024
v_mad_u32_u24 v92,v86,s94,v87
q7 232,124,212
v_add_u32 v93,s48,v92
v_add_u32 v94,s48,v93
q7 228,120,200
s_mov_b32 s76,m0
s_add_u32 m0,s82,0
v_add_u32 v95,s48,v94
q7 228,124,204
q8 92,0
s_mov_b32 m0,s76
q7 224,120,192
q2 9,1024
q7 224,124,196
q0 0
q7 0,96,128
q7 0,100,132
q5 104,16,2048
q7 4,96,136
q5 108,17,2048
q7 4,100,140
q4 192,20,16384
q7 8,96,144
q4 194,21,16640
q7 8,100,148
q4 196,20,20480
q7 12,96,152
q4 198,21,20736
q7 12,100,156
q4 200,21,16384
q7 16,96,160
q4 202,20,16640
q7 16,100,164
q2 10,2048
q7 20,96,168
q7 20,100,172
q7 24,96,176
s_xor_b32 s87,s13,3145856
q2 11,3072
s_add_u32 s87,s87,s65
q7 24,100,180
s_add_u32 m0,s89,98304
q7 28,96,184
q7 28,100,188
q0 6
q7 60,104,184
q7 60,108,188
q5 112,16,4096
q7 56,104,176
q5 116,17,4096
q7 56,108,180
q4 204,21,20480
q7 52,104,168
q4 206,20,20736
q7 52,108,172
q4 208,20,16896
q7 48,104,160
q4 210,21,17152
q7 48,108,164
q4 212,20,20992
q7 44,104,152
q4 214,21,21248
q7 44,108,156
q1 12,0
q7 40,104,144
q7 40,108,148
LHH6_3kf:
s_cbranch_execz LHRPkf
q1 13,1024
q7 36,104,136
s_mov_b32 s76,m0
s_add_u32 m0,s82,0
q7 36,108,140
q8 93,1024
s_mov_b32 m0,s76
q7 32,104,128
q7 32,108,132
q0 6
q7 64,112,128
q7 64,116,132
q5 120,16,6144
q7 68,112,136
q5 124,17,6144
q7 68,116,140
q4 216,21,16896
q7 72,112,144
q4 218,20,17152
q7 72,116,148
q4 220,21,20992
q7 76,112,152
q4 222,20,21248
q7 76,116,156
q1 14,2048
q7 80,112,160
q7 80,116,164
q1 15,3072
q7 84,112,168
q7 84,116,172
q7 88,112,176
q7 88,116,180
q7 92,112,184
q7 92,116,188
q0 4
q7 124,120,184
s_waitcnt vmcnt(20) lgkmcnt(0)
s_barrier
q7 124,124,188
q5 96,16,32768
q7 120,120,176
q5 100,17,32768
q7 120,124,180
q4 224,20,49152
q7 116,120,168
s_xor_b32 s86,s12,768
q4 226,21,49408
s_add_u32 s86,s86,s64
q7 116,124,172
s_add_u32 m0,s88,0
q4 228,20,53248
q7 112,120,160
q4 230,21,53504
q7 112,124,164
q2 8,0
q7 108,120,152
q7 108,124,156
q7 104,120,144
s_mov_b32 s76,m0
s_add_u32 m0,s82,0
q7 104,124,148
q8 94,2048
s_mov_b32 m0,s76
q7 100,120,136
q7 100,124,140
q7 96,120,128
q2 9,1024
q7 96,124,132
q0 4
q7 128,96,128
q7 128,100,132
q5 104,16,34816
q7 132,96,136
q5 108,17,34816
q7 132,100,140
q4 232,21,49152
q7 136,96,144
q4 234,20,49408
q7 136,100,148
q4 236,21,53248
q7 140,96,152
q4 238,20,53504
q7 140,100,156
q2 10,2048
q7 144,96,160
q7 144,100,164
q7 148,96,168
q7 148,100,172
q7 152,96,176
s_xor_b32 s87,s13,3670016
q2 11,3072
s_add_u32 s87,s87,s65
q7 152,100,180
s_add_u32 m0,s89,0
q7 156,96,184
q7 156,100,188
q0 4
q7 188,104,184
q7 188,108,188
q5 112,16,36864
q7 184,104,176
q5 116,17,36864
q7 184,108,180
q4 240,20,49664
q7 180,104,168
q4 242,21,49920
q7 180,108,172
q4 244,20,53760
q7 176,104,160
q4 246,21,54016
q7 176,108,164
q1 12,0
q7 172,104,152
s_mov_b32 s76,m0
s_add_u32 m0,s82,0
q7 172,108,156
q8 95,3072
s_mov_b32 m0,s76
q7 168,104,144
q3 37
q4 56,85,4096
q7 168,108,148
q1 13,1024
q7 164,104,136
q4 58,85,6144
q7 164,108,140
q4 60,85,4104
q7 160,104,128
q4 62,85,6152
q7 160,108,132
q0 8
q4 64,85,4608
q7 192,112,128
q4 66,85,6656
q7 192,116,132
q5 120,16,38912
q7 196,112,136
q5 124,17,38912
q7 196,116,140
q4 248,21,49664
q7 200,112,144
q4 250,20,49920
q7 200,116,148
q4 252,21,53760
q7 204,112,152
q4 254,20,54016
q7 204,116,156
q1 14,2048
q7 208,112,160
q4 68,85,4616
q7 208,116,164
q1 15,3072
q7 212,112,168
q4 70,85,6664
q7 212,116,172
q7 216,112,176
q7 216,116,180
q7 220,112,184
q7 220,116,188
q0 6
q7 252,120,184
s_waitcnt vmcnt(20) lgkmcnt(0)
s_barrier
q7 252,124,188
q5 96,18,0
q7 248,120,176
q5 100,19,0
q7 248,124,180
buffer_store_dwordx4 v[56:59],v2,s[32:35],s92 offen sc1
s_add_u32 s92,s92,8192
q7 244,120,168
s_add_u32 m0,s88,32768
s_xor_b32 s86,s12,131840
s_add_u32 s86,s86,s64
q7 244,124,172
q7 240,120,160
q7 240,124,164
q2 8,0
q7 236,120,152
q7 236,124,156
q7 232,120,144
q7 232,124,148
q7 228,120,136
q7 228,124,140
q7 224,120,128
q2 9,1024
q7 224,124,132
q0 0
q7 0,96,192
q7 0,100,196
q5 104,18,2048
q7 4,96,200
q5 108,19,2048
q7 4,100,204
q4 128,22,16384
q7 8,96,208
q4 130,23,16640
q7 8,100,212
q4 132,22,20480
q7 12,96,216
q4 134,23,20736
q7 12,100,220
q4 136,23,16384
q7 16,96,224
q4 138,22,16640
q7 16,100,228
q2 10,2048
q7 20,96,232
q7 20,100,236
q7 24,96,240
s_xor_b32 s87,s13,3670144
q2 11,3072
s_add_u32 s87,s87,s65
q7 24,100,244
s_add_u32 m0,s89,32768
q7 28,96,248
q7 28,100,252
q0 6
buffer_store_dwordx4 v[60:63],v2,s[32:35],s92 offen sc1
s_add_u32 s92,s92,8192
q7 60,104,248
q7 60,108,252
q5 112,18,4096
q7 56,104,240
q5 116,19,4096
q7 56,108,244
q4 140,23,20480
q7 52,104,232
q4 142,22,20736
q7 52,108,236
q4 144,22,16896
q7 48,104,224
q4 146,23,17152
q7 48,108,228
q4 148,22,20992
q7 44,104,216
q4 150,23,21248
q7 44,108,220
q1 12,0
q7 40,104,208
q7 40,108,212
q1 13,1024
q7 36,104,200
q7 36,108,204
q7 32,104,192
q7 32,108,196
q0 6
q7 64,112,192
q7 64,116,196
q5 120,18,6144
q7 68,112,200
q5 124,19,6144
q7 68,116,204
q4 152,23,16896
q7 72,112,208
q4 154,22,17152
q7 72,116,212
q4 156,23,20992
q7 76,112,216
q4 158,22,21248
q7 76,116,220
q1 14,2048
q7 80,112,224
q7 80,116,228
q1 15,3072
q7 84,112,232
q7 84,116,236
buffer_store_dwordx4 v[64:67],v2,s[32:35],s92 offen sc1
s_add_u32 s92,s92,8192
q7 88,112,240
q7 88,116,244
q7 92,112,248
q7 92,116,252
q0 4
q7 124,120,248
s_waitcnt vmcnt(21) lgkmcnt(0)
s_barrier
q7 124,124,252
q5 96,18,32768
q7 120,120,240
q5 100,19,32768
q7 120,124,244
q4 160,22,49152
q7 116,120,232
s_xor_b32 s86,s12,896
q4 162,23,49408
s_add_u32 s86,s86,s64
q7 116,124,236
s_add_u32 m0,s88,65536
q4 164,22,53248
q7 112,120,224
q4 166,23,53504
q7 112,124,228
q2 8,0
q7 108,120,216
ds_write_b128 v30,v[32:35]
q7 108,124,220
ds_write_b128 v31,v[36:39]
q7 104,120,208
q7 104,124,212
q7 100,120,200
q7 100,124,204
q7 96,120,192
q2 9,1024
q7 96,124,196
q0 6
q7 128,96,192
q7 128,100,196
q5 104,18,34816
q7 132,96,200
q5 108,19,34816
q7 132,100,204
q4 168,23,49152
q7 136,96,208
q4 170,22,49408
q7 136,100,212
q4 172,23,53248
q7 140,96,216
q4 174,22,53504
q7 140,100,220
q2 10,2048
q7 144,96,224
buffer_store_dwordx4 v[68:71],v2,s[32:35],s92 offen sc1
q7 144,100,228
q7 148,96,232
q7 148,100,236
q7 152,96,240
s_xor_b32 s87,s13,4194304
q2 11,3072
s_add_u32 s87,s87,s65
q7 152,100,244
s_add_u32 m0,s89,65536
q7 156,96,248
q7 156,100,252
q0 4
q7 188,104,248
q7 188,108,252
q5 112,18,36864
q7 184,104,240
q5 116,19,36864
q7 184,108,244
q4 176,22,49664
q7 180,104,232
q4 178,23,49920
q7 180,108,236
q4 180,22,53760
q7 176,104,224
q4 182,23,54016
q7 176,108,228
q1 12,0
q7 172,104,216
q7 172,108,220
q7 168,104,208
q7 168,108,212
q1 13,1024
q7 164,104,200
q7 164,108,204
q7 160,104,192
q7 160,108,196
q0 4
q7 192,112,192
q7 192,116,196
q5 120,18,38912
q7 196,112,200
q5 124,19,38912
q7 196,116,204
q4 184,23,49664
q7 200,112,208
q4 186,22,49920
q7 200,116,212
q4 188,23,53760
q7 204,112,216
q4 190,22,54016
q7 204,116,220
q1 14,2048
q7 208,112,224
s_cmp_eq_u32 s95,0
s_cselect_b64 vcc,-1,0
s_mov_b32 s49,2048
s_cselect_b32 s49,256,s49
s_cselect_b32 s50,32,8
q7 208,116,228
q1 15,3072
v_cndmask_b32 v88,v85,v84,vcc
q7 212,112,232
v_add_u32 v89,s49,v88
q7 212,116,236
v_add_u32 v90,s50,v88
q7 216,112,240
v_add_u32 v91,s50,v89
q7 216,116,244
q3 23
q4 56,88,0
q7 220,112,248
q4 58,89,0
q7 220,116,252
q0 6
q4 60,90,0
q7 252,120,248
s_waitcnt vmcnt(20) lgkmcnt(0)
s_barrier
q7 252,124,252
q5 96,16,0
q7 248,120,240
q5 100,17,0
q7 248,124,244
q4 62,91,0
q7 244,120,232
s_add_u32 m0,s88,98304
s_xor_b32 s86,s12,131968
s_add_u32 s86,s86,s64
q7 244,124,236
q4 64,88,512
q7 240,120,224
q4 66,89,512
q7 240,124,228
q2 8,0
q7 236,120,216
q4 68,90,512
q7 236,124,220
q4 70,91,512
q7 232,120,208
q7 232,124,212
q7 228,120,200
q7 228,124,204
q7 224,120,192
q2 9,1024
q7 224,124,196
q0 5
q7 0,96,128
q7 0,100,132
q5 104,16,2048
q7 4,96,136
q5 108,17,2048
q7 4,100,140
q4 192,20,16384
q7 8,96,144
q4 194,21,16640
q7 8,100,148
q4 196,20,20480
q7 12,96,152
q4 198,21,20736
q7 12,100,156
q4 200,21,16384
q7 16,96,160
q4 202,20,16640
q7 16,100,164
q2 10,2048
q7 20,96,168
buffer_store_dwordx4 v[56:59],v2,s[32:35],s93 offen sc1
q7 20,100,172
q7 24,96,176
s_xor_b32 s87,s13,4194432
q2 11,3072
s_add_u32 s87,s87,s65
q7 24,100,180
s_add_u32 m0,s89,98304
q7 28,96,184
q7 28,100,188
q0 6
q7 60,104,184
q7 60,108,188
q5 112,16,4096
q7 56,104,176
q5 116,17,4096
q7 56,108,180
q4 204,21,20480
q7 52,104,168
q4 206,20,20736
q7 52,108,172
q4 208,20,16896
q7 48,104,160
q4 210,21,17152
q7 48,108,164
q4 212,20,20992
q7 44,104,152
q4 214,21,21248
q7 44,108,156
q1 12,0
q7 40,104,144
q7 40,108,148
q1 13,1024
q7 36,104,136
q7 36,108,140
q7 32,104,128
q7 32,108,132
q0 6
q7 64,112,128
q7 64,116,132
q5 120,16,6144
q7 68,112,136
q5 124,17,6144
q7 68,116,140
q4 216,21,16896
q7 72,112,144
q4 218,20,17152
q7 72,116,148
q4 220,21,20992
q7 76,112,152
q4 222,20,21248
q7 76,116,156
q1 14,2048
q7 80,112,160
buffer_store_dwordx4 v[60:63],v2,s[32:35],s78 offen sc1
q7 80,116,164
q1 15,3072
q7 84,112,168
q7 84,116,172
q7 88,112,176
q7 88,116,180
q7 92,112,184
q7 92,116,188
q0 4
q7 124,120,184
s_waitcnt vmcnt(20) lgkmcnt(0)
s_barrier
q7 124,124,188
q5 96,16,32768
q7 120,120,176
q5 100,17,32768
q7 120,124,180
q4 224,20,49152
q7 116,120,168
s_xor_b32 s86,s12,1024
q4 226,21,49408
s_add_u32 s86,s86,s64
q7 116,124,172
s_add_u32 m0,s88,0
q4 228,20,53248
q7 112,120,160
q4 230,21,53504
q7 112,124,164
q2 8,0
q7 108,120,152
q7 108,124,156
q7 104,120,144
q7 104,124,148
q7 100,120,136
q7 100,124,140
q7 96,120,128
q2 9,1024
q7 96,124,132
LHH7_0kf:
s_cbranch_execz LHR7kf
q0 4
q7 128,96,128
q7 128,100,132
q5 104,16,34816
q7 132,96,136
q5 108,17,34816
q7 132,100,140
q4 232,21,49152
q7 136,96,144
q4 234,20,49408
q7 136,100,148
q4 236,21,53248
q7 140,96,152
q4 238,20,53504
q7 140,100,156
q2 10,2048
q7 144,96,160
buffer_store_dwordx4 v[64:67],v2,s[32:35],s79 offen sc1
q7 144,100,164
q7 148,96,168
q7 148,100,172
q7 152,96,176
s_xor_b32 s87,s13,4718592
q2 11,3072
s_add_u32 s87,s87,s65
q7 152,100,180
s_add_u32 m0,s89,0
q7 156,96,184
q7 156,100,188
q0 4
q7 188,104,184
q7 188,108,188
q5 112,16,36864
q7 184,104,176
q5 116,17,36864
q7 184,108,180
q4 240,20,49664
q7 180,104,168
q4 242,21,49920
q7 180,108,172
q4 244,20,53760
q7 176,104,160
q4 246,21,54016
q7 176,108,164
q1 12,0
q7 172,104,152
q7 172,108,156
q7 168,104,144
q7 168,108,148
q1 13,1024
q7 164,104,136
q7 164,108,140
q7 160,104,128
q7 160,108,132
q0 4
q7 192,112,128
q7 192,116,132
q5 120,16,38912
q7 196,112,136
q5 124,17,38912
q7 196,116,140
q4 248,21,49664
q7 200,112,144
q4 250,20,49920
q7 200,116,148
q4 252,21,53760
q7 204,112,152
q4 254,20,54016
q7 204,116,156
q1 14,2048
q7 208,112,160
buffer_store_dwordx4 v[68:71],v2,s[32:35],s80 offen sc1
q7 208,116,164
q1 15,3072
q7 212,112,168
q7 212,116,172
q7 216,112,176
q7 216,116,180
q7 220,112,184
q7 220,116,188
q0 4
q7 252,120,184
s_waitcnt vmcnt(20) lgkmcnt(0)
s_barrier
q7 252,124,188
q5 96,18,0
q7 248,120,176
q5 100,19,0
q7 248,124,180
q7 244,120,168
s_add_u32 m0,s88,32768
s_xor_b32 s86,s12,132096
s_add_u32 s86,s86,s64
q7 244,124,172
q7 240,120,160
q7 240,124,164
q2 8,0
q7 236,120,152
q7 236,124,156
q7 232,120,144
q7 232,124,148
q7 228,120,136
q7 228,124,140
q7 224,120,128
q2 9,1024
q7 224,124,132
q0 0
q7 0,96,192
q7 0,100,196
q5 104,18,2048
q7 4,96,200
q5 108,19,2048
q7 4,100,204
q4 128,22,16384
q7 8,96,208
q4 130,23,16640
q7 8,100,212
q4 132,22,20480
q7 12,96,216
q4 134,23,20736
q7 12,100,220
q4 136,23,16384
q7 16,96,224
q4 138,22,16640
q7 16,100,228
q2 10,2048
q7 20,96,232
s_lshr_b32 s52,s31,1
s_lshl_b32 s52,s52,13
q7 20,100,236
s_and_b32 s53,s31,1
s_lshl_b32 s53,s53,11
q7 24,96,240
s_xor_b32 s87,s13,4718720
q2 11,3072
s_add_u32 s87,s87,s65
q7 24,100,244
s_add_u32 s52,s52,s53
s_add_u32 s52,s52,135168
s_add_u32 m0,s89,32768
q7 28,96,248
v_add_u32 v72,s52,v2
q7 28,100,252
q0 6
ds_read_b128 v[40:43],v72
q7 60,104,248
q5 44,72,1024
q7 60,108,252
q5 112,18,4096
q7 56,104,240
q5 116,19,4096
q7 56,108,244
q4 140,23,20480
q7 52,104,232
q4 142,22,20736
q7 52,108,236
q4 144,22,16896
q7 48,104,224
q4 146,23,17152
q7 48,108,228
q4 148,22,20992
q7 44,104,216
q4 150,23,21248
q7 44,108,220
q1 12,0
q7 40,104,208
s_mov_b32 s51,925696
s_cmp_eq_u32 s31,3
s_cselect_b32 s52,1073741824,s51
q7 40,108,212
q1 13,1024
q7 36,104,200
q7 36,108,204
q7 32,104,192
q7 32,108,196
q0 6
q7 64,112,192
q7 64,116,196
q5 120,18,6144
q7 68,112,200
q5 124,19,6144
q7 68,116,204
q4 152,23,16896
q7 72,112,208
q4 154,22,17152
q7 72,116,212
q4 156,23,20992
q7 76,112,216
q4 158,22,21248
q7 76,116,220
q1 14,2048
q7 80,112,224
buffer_store_dwordx4 v[40:43],v77,s[32:35],s51 offen sc1
q7 80,116,228
q1 15,3072
q7 84,112,232
q7 84,116,236
q7 88,112,240
q7 88,116,244
q7 92,112,248
q7 92,116,252
q0 4
q7 124,120,248
s_waitcnt vmcnt(19) lgkmcnt(0)
s_barrier
q7 124,124,252
q5 96,18,32768
q7 120,120,240
q5 100,19,32768
q7 120,124,244
q4 160,22,49152
q7 116,120,232
s_xor_b32 s86,s12,1152
q4 162,23,49408
s_add_u32 s86,s86,s64
q7 116,124,236
s_add_u32 m0,s88,65536
q4 164,22,53248
q7 112,120,224
q4 166,23,53504
q7 112,124,228
q2 8,0
q7 108,120,216
q7 108,124,220
q7 104,120,208
q7 104,124,212
q7 100,120,200
q7 100,124,204
q7 96,120,192
q2 9,1024
q7 96,124,196
q0 4
q7 128,96,192
q7 128,100,196
q5 104,18,34816
q7 132,96,200
q5 108,19,34816
q7 132,100,204
q4 168,23,49152
q7 136,96,208
q4 170,22,49408
q7 136,100,212
q4 172,23,53248
q7 140,96,216
q4 174,22,53504
q7 140,100,220
q2 10,2048
q7 144,96,224
buffer_store_dwordx4 v[44:47],v77,s[32:35],s52 offen offset:2048 sc1
q7 144,100,228
q7 148,96,232
q7 148,100,236
q7 152,96,240
s_xor_b32 s87,s13,5242880
q2 11,3072
s_add_u32 s87,s87,s65
q7 152,100,244
s_add_u32 m0,s89,65536
q7 156,96,248
q7 156,100,252
q0 4
q7 188,104,248
q7 188,108,252
q5 112,18,36864
q7 184,104,240
q5 116,19,36864
q7 184,108,244
q4 176,22,49664
q7 180,104,232
q4 178,23,49920
q7 180,108,236
q4 180,22,53760
q7 176,104,224
q4 182,23,54016
q7 176,108,228
q1 12,0
q7 172,104,216
q7 172,108,220
q7 168,104,208
q7 168,108,212
q1 13,1024
q7 164,104,200
q7 164,108,204
q7 160,104,192
q7 160,108,196
q0 4
q7 192,112,192
q7 192,116,196
q5 120,18,38912
q7 196,112,200
q5 124,19,38912
q7 196,116,204
q4 184,23,49664
q7 200,112,208
q4 186,22,49920
q7 200,116,212
q4 188,23,53760
q7 204,112,216
q4 190,22,54016
q7 204,116,220
q1 14,2048
q7 208,112,224
q7 208,116,228
q1 15,3072
q7 212,112,232
q7 212,116,236
q7 216,112,240
q7 216,116,244
q7 220,112,248
q7 220,116,252
q0 4
q7 252,120,248
s_waitcnt vmcnt(18) lgkmcnt(0)
s_barrier
q7 252,124,252
q5 96,16,0
q7 248,120,240
q5 100,17,0
q7 248,124,244
q7 244,120,232
s_add_u32 m0,s88,98304
s_xor_b32 s86,s12,132224
s_add_u32 s86,s86,s64
q7 244,124,236
q7 240,120,224
q7 240,124,228
q2 8,0
q7 236,120,216
q7 236,124,220
q7 232,120,208
q7 232,124,212
q7 228,120,200
q7 228,124,204
q7 224,120,192
q2 9,1024
q7 224,124,196
q0 0
q7 0,96,128
q7 0,100,132
q5 104,16,2048
q7 4,96,136
q5 108,17,2048
q7 4,100,140
q4 192,20,16384
q7 8,96,144
q4 194,21,16640
q7 8,100,148
q4 196,20,20480
q7 12,96,152
q4 198,21,20736
q7 12,100,156
q4 200,21,16384
q7 16,96,160
q4 202,20,16640
q7 16,100,164
q2 10,2048
q7 20,96,168
q7 20,100,172
q7 24,96,176
s_xor_b32 s87,s13,5243008
q2 11,3072
s_add_u32 s87,s87,s65
q7 24,100,180
s_add_u32 m0,s89,98304
q7 28,96,184
q7 28,100,188
q0 6
q7 60,104,184
q7 60,108,188
q5 112,16,4096
q7 56,104,176
q5 116,17,4096
q7 56,108,180
q4 204,21,20480
q7 52,104,168
q4 206,20,20736
q7 52,108,172
q4 208,20,16896
q7 48,104,160
q4 210,21,17152
q7 48,108,164
q4 212,20,20992
q7 44,104,152
q4 214,21,21248
q7 44,108,156
q1 12,0
q7 40,104,144
q7 40,108,148
q1 13,1024
q7 36,104,136
q7 36,108,140
q7 32,104,128
q7 32,108,132
q0 6
q7 64,112,128
q7 64,116,132
q5 120,16,6144
q7 68,112,136
q5 124,17,6144
q7 68,116,140
q4 216,21,16896
q7 72,112,144
q4 218,20,17152
q7 72,116,148
q4 220,21,20992
q7 76,112,152
q4 222,20,21248
q7 76,116,156
q1 14,2048
q7 80,112,160
q7 80,116,164
q1 15,3072
q7 84,112,168
q7 84,116,172
q7 88,112,176
q7 88,116,180
q7 92,112,184
q7 92,116,188
q0 4
q7 124,120,184
s_waitcnt vmcnt(17) lgkmcnt(0)
s_barrier
q7 124,124,188
q5 96,16,32768
q7 120,120,176
q5 100,17,32768
q7 120,124,180
q4 224,20,49152
q7 116,120,168
s_xor_b32 s86,s12,1280
q4 226,21,49408
s_add_u32 s86,s86,s64
q7 116,124,172
s_add_u32 m0,s88,0
q4 228,20,53248
q7 112,120,160
q4 230,21,53504
q7 112,124,164
q2 8,0
q7 108,120,152
q7 108,124,156
q7 104,120,144
q7 104,124,148
q7 100,120,136
q7 100,124,140
q7 96,120,128
q2 9,1024
q7 96,124,132
q0 4
q7 128,96,128
q7 128,100,132
q5 104,16,34816
q7 132,96,136
q5 108,17,34816
q7 132,100,140
q4 232,21,49152
q7 136,96,144
q4 234,20,49408
q7 136,100,148
q4 236,21,53248
q7 140,96,152
q4 238,20,53504
q7 140,100,156
q2 10,2048
q7 144,96,160
q7 144,100,164
q7 148,96,168
q7 148,100,172
q7 152,96,176
s_xor_b32 s87,s13,5767168
q2 11,3072
s_add_u32 s87,s87,s65
q7 152,100,180
s_add_u32 m0,s89,0
q7 156,96,184
q7 156,100,188
q0 4
q7 188,104,184
q7 188,108,188
q5 112,16,36864
q7 184,104,176
q5 116,17,36864
q7 184,108,180
q4 240,20,49664
q7 180,104,168
q4 242,21,49920
q7 180,108,172
q4 244,20,53760
q7 176,104,160
q4 246,21,54016
q7 176,108,164
q1 12,0
q7 172,104,152
q7 172,108,156
q7 168,104,144
q7 168,108,148
q1 13,1024
q7 164,104,136
q7 164,108,140
q7 160,104,128
q7 160,108,132
q0 4
q7 192,112,128
q7 192,116,132
q5 120,16,38912
q7 196,112,136
q5 124,17,38912
q7 196,116,140
q4 248,21,49664
q7 200,112,144
q4 250,20,49920
q7 200,116,148
q4 252,21,53760
q7 204,112,152
q4 254,20,54016
q7 204,116,156
q1 14,2048
q7 208,112,160
q7 208,116,164
q1 15,3072
q7 212,112,168
q7 212,116,172
q7 216,112,176
q7 216,116,180
q7 220,112,184
q7 220,116,188
s_waitcnt vmcnt(21) lgkmcnt(4)
q7 252,120,184
s_waitcnt vmcnt(16) lgkmcnt(0)
s_barrier
q7 252,124,188
s_lshl_b32 s48,s30,2
q5 96,18,0
s_add_u32 s48,s48,0
q7 248,120,176
q5 100,19,0
q7 248,124,180
LHH7_1kf:
s_cbranch_execz LHR7kf
s_cmp_eq_u32 s31,0
s_cselect_b32 s48,s48,1073741824
v_mov_b32 v24,s81
q7 244,120,168
s_add_u32 m0,s88,32768
s_xor_b32 s86,s12,132352
s_add_u32 s86,s86,s64
q7 244,124,172
v_mov_b32 v25,0
q7 240,120,160
buffer_store_dword v24,v25,s[32:35],s48 offen sc1
q7 240,124,164
q2 8,0
q7 236,120,152
q7 236,124,156
q7 232,120,144
q7 232,124,148
q7 228,120,136
q7 228,124,140
q7 224,120,128
q2 9,1024
q7 224,124,132
q0 0
q7 0,96,192
q7 0,100,196
q5 104,18,2048
q7 4,96,200
q5 108,19,2048
q7 4,100,204
q4 128,22,16384
q7 8,96,208
q4 130,23,16640
q7 8,100,212
)"
R"(q4 132,22,20480
q7 12,96,216
q4 134,23,20736
q7 12,100,220
q4 136,23,16384
q7 16,96,224
q4 138,22,16640
q7 16,100,228
q2 10,2048
q7 20,96,232
q7 20,100,236
q7 24,96,240
s_xor_b32 s87,s13,5767296
q2 11,3072
s_add_u32 s87,s87,s65
q7 24,100,244
s_add_u32 m0,s89,32768
q7 28,96,248
q7 28,100,252
q0 6
q7 60,104,248
q7 60,108,252
q5 112,18,4096
q7 56,104,240
q5 116,19,4096
q7 56,108,244
q4 140,23,20480
q7 52,104,232
q4 142,22,20736
q7 52,108,236
q4 144,22,16896
q7 48,104,224
q4 146,23,17152
q7 48,108,228
q4 148,22,20992
q7 44,104,216
q4 150,23,21248
q7 44,108,220
q1 12,0
q7 40,104,208
q7 40,108,212
q1 13,1024
q7 36,104,200
q7 36,108,204
q7 32,104,192
q7 32,108,196
q0 6
q7 64,112,192
q7 64,116,196
q5 120,18,6144
q7 68,112,200
q5 124,19,6144
q7 68,116,204
q4 152,23,16896
q7 72,112,208
q4 154,22,17152
q7 72,116,212
q4 156,23,20992
q7 76,112,216
q4 158,22,21248
q7 76,116,220
q1 14,2048
q7 80,112,224
q7 80,116,228
q1 15,3072
q7 84,112,232
q7 84,116,236
q7 88,112,240
q7 88,116,244
q7 92,112,248
q7 92,116,252
q0 4
q7 124,120,248
s_waitcnt vmcnt(17) lgkmcnt(0)
s_barrier
q7 124,124,252
q5 96,18,32768
q7 120,120,240
q5 100,19,32768
q7 120,124,244
q4 160,22,49152
q7 116,120,232
s_xor_b32 s86,s12,1408
q4 162,23,49408
s_add_u32 s86,s86,s64
q7 116,124,236
s_add_u32 m0,s88,65536
q4 164,22,53248
q7 112,120,224
q4 166,23,53504
q7 112,124,228
q2 8,0
q7 108,120,216
q7 108,124,220
q7 104,120,208
q7 104,124,212
q7 100,120,200
q7 100,124,204
q7 96,120,192
q2 9,1024
q7 96,124,196
q0 4
q7 128,96,192
q7 128,100,196
q5 104,18,34816
q7 132,96,200
q5 108,19,34816
q7 132,100,204
q4 168,23,49152
q7 136,96,208
q4 170,22,49408
q7 136,100,212
q4 172,23,53248
q7 140,96,216
q4 174,22,53504
q7 140,100,220
q2 10,2048
q7 144,96,224
q7 144,100,228
q7 148,96,232
q7 148,100,236
q7 152,96,240
s_xor_b32 s87,s13,6291456
q2 11,3072
s_add_u32 s87,s87,s65
q7 152,100,244
s_add_u32 m0,s89,65536
q7 156,96,248
q7 156,100,252
q0 4
q7 188,104,248
q7 188,108,252
q5 112,18,36864
q7 184,104,240
q5 116,19,36864
q7 184,108,244
q4 176,22,49664
q7 180,104,232
q4 178,23,49920
q7 180,108,236
q4 180,22,53760
q7 176,104,224
q4 182,23,54016
q7 176,108,228
q1 12,0
q7 172,104,216
q7 172,108,220
q7 168,104,208
q7 168,108,212
q1 13,1024
q7 164,104,200
q7 164,108,204
q7 160,104,192
q7 160,108,196
q0 4
q7 192,112,192
q7 192,116,196
q5 120,18,38912
q7 196,112,200
q5 124,19,38912
q7 196,116,204
q4 184,23,49664
q7 200,112,208
q4 186,22,49920
q7 200,116,212
q4 188,23,53760
q7 204,112,216
q4 190,22,54016
q7 204,116,220
q1 14,2048
q7 208,112,224
q7 208,116,228
q1 15,3072
q7 212,112,232
q7 212,116,236
q7 216,112,240
q7 216,116,244
q7 220,112,248
q7 220,116,252
q0 4
q7 252,120,248
s_waitcnt vmcnt(17) lgkmcnt(0)
s_barrier
q7 252,124,252
q5 96,16,0
q7 248,120,240
q5 100,17,0
q7 248,124,244
q7 244,120,232
s_add_u32 m0,s88,98304
s_xor_b32 s86,s12,132480
s_add_u32 s86,s86,s64
q7 244,124,236
q7 240,120,224
q7 240,124,228
q2 8,0
q7 236,120,216
q7 236,124,220
q7 232,120,208
q7 232,124,212
q7 228,120,200
q7 228,124,204
q7 224,120,192
q2 9,1024
q7 224,124,196
q0 0
q7 0,96,128
q7 0,100,132
q5 104,16,2048
q7 4,96,136
q5 108,17,2048
q7 4,100,140
q4 192,20,16384
q7 8,96,144
q4 194,21,16640
q7 8,100,148
q4 196,20,20480
q7 12,96,152
q4 198,21,20736
q7 12,100,156
q4 200,21,16384
q7 16,96,160
q4 202,20,16640
q7 16,100,164
q2 10,2048
q7 20,96,168
q7 20,100,172
q7 24,96,176
s_xor_b32 s87,s13,6291584
q2 11,3072
s_add_u32 s87,s87,s65
q7 24,100,180
s_mov_b32 s93,0
buffer_load_dwordx4 v[80:83],v2,s[32:35],s93 offen sc1
s_add_u32 m0,s89,98304
q7 28,96,184
q7 28,100,188
q0 6
q7 60,104,184
q7 60,108,188
q5 112,16,4096
q7 56,104,176
q5 116,17,4096
q7 56,108,180
q4 204,21,20480
q7 52,104,168
q4 206,20,20736
q7 52,108,172
q4 208,20,16896
q7 48,104,160
q4 210,21,17152
q7 48,108,164
q4 212,20,20992
q7 44,104,152
q4 214,21,21248
q7 44,108,156
q1 12,0
q7 40,104,144
q7 40,108,148
q1 13,1024
q7 36,104,136
q7 36,108,140
q7 32,104,128
q7 32,108,132
q0 6
q7 64,112,128
q7 64,116,132
q5 120,16,6144
q7 68,112,136
q5 124,17,6144
q7 68,116,140
q4 216,21,16896
q7 72,112,144
q4 218,20,17152
q7 72,116,148
q4 220,21,20992
q7 76,112,152
q4 222,20,21248
q7 76,116,156
q1 14,2048
q7 80,112,160
q7 80,116,164
q1 15,3072
q7 84,112,168
q7 84,116,172
q7 88,112,176
q7 88,116,180
q7 92,112,184
q7 92,116,188
q0 4
q7 124,120,184
s_waitcnt vmcnt(17) lgkmcnt(0)
s_barrier
q7 124,124,188
q5 96,16,32768
q7 120,120,176
q5 100,17,32768
q7 120,124,180
q4 224,20,49152
q7 116,120,168
s_xor_b32 s86,s12,1536
q4 226,21,49408
s_add_u32 s86,s86,s64
q7 116,124,172
s_add_u32 m0,s88,0
q4 228,20,53248
q7 112,120,160
q4 230,21,53504
q7 112,124,164
q2 8,0
q7 108,120,152
q7 108,124,156
q7 104,120,144
q7 104,124,148
q7 100,120,136
q7 100,124,140
q7 96,120,128
q2 9,1024
q7 96,124,132
q0 4
q7 128,96,128
q7 128,100,132
q5 104,16,34816
q7 132,96,136
q5 108,17,34816
q7 132,100,140
q4 232,21,49152
q7 136,96,144
q4 234,20,49408
q7 136,100,148
q4 236,21,53248
q7 140,96,152
q4 238,20,53504
q7 140,100,156
q2 10,2048
q7 144,96,160
q7 144,100,164
q7 148,96,168
q7 148,100,172
q7 152,96,176
s_xor_b32 s87,s13,6815744
q2 11,3072
s_add_u32 s87,s87,s65
q7 152,100,180
s_add_u32 m0,s89,0
q7 156,96,184
q7 156,100,188
q0 4
q7 188,104,184
q7 188,108,188
q5 112,16,36864
q7 184,104,176
q5 116,17,36864
q7 184,108,180
q4 240,20,49664
q7 180,104,168
q4 242,21,49920
q7 180,108,172
q4 244,20,53760
q7 176,104,160
q4 246,21,54016
q7 176,108,164
q1 12,0
q7 172,104,152
q7 172,108,156
q7 168,104,144
q7 168,108,148
q1 13,1024
q7 164,104,136
q7 164,108,140
q7 160,104,128
q7 160,108,132
q0 4
q7 192,112,128
q7 192,116,132
q5 120,16,38912
q7 196,112,136
q5 124,17,38912
q7 196,116,140
q4 248,21,49664
q7 200,112,144
q4 250,20,49920
q7 200,116,148
q4 252,21,53760
q7 204,112,152
q4 254,20,54016
q7 204,116,156
q1 14,2048
q7 208,112,160
q7 208,116,164
q1 15,3072
q7 212,112,168
q7 212,116,172
q7 216,112,176
q7 216,116,180
q7 220,112,184
q7 220,116,188
q0 4
q7 252,120,184
s_waitcnt vmcnt(17) lgkmcnt(0)
s_barrier
q7 252,124,188
q5 96,18,0
q7 248,120,176
q5 100,19,0
q7 248,124,180
q7 244,120,168
s_add_u32 m0,s88,32768
s_xor_b32 s86,s12,132608
s_add_u32 s86,s86,s64
q7 244,124,172
q7 240,120,160
q7 240,124,164
q2 8,0
q7 236,120,152
q7 236,124,156
q7 232,120,144
q7 232,124,148
q7 228,120,136
q7 228,124,140
q7 224,120,128
q2 9,1024
q7 224,124,132
q0 0
q7 0,96,192
q7 0,100,196
q5 104,18,2048
q7 4,96,200
q5 108,19,2048
q7 4,100,204
q4 128,22,16384
q7 8,96,208
q4 130,23,16640
q7 8,100,212
q4 132,22,20480
q7 12,96,216
q4 134,23,20736
q7 12,100,220
q4 136,23,16384
q7 16,96,224
q4 138,22,16640
q7 16,100,228
q2 10,2048
q7 20,96,232
q7 20,100,236
q7 24,96,240
s_xor_b32 s87,s13,6815872
q2 11,3072
s_add_u32 s87,s87,s65
q7 24,100,244
s_add_u32 m0,s89,32768
q7 28,96,248
q7 28,100,252
s_waitcnt vmcnt(16) lgkmcnt(6)
q7 60,104,248
v_xor_b32 v80,s81,v80
q7 60,108,252
q5 112,18,4096
v_xor_b32 v81,s81,v81
q7 56,104,240
q5 116,19,4096
v_xor_b32 v82,s81,v82
q7 56,108,244
q4 140,23,20480
v_xor_b32 v83,s81,v83
q7 52,104,232
q4 142,22,20736
v_or3_b32 v80,v80,v81,v82
q7 52,108,236
q4 144,22,16896
v_or_b32 v80,v80,v83
q7 48,104,224
q4 146,23,17152
v_cmp_ne_u32 vcc,0,v80
q7 48,108,228
q4 148,22,20992
s_cbranch_vccnz Lslow37kf_nb
Lback38kf_nb:
q7 44,104,216
s_or_b32 s49,s30,s31
q4 150,23,21248
q7 44,108,220
q1 12,0
q7 40,104,208
s_cmp_eq_u32 s49,0
s_mov_b32 s49,1073741824
s_cselect_b32 s49,32768,s49
q7 40,108,212
q1 13,1024
v_mov_b32 v30,s99
q7 36,104,200
v_mov_b32 v31,0
q7 36,108,204
s_lshl_b32 s92,s68,11
buffer_store_dword v30,v31,s[32:35],s49 offen sc1
s_add_u32 s92,s92,65536
q7 32,104,192
s_lshl3_add_u32 s48,s69,s70
s_lshl_b32 s48,s48,7
s_add_u32 s48,s48,s92
q7 32,108,196
q0 6
s_cmp_eq_u32 s31,0
s_cselect_b32 s91,s48,1073741824
q7 64,112,192
s_sub_u32 s50,15,s70
s_lshl_b32 s48,s50,3
s_add_u32 s49,s48,s31
q7 64,116,196
s_mul_i32 s49,s49,14336
q5 120,18,6144
s_add_u32 s78,s49,925696
q7 68,112,200
LHH7_2kf:
s_cbranch_execz LHR7kf
s_sub_u32 s49,s48,s31
q5 124,19,6144
s_add_u32 s49,s49,7
q7 68,116,204
s_mul_i32 s49,s49,14336
q4 152,23,16896
s_add_u32 s79,s49,925696
q7 72,112,208
q4 154,22,17152
q7 72,116,212
q4 156,23,20992
q7 76,112,216
q4 158,22,21248
q7 76,116,220
q1 14,2048
q7 80,112,224
q7 80,116,228
q1 15,3072
q7 84,112,232
q7 84,116,236
q7 88,112,240
q7 88,116,244
q7 92,112,248
q7 92,116,252
q0 4
q7 124,120,248
s_waitcnt vmcnt(17) lgkmcnt(0)
s_barrier
q7 124,124,252
q5 96,18,32768
q7 120,120,240
q5 100,19,32768
q7 120,124,244
q4 160,22,49152
q7 116,120,232
s_xor_b32 s86,s12,1664
q4 162,23,49408
s_add_u32 s86,s86,s64
q7 116,124,236
s_add_u32 m0,s88,65536
q4 164,22,53248
q7 112,120,224
q4 166,23,53504
q7 112,124,228
q2 8,0
q7 108,120,216
q7 108,124,220
q7 104,120,208
q7 104,124,212
q7 100,120,200
q7 100,124,204
q7 96,120,192
q2 9,1024
q7 96,124,196
q0 4
q7 128,96,192
q7 128,100,196
q5 104,18,34816
q7 132,96,200
q5 108,19,34816
q7 132,100,204
q4 168,23,49152
q7 136,96,208
q4 170,22,49408
q7 136,100,212
q4 172,23,53248
q7 140,96,216
q4 174,22,53504
q7 140,100,220
q2 10,2048
q7 144,96,224
q7 144,100,228
q7 148,96,232
q7 148,100,236
q7 152,96,240
s_xor_b32 s87,s13,7340032
q2 11,3072
s_add_u32 s87,s87,s65
q7 152,100,244
s_add_u32 m0,s89,65536
q7 156,96,248
q7 156,100,252
q0 4
q7 188,104,248
q7 188,108,252
q5 112,18,36864
q7 184,104,240
q5 116,19,36864
q7 184,108,244
q4 176,22,49664
q7 180,104,232
q4 178,23,49920
q7 180,108,236
q4 180,22,53760
q7 176,104,224
q4 182,23,54016
q7 176,108,228
q1 12,0
q7 172,104,216
q7 172,108,220
q7 168,104,208
q7 168,108,212
q1 13,1024
q7 164,104,200
q7 164,108,204
q7 160,104,192
q7 160,108,196
q0 4
q7 192,112,192
q7 192,116,196
q5 120,18,38912
q7 196,112,200
q5 124,19,38912
q7 196,116,204
q4 184,23,49664
q7 200,112,208
q4 186,22,49920
q7 200,116,212
q4 188,23,53760
q7 204,112,216
q4 190,22,54016
q7 204,116,220
q1 14,2048
q7 208,112,224
q7 208,116,228
q1 15,3072
q7 212,112,232
q7 212,116,236
q7 216,112,240
q7 216,116,244
q7 220,112,248
q7 220,116,252
q0 4
q7 252,120,248
s_waitcnt vmcnt(17) lgkmcnt(0)
s_barrier
q7 252,124,252
q5 96,16,0
q7 248,120,240
q5 100,17,0
q7 248,124,244
q7 244,120,232
s_add_u32 m0,s88,98304
s_xor_b32 s86,s12,132736
s_add_u32 s86,s86,s64
q7 244,124,236
q7 240,120,224
q7 240,124,228
q2 8,0
q7 236,120,216
q7 236,124,220
q7 232,120,208
q7 232,124,212
q7 228,120,200
q7 228,124,204
q7 224,120,192
q2 9,1024
q7 224,124,196
q0 0
q7 0,96,128
q7 0,100,132
q5 104,16,2048
q7 4,96,136
q5 108,17,2048
q7 4,100,140
q4 192,20,16384
q7 8,96,144
q4 194,21,16640
q7 8,100,148
q4 196,20,20480
q7 12,96,152
q4 198,21,20736
q7 12,100,156
q4 200,21,16384
q7 16,96,160
q4 202,20,16640
q7 16,100,164
q2 10,2048
q7 20,96,168
q7 20,100,172
q7 24,96,176
s_xor_b32 s87,s13,7340160
q2 11,3072
s_add_u32 s87,s87,s65
q7 24,100,180
s_add_u32 m0,s89,98304
q7 28,96,184
q7 28,100,188
q0 6
q7 60,104,184
q7 60,108,188
q5 112,16,4096
q7 56,104,176
q5 116,17,4096
q7 56,108,180
q4 204,21,20480
q7 52,104,168
q4 206,20,20736
q7 52,108,172
q4 208,20,16896
q7 48,104,160
q4 210,21,17152
q7 48,108,164
q4 212,20,20992
q7 44,104,152
q4 214,21,21248
q7 44,108,156
q1 12,0
q7 40,104,144
q7 40,108,148
q1 13,1024
q7 36,104,136
q7 36,108,140
q7 32,104,128
q7 32,108,132
q0 6
q7 64,112,128
q7 64,116,132
q5 120,16,6144
q7 68,112,136
q5 124,17,6144
q7 68,116,140
q4 216,21,16896
q7 72,112,144
q4 218,20,17152
q7 72,116,148
q4 220,21,20992
q7 76,112,152
q4 222,20,21248
q7 76,116,156
q1 14,2048
q7 80,112,160
q7 80,116,164
q1 15,3072
q7 84,112,168
q7 84,116,172
q7 88,112,176
q7 88,116,180
q7 92,112,184
q7 92,116,188
q0 4
q7 124,120,184
s_waitcnt vmcnt(16) lgkmcnt(0)
s_barrier
q7 124,124,188
q5 96,16,32768
q7 120,120,176
q5 100,17,32768
q7 120,124,180
q4 224,20,49152
q7 116,120,168
s_xor_b32 s86,s12,1792
q4 226,21,49408
s_add_u32 s86,s86,s64
q7 116,124,172
s_add_u32 m0,s88,0
q4 228,20,53248
q7 112,120,160
q4 230,21,53504
q7 112,124,164
q2 8,0
q7 108,120,152
q7 108,124,156
q7 104,120,144
q7 104,124,148
q7 100,120,136
q7 100,124,140
q7 96,120,128
q2 9,1024
q7 96,124,132
q0 4
q7 128,96,128
s_mov_b32 s46,LENDkf-LCODEkf
s_mov_b32 s45,s7
s_mov_b32 s44,s6
q7 128,100,132
s_mov_b32 s47,0x20000
q5 104,16,34816
s_and_b32 s54,s68,1
q7 132,96,136
s_lshl1_add_u32 s54,s70,s54
q5 108,17,34816
s_lshl2_add_u32 s54,s54,s31
q7 132,100,140
s_lshl_b32 s54,s54,12
q4 232,21,49152
v_and_b32 v31,31,v1
q7 136,96,144
q4 234,20,49408
v_lshlrev_b32 v31,7,v31
q7 136,100,148
q4 236,21,53248
q7 140,96,152
q4 238,20,53504
q7 140,100,156
q2 10,2048
q7 144,96,160
buffer_load_dword v31,v31,s[44:47],s54 offen
q7 144,100,164
q7 148,96,168
q7 148,100,172
q7 152,96,176
s_xor_b32 s87,s13,7864320
q2 11,3072
s_add_u32 s87,s87,s65
q7 152,100,180
s_add_u32 m0,s89,0
q7 156,96,184
q7 156,100,188
q0 4
q7 188,104,184
q7 188,108,188
q5 112,16,36864
q7 184,104,176
q5 116,17,36864
q7 184,108,180
q4 240,20,49664
q7 180,104,168
q4 242,21,49920
q7 180,108,172
q4 244,20,53760
q7 176,104,160
q4 246,21,54016
q7 176,108,164
q1 12,0
q7 172,104,152
q7 172,108,156
q7 168,104,144
q7 168,108,148
q1 13,1024
q7 164,104,136
q7 164,108,140
q7 160,104,128
q7 160,108,132
q0 4
q7 192,112,128
q7 192,116,132
q5 120,16,38912
q7 196,112,136
q5 124,17,38912
q7 196,116,140
q4 248,21,49664
q7 200,112,144
q4 250,20,49920
q7 200,116,148
q4 252,21,53760
q7 204,112,152
q4 254,20,54016
q7 204,116,156
q1 14,2048
q7 208,112,160
q7 208,116,164
q1 15,3072
q7 212,112,168
q7 212,116,172
q7 216,112,176
q7 216,116,180
q7 220,112,184
q7 220,116,188
q0 4
q7 252,120,184
s_waitcnt vmcnt(17) lgkmcnt(0)
s_barrier
q7 252,124,188
q5 96,18,0
q7 248,120,176
q5 100,19,0
q7 248,124,180
q7 244,120,168
s_add_u32 m0,s88,32768
s_xor_b32 s86,s12,132864
s_add_u32 s86,s86,s64
q7 244,124,172
q7 240,120,160
q7 240,124,164
q2 8,0
q7 236,120,152
q7 236,124,156
q7 232,120,144
q7 232,124,148
q7 228,120,136
q7 228,124,140
q7 224,120,128
q2 9,1024
q7 224,124,132
q0 0
q7 0,96,192
q7 0,100,196
q5 104,18,2048
q7 4,96,200
q5 108,19,2048
q7 4,100,204
q4 128,22,16384
q7 8,96,208
q4 130,23,16640
q7 8,100,212
q4 132,22,20480
q7 12,96,216
q4 134,23,20736
q7 12,100,220
q4 136,23,16384
q7 16,96,224
q4 138,22,16640
q7 16,100,228
q2 10,2048
q7 20,96,232
q7 20,100,236
q7 24,96,240
s_xor_b32 s87,s13,7864448
q2 11,3072
s_add_u32 s87,s87,s65
q7 24,100,244
s_add_u32 m0,s89,32768
q7 28,96,248
q7 28,100,252
q0 6
q7 60,104,248
q7 60,108,252
q5 112,18,4096
q7 56,104,240
q5 116,19,4096
q7 56,108,244
q4 140,23,20480
q7 52,104,232
q4 142,22,20736
q7 52,108,236
q4 144,22,16896
q7 48,104,224
q4 146,23,17152
q7 48,108,228
q4 148,22,20992
q7 44,104,216
q4 150,23,21248
q7 44,108,220
q1 12,0
q7 40,104,208
q7 40,108,212
q1 13,1024
q7 36,104,200
q7 36,108,204
q7 32,104,192
q7 32,108,196
q0 6
q7 64,112,192
q7 64,116,196
q5 120,18,6144
q7 68,112,200
q5 124,19,6144
q7 68,116,204
q4 152,23,16896
q7 72,112,208
s_add_u32 s48,s78,0
q4 154,22,17152
q7 72,116,212
q4 156,23,20992
q7 76,112,216
q4 158,22,21248
q7 76,116,220
q1 14,2048
q7 80,112,224
buffer_load_dwordx4 v[32:35],v2,s[32:35],s48 offen offset:0 sc1
q7 80,116,228
q1 15,3072
q7 84,112,232
q7 84,116,236
q7 88,112,240
q7 88,116,244
q7 92,112,248
q7 92,116,252
q0 4
q7 124,120,248
s_waitcnt vmcnt(18) lgkmcnt(0)
s_barrier
q7 124,124,252
q5 96,18,32768
q7 120,120,240
q5 100,19,32768
q7 120,124,244
q4 160,22,49152
q7 116,120,232
s_xor_b32 s86,s12,1920
q4 162,23,49408
s_add_u32 s86,s86,s64
q7 116,124,236
s_add_u32 m0,s88,65536
q4 164,22,53248
q7 112,120,224
q4 166,23,53504
q7 112,124,228
q2 8,0
q7 108,120,216
buffer_load_dwordx4 v[36:39],v2,s[32:35],s48 offen offset:1024 sc1
s_add_u32 s48,s79,0
q7 108,124,220
q7 104,120,208
q7 104,124,212
q7 100,120,200
q7 100,124,204
q7 96,120,192
q7 96,124,196
q0 4
q7 128,96,192
q7 128,100,196
q5 104,18,34816
q7 132,96,200
q5 108,19,34816
q7 132,100,204
q4 168,23,49152
q7 136,96,208
q4 170,22,49408
q7 136,100,212
q4 172,23,53248
q7 140,96,216
q4 174,22,53504
q7 140,100,220
q2 9,1024
q7 144,96,224
buffer_load_dwordx4 v[40:43],v2,s[32:35],s48 offen offset:0 sc1
q7 144,100,228
q7 148,96,232
q7 148,100,236
q7 152,96,240
q7 152,100,244
q7 156,96,248
q7 156,100,252
q0 4
q7 188,104,248
q7 188,108,252
q5 112,18,36864
q7 184,104,240
q5 116,19,36864
q7 184,108,244
q4 176,22,49664
q7 180,104,232
q4 178,23,49920
q7 180,108,236
LHH7_3kf:
s_cbranch_execz LHR7kf
q4 180,22,53760
q7 176,104,224
q4 182,23,54016
q7 176,108,228
q2 10,2048
q7 172,104,216
buffer_load_dwordx4 v[44:47],v2,s[32:35],s48 offen offset:1024 sc1
q7 172,108,220
q7 168,104,208
q7 168,108,212
q7 164,104,200
q7 164,108,204
q7 160,104,192
q7 160,108,196
q0 4
q7 192,112,192
q3 20
q7 192,116,196
q5 120,18,38912
q7 196,112,200
q5 124,19,38912
q7 196,116,204
s_add_u32 s48,s78,0
q4 184,23,49664
q7 200,112,208
q4 186,22,49920
q7 200,116,212
q4 188,23,53760
q7 204,112,216
q4 190,22,54016
q7 204,116,220
q2 11,3072
q7 208,112,224
buffer_load_dwordx4 v[48:51],v2,s[32:35],s48 offen offset:2048 sc1
q7 208,116,228
q7 212,112,232
q7 212,116,236
q7 216,112,240
q7 216,116,244
q7 220,112,248
q7 220,116,252
q0 4
q7 252,120,248
s_waitcnt vmcnt(17) lgkmcnt(0)
s_barrier
q7 252,124,252
q5 96,16,0
q7 248,120,240
q5 100,17,0
q7 248,124,244
buffer_load_dwordx4 v[52:55],v2,s[32:35],s48 offen offset:3072 sc1
s_add_u32 s48,s79,0
q7 244,120,232
s_add_u32 m0,s88,98304
s_xor_b32 s86,s12,132992
s_add_u32 s86,s86,s64
q7 244,124,236
q7 240,120,224
q7 240,124,228
q2 8,0
q7 236,120,216
q7 236,124,220
q7 232,120,208
q7 232,124,212
q7 228,120,200
q7 228,124,204
q7 224,120,192
buffer_load_dwordx4 v[56:59],v2,s[32:35],s48 offen offset:2048 sc1
q7 224,124,196
q0 0
q7 0,96,128
q7 0,100,132
q5 104,16,2048
q7 4,96,136
q5 108,17,2048
q7 4,100,140
q4 192,20,16384
q7 8,96,144
q4 194,21,16640
q7 8,100,148
q4 196,20,20480
q7 12,96,152
q4 198,21,20736
q7 12,100,156
q4 200,21,16384
q7 16,96,160
q4 202,20,16640
q7 16,100,164
q2 9,1024
q7 20,96,168
buffer_load_dwordx4 v[60:63],v2,s[32:35],s48 offen offset:3072 sc1
q7 20,100,172
q7 24,96,176
q7 24,100,180
q7 28,96,184
q7 28,100,188
q0 6
q7 60,104,184
q7 60,108,188
q5 112,16,4096
q7 56,104,176
q5 116,17,4096
q7 56,108,180
q4 204,21,20480
q7 52,104,168
q4 206,20,20736
q7 52,108,172
s_add_u32 s48,s78,4096
q4 208,20,16896
q7 48,104,160
q4 210,21,17152
q7 48,108,164
q4 212,20,20992
q7 44,104,152
q4 214,21,21248
q7 44,108,156
q2 10,2048
q7 40,104,144
buffer_load_dwordx4 v[64:67],v2,s[32:35],s48 offen offset:0 sc1
q7 40,108,148
q7 36,104,136
q7 36,108,140
q7 32,104,128
q7 32,108,132
q0 6
q7 64,112,128
q7 64,116,132
q5 120,16,6144
q7 68,112,136
q5 124,17,6144
q7 68,116,140
q4 216,21,16896
q7 72,112,144
q4 218,20,17152
q7 72,116,148
q4 220,21,20992
q7 76,112,152
q4 222,20,21248
q7 76,116,156
q2 11,3072
q7 80,112,160
buffer_load_dwordx4 v[68:71],v2,s[32:35],s48 offen offset:1024 sc1
s_add_u32 s48,s79,4096
q7 80,116,164
q7 84,112,168
q7 84,116,172
q7 88,112,176
q7 88,116,180
q7 92,112,184
q7 92,116,188
q0 4
q7 124,120,184
s_waitcnt vmcnt(17) lgkmcnt(0)
s_barrier
q7 124,124,188
q5 96,16,32768
q7 120,120,176
q5 100,17,32768
q7 120,124,180
q4 224,20,49152
q7 116,120,168
s_mov_b32 m0,s74
q4 226,21,49408
q7 116,124,172
q4 228,20,53248
q7 112,120,160
q4 230,21,53504
q7 112,124,164
qc 2,0
q7 108,120,152
buffer_load_dwordx4 v[72:75],v2,s[32:35],s48 offen offset:0 sc1
q7 108,124,156
q7 104,120,144
q7 104,124,148
q7 100,120,136
q7 100,124,140
qc 2,1024
q7 96,120,128
q7 96,124,132
q0 4
q7 128,96,128
q7 128,100,132
q5 104,16,34816
q7 132,96,136
q5 108,17,34816
q7 132,100,140
q4 232,21,49152
q7 136,96,144
q4 234,20,49408
q7 136,100,148
q4 236,21,53248
q7 140,96,152
q4 238,20,53504
q7 140,100,156
qc 2,2048
q7 144,96,160
buffer_load_dwordx4 v[76:79],v2,s[32:35],s48 offen offset:1024 sc1
q7 144,100,164
qc 2,3072
s_add_u32 m0,s74,4096
q7 148,96,168
q7 148,100,172
q7 152,96,176
q7 152,100,180
q7 156,96,184
q7 156,100,188
q0 4
q7 188,104,184
qc 3,0
q7 188,108,188
q5 112,16,36864
q7 184,104,176
q5 116,17,36864
q7 184,108,180
s_add_u32 s48,s78,4096
q4 240,20,49664
q7 180,104,168
q4 242,21,49920
q7 180,108,172
q4 244,20,53760
q7 176,104,160
q4 246,21,54016
q7 176,108,164
qc 3,1024
q7 172,104,152
buffer_load_dwordx4 v[80:83],v2,s[32:35],s48 offen offset:2048 sc1
q7 172,108,156
q7 168,104,144
q7 168,108,148
q7 164,104,136
qc 3,2048
q7 164,108,140
q7 160,104,128
q7 160,108,132
q0 4
q7 192,112,128
q7 192,116,132
q5 120,16,38912
q7 196,112,136
q5 124,17,38912
q7 196,116,140
q4 248,21,49664
q7 200,112,144
q4 250,20,49920
q7 200,116,148
q4 252,21,53760
q7 204,112,152
q4 254,20,54016
q7 204,116,156
s_add_u32 s72,s72,1
qc 3,3072
v_readlane_b32 s73,v5,s72
q7 208,112,160
v_readlane_b32 s74,v6,s72
v_readlane_b32 s75,v7,s72
q7 208,116,164
buffer_load_dwordx4 v[84:87],v2,s[32:35],s48 offen offset:3072 sc1
s_add_u32 s48,s79,4096
q7 212,112,168
q7 212,116,172
q7 216,112,176
q7 216,116,180
q7 220,112,184
q7 220,116,188
q0 4
q7 252,120,184
s_waitcnt vmcnt(22) lgkmcnt(0)
s_barrier
q7 252,124,188
q5 96,18,0
q7 248,120,176
q5 100,19,0
q7 248,124,180
q7 244,120,168
s_mov_b32 m0,s74
buffer_load_dwordx4 v[88:91],v2,s[32:35],s48 offen offset:2048 sc1
q7 244,124,172
q7 240,120,160
q7 240,124,164
qc 2,0
q7 236,120,152
q7 236,124,156
q7 232,120,144
q7 232,124,148
q7 228,120,136
q7 228,124,140
qc 2,1024
q7 224,120,128
q7 224,124,132
q0 0
buffer_load_dwordx4 v[92:95],v2,s[32:35],s48 offen offset:3072 sc1
q7 0,96,192
q7 0,100,196
q5 104,18,2048
q7 4,96,200
q5 108,19,2048
q7 4,100,204
qc 2,2048
q7 8,96,208
q7 8,100,212
q7 12,96,216
q7 12,100,220
q7 16,96,224
q7 16,100,228
qc 2,3072
s_add_u32 m0,s74,4096
q7 20,96,232
q7 20,100,236
q7 24,96,240
q7 24,100,244
q7 28,96,248
q7 28,100,252
q0 0
q7 60,104,248
qc 3,0
q7 60,108,252
q5 112,18,4096
q7 56,104,240
q5 116,19,4096
q7 56,108,244
q7 52,104,232
q7 52,108,236
q7 48,104,224
qc 3,1024
q7 48,108,228
q7 44,104,216
q7 44,108,220
q7 40,104,208
q7 40,108,212
q7 36,104,200
qc 3,2048
q7 36,108,204
q7 32,104,192
q7 32,108,196
q0 0
q7 64,112,192
q7 64,116,196
q5 120,18,6144
q7 68,112,200
q5 124,19,6144
q7 68,116,204
s_add_u32 s72,s72,1
qc 3,3072
v_readlane_b32 s73,v5,s72
q7 72,112,208
v_readlane_b32 s74,v6,s72
v_readlane_b32 s75,v7,s72
q7 72,116,212
s_add_u32 s49,s90,0
qd 24,0
qd 25,1
q7 76,112,216
qd 26,2
qd 27,3
q7 76,116,220
qd 28,32
qd 29,33
q7 80,112,224
qd 30,34
qd 31,35
q7 80,116,228
qb 8,24,25
qb 9,26,27
q7 84,112,232
qb 10,28,29
qb 11,30,31
q7 84,116,236
buffer_store_dwordx4 v[8:11],v2,s[32:35],s49 offen offset:0 sc1
q7 88,112,240
q7 88,116,244
q7 92,112,248
q7 92,116,252
q0 0
qd 24,4
qd 25,5
q7 124,120,248
s_waitcnt vmcnt(24) lgkmcnt(0)
s_barrier
qd 26,6
q7 124,124,252
q5 96,18,32768
qd 27,7
q7 120,120,240
q5 100,19,32768
qd 28,36
q7 120,124,244
qd 29,37
qd 30,38
q7 116,120,232
s_mov_b32 m0,s74
qd 31,39
qb 12,24,25
q7 116,124,236
qb 13,26,27
qb 14,28,29
q7 112,120,224
qb 15,30,31
q7 112,124,228
qc 2,0
q7 108,120,216
buffer_store_dwordx4 v[12:15],v2,s[32:35],s49 offen offset:2048 sc1
q7 108,124,220
q7 104,120,208
s_add_u32 s49,s90,4096
qd 24,8
qd 25,9
q7 104,124,212
qd 26,10
qd 27,11
q7 100,120,200
qd 28,40
qd 29,41
q7 100,124,204
qc 2,1024
qd 30,42
q7 96,120,192
qd 31,43
qb 8,24,25
q7 96,124,196
q0 0
qb 9,26,27
qb 10,28,29
q7 128,96,192
qb 11,30,31
q7 128,100,196
q5 104,18,34816
q7 132,96,200
q5 108,19,34816
q7 132,100,204
qc 2,2048
q7 136,96,208
buffer_store_dwordx4 v[8:11],v2,s[32:35],s49 offen offset:0 sc1
qd 24,12
q7 136,100,212
qd 25,13
qd 26,14
q7 140,96,216
qd 27,15
qd 28,44
q7 140,100,220
qd 29,45
qd 30,46
q7 144,96,224
qd 31,47
qb 12,24,25
q7 144,100,228
qc 2,3072
)"
R"(s_add_u32 m0,s74,4096
q7 148,96,232
qb 13,26,27
qb 14,28,29
q7 148,100,236
qb 15,30,31
q7 152,96,240
buffer_store_dwordx4 v[12:15],v2,s[32:35],s49 offen offset:2048 sc1
q7 152,100,244
q7 156,96,248
q7 156,100,252
q0 0
s_add_u32 s49,s90,8192
qd 24,16
q7 188,104,248
qc 3,0
qd 25,17
q7 188,108,252
q5 112,18,36864
qd 26,18
q7 184,104,240
q5 116,19,36864
qd 27,19
q7 184,108,244
qd 28,48
qd 29,49
q7 180,104,232
qd 30,50
qd 31,51
q7 180,108,236
qb 8,24,25
qb 9,26,27
q7 176,104,224
qc 3,1024
qb 10,28,29
q7 176,108,228
qb 11,30,31
q7 172,104,216
buffer_store_dwordx4 v[8:11],v2,s[32:35],s49 offen offset:0 sc1
q7 172,108,220
q7 168,104,208
qd 24,20
qd 25,21
q7 168,108,212
qd 26,22
qd 27,23
q7 164,104,200
qc 3,2048
qd 28,52
q7 164,108,204
qd 29,53
qd 30,54
q7 160,104,192
qd 31,55
qb 12,24,25
q7 160,108,196
q0 0
qb 13,26,27
qb 14,28,29
q7 192,112,192
qb 15,30,31
q7 192,116,196
q5 120,18,38912
q7 196,112,200
q5 124,19,38912
q7 196,116,204
s_add_u32 s72,s72,1
qc 3,3072
v_readlane_b32 s73,v5,s72
q7 200,112,208
v_readlane_b32 s74,v6,s72
v_readlane_b32 s75,v7,s72
q7 200,116,212
buffer_store_dwordx4 v[12:15],v2,s[32:35],s49 offen offset:2048 sc1
s_add_u32 s49,s90,12288
q7 204,112,216
qd 24,24
qd 25,25
q7 204,116,220
qd 26,26
qd 27,27
q7 208,112,224
LHH8_0kf:
s_cbranch_execz LHR8kf
qd 28,56
qd 29,57
q7 208,116,228
qd 30,58
qd 31,59
q7 212,112,232
qb 8,24,25
qb 9,26,27
q7 212,116,236
qb 10,28,29
qb 11,30,31
q7 216,112,240
buffer_store_dwordx4 v[8:11],v2,s[32:35],s49 offen offset:0 sc1
q7 216,116,244
q7 220,112,248
q7 220,116,252
q0 0
qd 24,28
qd 25,29
q7 252,120,248
q3 26
s_barrier
v_add_u32 v4,s75,v2
q7 252,124,252
qd 26,30
qd 27,31
q7 248,120,240
q5 96,4,0
qd 28,60
q7 248,124,244
qd 29,61
qd 30,62
q7 244,120,232
q5 100,4,1024
qd 31,63
q7 244,124,236
qb 12,24,25
qb 13,26,27
q7 240,120,224
q5 104,4,2048
qb 14,28,29
q7 240,124,228
qb 15,30,31
q7 236,120,216
q5 108,4,3072
q7 236,124,220
buffer_store_dwordx4 v[12:15],v2,s[32:35],s49 offen offset:2048 sc1
q7 232,120,208
q5 112,4,4096
q7 232,124,212
q7 228,120,200
q5 116,4,5120
q7 228,124,204
q7 224,120,192
q7 224,124,196
s_lshl_b32 s80,s83,14
s_add_u32 s80,s80,47325184
s_lshl_b32 s93,s83,7
s_add_u32 s93,s93,196608
s_waitcnt vmcnt(49) lgkmcnt(4)
qq 192,96,40
s_mov_b32 m0,s74
q5 120,4,6144
qf 192,100,44
qc 2,0
qq 128,100,36
s_add_u32 s49,s90,32768
q5 124,4,7168
qd 24,128
qf 128,96,32
q0 4
qd 25,129
qd 26,130
qq 132,104,32
q5 96,4,8192
qd 27,131
qf 132,108,36
qd 28,160
qd 29,161
qq 196,108,44
q5 100,4,9216
qd 30,162
qf 196,104,40
q0 4
qc 2,1024
qd 31,163
qq 200,112,40
q5 104,4,10240
qb 8,24,25
qf 200,116,44
qb 9,26,27
qb 10,28,29
qq 136,116,36
q5 108,4,11264
qb 11,30,31
qf 136,112,32
q0 4
qc 2,2048
qq 140,120,32
q5 112,4,12288
qf 140,124,36
buffer_store_dwordx4 v[8:11],v2,s[32:35],s49 offen offset:0 sc1
qq 204,124,44
q5 116,4,13312
qf 204,120,40
q0 4
qq 208,96,40
q5 120,4,14336
qf 208,100,44
qc 2,3072
s_add_u32 m0,s74,4096
qq 144,100,36
q5 124,4,15360
qd 24,132
qf 144,96,32
q0 4
qd 25,133
qd 26,134
qq 148,104,32
q5 96,4,16384
qd 27,135
qf 148,108,36
qd 28,164
qd 29,165
qq 212,108,44
q5 100,4,17408
qd 30,166
qf 212,104,40
q0 4
qc 3,0
qd 31,167
qq 216,112,40
q5 104,4,18432
qb 12,24,25
qf 216,116,44
qb 13,26,27
qb 14,28,29
qq 152,116,36
q5 108,4,19456
qb 15,30,31
qf 152,112,32
q0 4
qc 3,1024
qq 156,120,32
q5 112,4,20480
qf 156,124,36
buffer_store_dwordx4 v[12:15],v2,s[32:35],s49 offen offset:2048 sc1
qq 220,124,44
q5 116,4,21504
qf 220,120,40
q0 4
qq 224,96,40
q5 120,4,22528
qf 224,100,44
qc 3,2048
qq 160,100,36
s_add_u32 s49,s90,36864
q5 124,4,23552
qd 24,136
qf 160,96,32
q0 4
qd 25,137
qd 26,138
qq 164,104,32
q5 96,4,24576
qd 27,139
qf 164,108,36
qd 28,168
qd 29,169
qq 228,108,44
q5 100,4,25600
qd 30,170
qf 228,104,40
q0 4
qc 3,3072
s_add_u32 s72,s72,1
qq 232,112,40
q5 104,4,26624
v_readlane_b32 s73,v5,s72
qf 232,116,44
v_readlane_b32 s74,v6,s72
v_readlane_b32 s75,v7,s72
qq 168,116,36
q5 108,4,27648
qd 31,171
qf 168,112,32
q0 4
qb 8,24,25
qb 9,26,27
qq 172,120,32
q5 112,4,28672
qb 10,28,29
qf 172,124,36
qb 11,30,31
qq 236,124,44
q5 116,4,29696
qf 236,120,40
q0 4
buffer_store_dwordx4 v[8:11],v2,s[32:35],s49 offen offset:0 sc1
qq 240,96,40
q5 120,4,30720
qf 240,100,44
qq 176,100,36
q5 124,4,31744
qd 24,140
qf 176,96,32
s_waitcnt vmcnt(27) lgkmcnt(4)
s_barrier
v_add_u32 v4,s75,v2
qq 180,104,32
q5 96,4,0
qd 25,141
qf 180,108,36
qd 26,142
qd 27,143
qq 244,108,44
q5 100,4,1024
qd 28,172
qf 244,104,40
q0 4
qd 29,173
qd 30,174
qq 248,112,40
q5 104,4,2048
qd 31,175
qf 248,116,44
qb 12,24,25
qb 13,26,27
qq 184,116,36
q5 108,4,3072
qb 14,28,29
qf 184,112,32
q0 4
qb 15,30,31
qq 188,120,32
q5 112,4,4096
qf 188,124,36
buffer_store_dwordx4 v[12:15],v2,s[32:35],s49 offen offset:2048 sc1
qq 252,124,44
q5 116,4,5120
qf 252,120,40
s_waitcnt vmcnt(54) lgkmcnt(4)
qf 192,96,56
s_mov_b32 m0,s74
q5 120,4,6144
qf 192,100,60
s_mov_b64 exec,0
s_cmp_eq_u32 s31,0
s_cbranch_scc1 LHH9_0kf
s_cmp_eq_u32 s31,1
s_cbranch_scc1 LHH9_1kf
s_cmp_eq_u32 s31,2
s_cbranch_scc1 LHH9_2kf
s_branch LHH9_3kf
LHR9kf:
s_mov_b64 exec,-1
qc 2,0
qf 128,100,52
s_add_u32 s49,s90,40960
q5 124,4,7168
qd 24,144
qf 128,96,48
q0 4
qd 25,145
qd 26,146
qf 132,104,48
q5 96,4,8192
qd 27,147
qf 132,108,52
s_add_u32 s48,s78,8192
buffer_load_dwordx4 v[32:35],v2,s[32:35],s48 offen offset:0 sc1
qd 28,176
qf 196,108,60
q5 100,4,9216
qd 29,177
qf 196,104,56
q0 4
qc 2,1024
qd 30,178
qf 200,112,56
q5 104,4,10240
qd 31,179
qf 200,116,60
qb 8,24,25
qb 9,26,27
qf 136,116,52
q5 108,4,11264
qb 10,28,29
qf 136,112,48
q0 4
qc 2,2048
qb 11,30,31
qf 140,120,48
q5 112,4,12288
qf 140,124,52
buffer_store_dwordx4 v[8:11],v2,s[32:35],s49 offen offset:0 sc1
qf 204,124,60
q5 116,4,13312
qf 204,120,56
q0 4
qf 208,96,56
q5 120,4,14336
qf 208,100,60
qc 2,3072
s_add_u32 m0,s74,4096
qf 144,100,52
q5 124,4,15360
qd 24,148
qf 144,96,48
q0 4
buffer_load_dwordx4 v[36:39],v2,s[32:35],s48 offen offset:1024 sc1
s_add_u32 s48,s79,8192
qf 148,104,48
q5 96,4,16384
qd 25,149
qf 148,108,52
qd 26,150
qd 27,151
qf 212,108,60
q5 100,4,17408
qd 28,180
qf 212,104,56
q0 4
qc 3,0
qd 29,181
qf 216,112,56
q5 104,4,18432
qd 30,182
qf 216,116,60
qd 31,183
qb 12,24,25
qf 152,116,52
q5 108,4,19456
qb 13,26,27
qf 152,112,48
q0 4
qc 3,1024
qb 14,28,29
qf 156,120,48
q5 112,4,20480
qb 15,30,31
qf 156,124,52
buffer_store_dwordx4 v[12:15],v2,s[32:35],s49 offen offset:2048 sc1
qf 220,124,60
q5 116,4,21504
qf 220,120,56
q0 4
buffer_load_dwordx4 v[40:43],v2,s[32:35],s48 offen offset:0 sc1
qf 224,96,56
q5 120,4,22528
qf 224,100,60
qc 3,2048
qf 160,100,52
s_add_u32 s49,s90,45056
q5 124,4,23552
qd 24,152
qf 160,96,48
q0 4
qd 25,153
qd 26,154
qf 164,104,48
q5 96,4,24576
qd 27,155
qf 164,108,52
qd 28,184
qd 29,185
qf 228,108,60
q5 100,4,25600
qd 30,186
qf 228,104,56
q0 4
qc 3,3072
s_add_u32 s72,s72,1
qf 232,112,56
q5 104,4,26624
v_readlane_b32 s73,v5,s72
qf 232,116,60
v_readlane_b32 s74,v6,s72
v_readlane_b32 s75,v7,s72
qf 168,116,52
q5 108,4,27648
qd 31,187
qf 168,112,48
q0 4
buffer_load_dwordx4 v[44:47],v2,s[32:35],s48 offen offset:1024 sc1
qb 8,24,25
qf 172,120,48
q5 112,4,28672
qb 9,26,27
qf 172,124,52
qb 10,28,29
qb 11,30,31
qf 236,124,60
q5 116,4,29696
qf 236,120,56
q0 4
buffer_store_dwordx4 v[8:11],v2,s[32:35],s49 offen offset:0 sc1
qf 240,96,56
q5 120,4,30720
qf 240,100,60
qf 176,100,52
q5 124,4,31744
qd 24,156
qf 176,96,48
s_waitcnt vmcnt(30) lgkmcnt(4)
s_barrier
v_add_u32 v4,s75,v2
qf 180,104,48
s_mov_b64 exec,0
s_cmp_eq_u32 s31,0
s_cbranch_scc1 LHH10_0kf
s_cmp_eq_u32 s31,1
s_cbranch_scc1 LHH10_1kf
s_cmp_eq_u32 s31,2
s_cbranch_scc1 LHH10_2kf
s_branch LHH10_3kf
LHR10kf:
s_mov_b64 exec,-1
q5 96,4,0
qd 25,157
qf 180,108,52
qd 26,158
qd 27,159
qf 244,108,60
q5 100,4,1024
qd 28,188
qf 244,104,56
q0 4
qd 29,189
qd 30,190
qf 248,112,56
q5 104,4,2048
qd 31,191
qf 248,116,60
qb 12,24,25
qb 13,26,27
qf 184,116,52
q5 108,4,3072
qb 14,28,29
qf 184,112,48
q0 4
qb 15,30,31
qf 188,120,48
q5 112,4,4096
qf 188,124,52
buffer_store_dwordx4 v[12:15],v2,s[32:35],s49 offen offset:2048 sc1
qf 252,124,60
q5 116,4,5120
qf 252,120,56
s_waitcnt vmcnt(61) lgkmcnt(4)
qf 192,96,72
s_mov_b32 m0,s74
q5 120,4,6144
qf 192,100,76
qc 2,0
qf 128,100,68
s_add_u32 s49,s90,0
q5 124,4,7168
qd 24,64
qf 128,96,64
q0 4
qd 25,65
qd 26,66
qf 132,104,64
q5 96,4,8192
qd 27,67
qf 132,108,68
s_add_u32 s48,s78,8192
buffer_load_dwordx4 v[48:51],v2,s[32:35],s48 offen offset:2048 sc1
qd 28,96
qf 196,108,76
q5 100,4,9216
qd 29,97
qf 196,104,72
q0 4
qc 2,1024
qd 30,98
qf 200,112,72
q5 104,4,10240
qd 31,99
qf 200,116,76
qb 8,24,25
qb 9,26,27
qf 136,116,68
q5 108,4,11264
qb 10,28,29
qf 136,112,64
q0 4
qc 2,2048
qb 11,30,31
qf 140,120,64
q5 112,4,12288
qf 140,124,68
buffer_store_dwordx4 v[8:11],v2,s[32:35],s49 offen offset:1024 sc1
qf 204,124,76
q5 116,4,13312
qf 204,120,72
q0 4
qf 208,96,72
q5 120,4,14336
qf 208,100,76
qc 2,3072
s_add_u32 m0,s74,4096
qf 144,100,68
q5 124,4,15360
qd 24,68
qf 144,96,64
q0 4
buffer_load_dwordx4 v[52:55],v2,s[32:35],s48 offen offset:3072 sc1
s_add_u32 s48,s79,8192
qf 148,104,64
q5 96,4,16384
qd 25,69
qf 148,108,68
qd 26,70
qd 27,71
qf 212,108,76
q5 100,4,17408
qd 28,100
qf 212,104,72
q0 4
qc 3,0
qd 29,101
qf 216,112,72
q5 104,4,18432
qd 30,102
qf 216,116,76
qd 31,103
qb 12,24,25
qf 152,116,68
q5 108,4,19456
qb 13,26,27
qf 152,112,64
q0 4
qc 3,1024
qb 14,28,29
qf 156,120,64
q5 112,4,20480
qb 15,30,31
qf 156,124,68
buffer_store_dwordx4 v[12:15],v2,s[32:35],s49 offen offset:3072 sc1
qf 220,124,76
q5 116,4,21504
qf 220,120,72
q0 4
buffer_load_dwordx4 v[56:59],v2,s[32:35],s48 offen offset:2048 sc1
qf 224,96,72
q5 120,4,22528
qf 224,100,76
qc 3,2048
qf 160,100,68
LHH8_1kf:
s_cbranch_execz LHR8kf
s_add_u32 s49,s90,4096
q5 124,4,23552
qd 24,72
qf 160,96,64
q0 4
qd 25,73
qd 26,74
qf 164,104,64
q5 96,4,24576
qd 27,75
qf 164,108,68
qd 28,104
qd 29,105
qf 228,108,76
q5 100,4,25600
qd 30,106
qf 228,104,72
q0 4
qc 3,3072
s_add_u32 s72,s72,1
qf 232,112,72
q5 104,4,26624
v_readlane_b32 s73,v5,s72
qf 232,116,76
v_readlane_b32 s74,v6,s72
v_readlane_b32 s75,v7,s72
qf 168,116,68
q5 108,4,27648
qd 31,107
qf 168,112,64
q0 4
buffer_load_dwordx4 v[60:63],v2,s[32:35],s48 offen offset:3072 sc1
qb 8,24,25
qf 172,120,64
s_nop 0
s_nop 0
s_nop 0
s_nop 0
s_nop 0
s_nop 0
s_nop 0
s_nop 0
s_nop 0
q5 112,4,28672
qb 9,26,27
qf 172,124,68
qb 10,28,29
qb 11,30,31
qf 236,124,76
q5 116,4,29696
qf 236,120,72
q0 4
buffer_store_dwordx4 v[8:11],v2,s[32:35],s49 offen offset:1024 sc1
qf 240,96,72
q5 120,4,30720
qf 240,100,76
qf 176,100,68
q5 124,4,31744
qd 24,76
qf 176,96,64
s_waitcnt vmcnt(33) lgkmcnt(4)
s_barrier
v_add_u32 v4,s75,v2
qf 180,104,64
q5 96,4,0
qd 25,77
qf 180,108,68
qd 26,78
qd 27,79
qf 244,108,76
q5 100,4,1024
qd 28,108
qf 244,104,72
q0 4
qd 29,109
qd 30,110
qf 248,112,72
q5 104,4,2048
qd 31,111
qf 248,116,76
qb 12,24,25
qb 13,26,27
qf 184,116,68
q5 108,4,3072
qb 14,28,29
qf 184,112,64
q0 4
qb 15,30,31
qf 188,120,64
q5 112,4,4096
qf 188,124,68
buffer_store_dwordx4 v[12:15],v2,s[32:35],s49 offen offset:3072 sc1
qf 252,124,76
q5 116,4,5120
qf 252,120,72
s_waitcnt vmcnt(63) lgkmcnt(4)
qf 192,96,88
s_mov_b32 m0,s74
q5 120,4,6144
qf 192,100,92
qc 2,0
qf 128,100,84
s_add_u32 s49,s90,8192
q5 124,4,7168
qd 24,80
qf 128,96,80
q0 4
qd 25,81
qd 26,82
qf 132,104,80
q5 96,4,8192
qd 27,83
qf 132,108,84
s_add_u32 s48,s78,12288
buffer_load_dwordx4 v[64:67],v2,s[32:35],s48 offen offset:0 sc1
qd 28,112
qf 196,108,92
q5 100,4,9216
qd 29,113
qf 196,104,88
q0 4
qc 2,1024
qd 30,114
qf 200,112,88
q5 104,4,10240
qd 31,115
qf 200,116,92
qb 8,24,25
qb 9,26,27
qf 136,116,84
q5 108,4,11264
qb 10,28,29
qf 136,112,80
q0 4
qc 2,2048
qb 11,30,31
qf 140,120,80
q5 112,4,12288
qf 140,124,84
buffer_store_dwordx4 v[8:11],v2,s[32:35],s49 offen offset:1024 sc1
qf 204,124,92
q5 116,4,13312
qf 204,120,88
q0 4
qf 208,96,88
q5 120,4,14336
qf 208,100,92
qc 2,3072
s_add_u32 m0,s74,4096
qf 144,100,84
q5 124,4,15360
qd 24,84
qf 144,96,80
q0 4
buffer_load_dwordx4 v[68:71],v2,s[32:35],s48 offen offset:1024 sc1
s_add_u32 s48,s79,12288
qf 148,104,80
q5 96,4,16384
qd 25,85
qf 148,108,84
qd 26,86
qd 27,87
qf 212,108,92
q5 100,4,17408
qd 28,116
qf 212,104,88
q0 4
qc 3,0
qd 29,117
qf 216,112,88
q5 104,4,18432
qd 30,118
qf 216,116,92
qd 31,119
qb 12,24,25
qf 152,116,84
q5 108,4,19456
qb 13,26,27
qf 152,112,80
q0 4
qc 3,1024
qb 14,28,29
qf 156,120,80
q5 112,4,20480
qb 15,30,31
qf 156,124,84
buffer_store_dwordx4 v[12:15],v2,s[32:35],s49 offen offset:3072 sc1
qf 220,124,92
q5 116,4,21504
qf 220,120,88
q0 4
buffer_load_dwordx4 v[72:75],v2,s[32:35],s48 offen offset:0 sc1
qf 224,96,88
q5 120,4,22528
qf 224,100,92
qc 3,2048
qf 160,100,84
s_add_u32 s49,s90,12288
q5 124,4,23552
qd 24,88
qf 160,96,80
q0 4
qd 25,89
qd 26,90
qf 164,104,80
q5 96,4,24576
qd 27,91
qf 164,108,84
qd 28,120
qd 29,121
qf 228,108,92
q5 100,4,25600
qd 30,122
qf 228,104,88
q0 4
qc 3,3072
s_add_u32 s72,s72,1
qf 232,112,88
q5 104,4,26624
v_readlane_b32 s73,v5,s72
qf 232,116,92
v_readlane_b32 s74,v6,s72
v_readlane_b32 s75,v7,s72
qf 168,116,84
q5 108,4,27648
qd 31,123
qf 168,112,80
q0 4
buffer_load_dwordx4 v[76:79],v2,s[32:35],s48 offen offset:1024 sc1
qb 8,24,25
qf 172,120,80
q5 112,4,28672
qb 9,26,27
qf 172,124,84
qb 10,28,29
qb 11,30,31
qf 236,124,92
q5 116,4,29696
qf 236,120,88
q0 4
buffer_store_dwordx4 v[8:11],v2,s[32:35],s49 offen offset:1024 sc1
qf 240,96,88
q5 120,4,30720
qf 240,100,92
qf 176,100,84
q5 124,4,31744
qd 24,92
qf 176,96,80
s_waitcnt vmcnt(34) lgkmcnt(4)
s_barrier
v_add_u32 v4,s75,v2
qf 180,104,80
q5 96,4,0
qd 25,93
qf 180,108,84
qd 26,94
qd 27,95
qf 244,108,92
q5 100,4,1024
qd 28,124
qf 244,104,88
q0 4
qd 29,125
qd 30,126
qf 248,112,88
q5 104,4,2048
qd 31,127
qf 248,116,92
qb 12,24,25
qb 13,26,27
qf 184,116,84
q5 108,4,3072
qb 14,28,29
qf 184,112,80
q0 4
qb 15,30,31
qf 188,120,80
q5 112,4,4096
qf 188,124,84
buffer_store_dwordx4 v[12:15],v2,s[32:35],s49 offen offset:3072 sc1
qf 252,124,92
q5 116,4,5120
qf 252,120,88
s_waitcnt vmcnt(34) lgkmcnt(4)
qf 192,96,40
s_add_u32 s72,s72,1
q5 120,4,6144
v_readlane_b32 s73,v5,s72
qf 192,100,44
s_lshl_b32 s50,s70,3
v_readlane_b32 s74,v6,s72
v_readlane_b32 s75,v7,s72
qf 128,100,36
s_add_u32 s51,s50,s31
q5 124,4,7168
s_mul_i32 s51,s51,14336
qf 128,96,32
q0 4
s_add_u32 s78,s51,925696
s_sub_u32 s51,s50,s31
qf 132,104,32
s_add_u32 s51,s51,7
q5 96,4,8192
s_mul_i32 s51,s51,14336
qf 132,108,36
s_add_u32 s48,s78,12288
buffer_load_dwordx4 v[80:83],v2,s[32:35],s48 offen offset:0 sc1
s_add_u32 s79,s51,925696
qf 196,108,44
s_add_u32 s49,s90,32768
q5 100,4,9216
qd 24,192
qf 196,104,40
q0 4
qd 25,193
qd 26,194
qf 200,112,40
q5 104,4,10240
qd 27,195
qf 200,116,44
qd 28,224
qd 29,225
qf 136,116,36
q5 108,4,11264
qd 30,226
qf 136,112,32
q0 4
qd 31,227
qb 8,24,25
qf 140,120,32
q5 112,4,12288
qb 9,26,27
qf 140,124,36
qb 10,28,29
qb 11,30,31
qf 204,124,44
q5 116,4,13312
qf 204,120,40
q0 4
buffer_store_dwordx4 v[8:11],v2,s[32:35],s49 offen offset:1024 sc1
qf 208,96,40
q5 120,4,14336
qf 208,100,44
buffer_load_dwordx4 v[84:87],v2,s[32:35],s48 offen offset:1024 sc1
s_add_u32 s48,s79,12288
qf 144,100,36
q5 124,4,15360
qd 24,196
qf 144,96,32
q0 4
qd 25,197
qd 26,198
qf 148,104,32
q5 96,4,16384
qd 27,199
qf 148,108,36
qd 28,228
qd 29,229
qf 212,108,44
q5 100,4,17408
qd 30,230
qf 212,104,40
q0 4
qd 31,231
qb 12,24,25
qf 216,112,40
q5 104,4,18432
qb 13,26,27
qf 216,116,44
qb 14,28,29
qb 15,30,31
qf 152,116,36
q5 108,4,19456
qf 152,112,32
q0 4
buffer_store_dwordx4 v[12:15],v2,s[32:35],s49 offen offset:3072 sc1
qf 156,120,32
q5 112,4,20480
qf 156,124,36
buffer_load_dwordx4 v[88:91],v2,s[32:35],s48 offen offset:0 sc1
qf 220,124,44
q5 116,4,21504
qf 220,120,40
q0 4
qf 224,96,40
q5 120,4,22528
qf 224,100,44
qf 160,100,36
s_add_u32 s49,s90,36864
q5 124,4,23552
qd 24,200
qf 160,96,32
q0 4
qd 25,201
qd 26,202
qf 164,104,32
q5 96,4,24576
qd 27,203
qf 164,108,36
qd 28,232
qd 29,233
qf 228,108,44
q5 100,4,25600
qd 30,234
qf 228,104,40
q0 4
qd 31,235
qb 8,24,25
qf 232,112,40
q5 104,4,26624
qb 9,26,27
qf 232,116,44
buffer_load_dwordx4 v[92:95],v2,s[32:35],s48 offen offset:1024 sc1
qb 10,28,29
qf 168,116,36
q5 108,4,27648
qb 11,30,31
qf 168,112,32
q0 4
buffer_store_dwordx4 v[8:11],v2,s[32:35],s49 offen offset:1024 sc1
qf 172,120,32
q5 112,4,28672
qf 172,124,36
qf 236,124,44
q5 116,4,29696
qf 236,120,40
q0 4
qf 240,96,40
q5 120,4,30720
qf 240,100,44
qf 176,100,36
q5 124,4,31744
qd 24,204
qf 176,96,32
s_waitcnt vmcnt(26) lgkmcnt(4)
s_barrier
v_add_u32 v4,s75,v2
qf 180,104,32
q5 96,4,0
qd 25,205
qf 180,108,36
qd 26,206
qd 27,207
qf 244,108,44
q5 100,4,1024
qd 28,236
qf 244,104,40
q0 4
qd 29,237
qd 30,238
qf 248,112,40
q5 104,4,2048
qd 31,239
qf 248,116,44
qb 12,24,25
qb 13,26,27
qf 184,116,36
q5 108,4,3072
qb 14,28,29
qf 184,112,32
q0 4
qb 15,30,31
qf 188,120,32
q5 112,4,4096
qf 188,124,36
buffer_store_dwordx4 v[12:15],v2,s[32:35],s49 offen offset:3072 sc1
qf 252,124,44
q5 116,4,5120
qf 252,120,40
s_waitcnt vmcnt(26) lgkmcnt(4)
qf 192,96,56
s_add_u32 s72,s72,1
q5 120,4,6144
v_readlane_b32 s73,v5,s72
qf 192,100,60
v_readlane_b32 s74,v6,s72
v_readlane_b32 s75,v7,s72
qf 128,100,52
s_add_u32 s49,s90,40960
q5 124,4,7168
qd 24,208
qf 128,96,48
q0 4
qd 25,209
qd 26,210
qf 132,104,48
q5 96,4,8192
qd 27,211
qf 132,108,52
s_add_u32 s48,s78,8192
buffer_load_dwordx4 v[32:35],v2,s[32:35],s48 offen offset:2048 sc1
qd 28,240
qf 196,108,60
q5 100,4,9216
qd 29,241
qf 196,104,56
q0 4
qd 30,242
qd 31,243
qf 200,112,56
q5 104,4,10240
qb 8,24,25
qf 200,116,60
qb 9,26,27
qb 10,28,29
qf 136,116,52
q5 108,4,11264
qb 11,30,31
qf 136,112,48
q0 4
buffer_store_dwordx4 v[8:11],v2,s[32:35],s49 offen offset:1024 sc1
qf 140,120,48
q5 112,4,12288
qf 140,124,52
qf 204,124,60
q5 116,4,13312
qf 204,120,56
q0 4
qf 208,96,56
q5 120,4,14336
qf 208,100,60
buffer_load_dwordx4 v[36:39],v2,s[32:35],s48 offen offset:3072 sc1
s_add_u32 s48,s79,8192
qf 144,100,52
q5 124,4,15360
qd 24,212
qf 144,96,48
q0 4
qd 25,213
qd 26,214
qf 148,104,48
q5 96,4,16384
qd 27,215
qf 148,108,52
qd 28,244
qd 29,245
qf 212,108,60
q5 100,4,17408
qd 30,246
qf 212,104,56
q0 4
qd 31,247
qb 12,24,25
qf 216,112,56
q5 104,4,18432
qb 13,26,27
qf 216,116,60
LHH8_2kf:
s_cbranch_execz LHR8kf
qb 14,28,29
qb 15,30,31
qf 152,116,52
q5 108,4,19456
qf 152,112,48
q0 4
buffer_store_dwordx4 v[12:15],v2,s[32:35],s49 offen offset:3072 sc1
qf 156,120,48
q5 112,4,20480
qf 156,124,52
buffer_load_dwordx4 v[40:43],v2,s[32:35],s48 offen offset:2048 sc1
qf 220,124,60
q5 116,4,21504
qf 220,120,56
q0 4
qf 224,96,56
q5 120,4,22528
qf 224,100,60
qf 160,100,52
s_add_u32 s49,s90,45056
q5 124,4,23552
qd 24,216
qf 160,96,48
q0 4
qd 25,217
qd 26,218
qf 164,104,48
q5 96,4,24576
qd 27,219
qf 164,108,52
qd 28,248
qd 29,249
qf 228,108,60
q5 100,4,25600
qd 30,250
qf 228,104,56
q0 4
qd 31,251
qb 8,24,25
qf 232,112,56
q5 104,4,26624
qb 9,26,27
qf 232,116,60
buffer_load_dwordx4 v[44:47],v2,s[32:35],s48 offen offset:3072 sc1
qb 10,28,29
qf 168,116,52
q5 108,4,27648
qb 11,30,31
qf 168,112,48
q0 4
buffer_store_dwordx4 v[8:11],v2,s[32:35],s49 offen offset:1024 sc1
qf 172,120,48
q5 112,4,28672
qf 172,124,52
qf 236,124,60
q5 116,4,29696
qf 236,120,56
q0 4
qf 240,96,56
q5 120,4,30720
qf 240,100,60
qf 176,100,52
q5 124,4,31744
qd 24,220
qf 176,96,48
s_waitcnt vmcnt(18) lgkmcnt(4)
s_barrier
v_add_u32 v4,s75,v2
qf 180,104,48
q5 96,4,0
qd 25,221
qf 180,108,52
qd 26,222
qd 27,223
qf 244,108,60
q5 100,4,1024
qd 28,252
qf 244,104,56
q0 4
qd 29,253
qd 30,254
qf 248,112,56
q5 104,4,2048
qd 31,255
qf 248,116,60
qb 12,24,25
qb 13,26,27
qf 184,116,52
q5 108,4,3072
qb 14,28,29
qf 184,112,48
q0 4
qb 15,30,31
qf 188,120,48
s_add_u32 s50,s80,8192
q5 112,4,4096
qf 188,124,52
buffer_store_dwordx4 v[12:15],v2,s[32:35],s49 offen offset:3072 sc1
qf 252,124,60
q5 116,4,5120
qf 252,120,56
s_waitcnt vmcnt(18) lgkmcnt(4)
qf 192,96,72
s_add_u32 s72,s72,1
q5 120,4,6144
v_readlane_b32 s73,v5,s72
qf 192,100,76
v_readlane_b32 s74,v6,s72
v_readlane_b32 s75,v7,s72
qf 128,100,68
q5 124,4,7168
qf 128,96,64
q0 4
qf 132,104,64
q5 96,4,8192
qf 132,108,68
s_add_u32 s48,s78,8192
buffer_load_dwordx4 v[48:51],v2,s[32:35],s48 offen offset:0 sc1
qf 196,108,76
q5 100,4,9216
qf 196,104,72
q0 4
qf 200,112,72
q5 104,4,10240
qf 200,116,76
qf 136,116,68
q5 108,4,11264
qf 136,112,64
q0 4
qf 140,120,64
q5 112,4,12288
qb 192,192,193
qf 140,124,68
qb 193,194,195
qb 194,196,197
qf 204,124,76
q5 116,4,13312
qb 195,198,199
qf 204,120,72
q0 4
qj 32,192
qj 33,193
qf 208,96,72
q5 120,4,14336
qj 34,194
qf 208,100,76
buffer_load_dwordx4 v[52:55],v2,s[32:35],s48 offen offset:1024 sc1
s_add_u32 s48,s79,8192
qf 144,100,68
q5 124,4,15360
qj 35,195
qf 144,96,64
q0 4
buffer_store_dwordx4 a[32:35],v2,s[32:35],s50 offen offset:0 sc1
qb 128,128,129
qf 148,104,64
q5 96,4,16384
qb 129,130,131
qf 148,108,68
qb 130,132,133
qb 131,134,135
qf 212,108,76
q5 100,4,17408
qj 0,128
qf 212,104,72
q0 4
qj 1,129
qj 2,130
qf 216,112,72
q5 104,4,18432
qj 3,131
qf 216,116,76
buffer_store_dwordx4 a[0:3],v2,s[32:35],s80 offen offset:0 sc1
qb 200,200,201
qf 152,116,68
q5 108,4,19456
qb 201,202,203
qf 152,112,64
q0 4
qb 202,204,205
qb 203,206,207
qf 156,120,64
q5 112,4,20480
qj 36,200
qf 156,124,68
buffer_load_dwordx4 v[56:59],v2,s[32:35],s48 offen offset:0 sc1
qj 37,201
qf 220,124,76
q5 116,4,21504
qj 38,202
qf 220,120,72
q0 4
qj 39,203
qb 136,136,137
qf 224,96,72
q5 120,4,22528
qb 137,138,139
qf 224,100,76
buffer_store_dwordx4 a[36:39],v2,s[32:35],s50 offen offset:1024 sc1
qb 138,140,141
qf 160,100,68
q5 124,4,23552
qb 139,142,143
qf 160,96,64
q0 4
qj 4,136
qj 5,137
qf 164,104,64
q5 96,4,24576
qj 6,138
qf 164,108,68
qj 7,139
qb 208,208,209
qf 228,108,76
q5 100,4,25600
qb 209,210,211
qf 228,104,72
q0 4
buffer_store_dwordx4 a[4:7],v2,s[32:35],s80 offen offset:1024 sc1
qb 210,212,213
qf 232,112,72
q5 104,4,26624
qb 211,214,215
qf 232,116,76
buffer_load_dwordx4 v[60:63],v2,s[32:35],s48 offen offset:1024 sc1
qj 40,208
qf 168,116,68
q5 108,4,27648
qj 41,209
qf 168,112,64
q0 4
qj 42,210
qj 43,211
qf 172,120,64
q5 112,4,28672
qb 144,144,145
qf 172,124,68
buffer_store_dwordx4 a[40:43],v2,s[32:35],s50 offen offset:2048 sc1
qb 145,146,147
qf 236,124,76
q5 116,4,29696
qb 146,148,149
qf 236,120,72
q0 4
qb 147,150,151
qj 8,144
qf 240,96,72
q5 120,4,30720
qj 9,145
qf 240,100,76
qj 10,146
qj 11,147
qf 176,100,68
q5 124,4,31744
qb 216,216,217
qf 176,96,64
q0 4
s_barrier
v_add_u32 v4,s75,v2
qf 180,104,64
qg 128,4,0
qb 217,218,219
qf 180,108,68
qg 132,4,1024
qb 218,220,221
qf 244,108,76
qg 136,4,2048
qb 219,222,223
qf 244,104,72
q0 5
qg 140,4,3072
qj 44,216
qf 248,112,72
qg 144,4,4096
qj 45,217
qf 248,116,76
buffer_store_dwordx4 a[8:11],v2,s[32:35],s80 offen offset:2048 sc1
qj 46,218
qf 184,116,68
qg 148,4,5120
qj 47,219
qf 184,112,64
q0 6
qg 152,4,6144
qb 152,152,153
qf 188,120,64
qg 156,4,7168
qb 153,154,155
qf 188,124,68
qg 160,4,8192
qb 154,156,157
qf 252,124,76
qg 164,4,9216
qb 155,158,159
qf 252,120,72
s_waitcnt vmcnt(20) lgkmcnt(8)
buffer_store_dwordx4 a[44:47],v2,s[32:35],s50 offen offset:3072 sc1
qj 12,152
qh 192,128,88
s_add_u32 s72,s72,1
qg 168,4,10240
v_readlane_b32 s73,v5,s72
qi 192,132,92
s_add_u32 s50,s50,4096
v_readlane_b32 s74,v6,s72
v_readlane_b32 s75,v7,s72
qh 128,132,84
qg 172,4,11264
qj 13,153
qi 128,128,80
q0 8
qj 14,154
qj 15,155
qh 132,136,80
)"
R"(qg 176,4,12288
qb 224,224,225
qi 132,140,84
s_add_u32 s48,s78,4096
buffer_load_dwordx4 v[64:67],v2,s[32:35],s48 offen offset:2048 sc1
qb 225,226,227
qh 196,140,92
qg 180,4,13312
qb 226,228,229
qi 196,136,88
q0 8
buffer_store_dwordx4 a[12:15],v2,s[32:35],s80 offen offset:3072 sc1
qb 227,230,231
qh 200,144,88
s_add_u32 s80,s80,4096
qg 184,4,14336
qj 48,224
qi 200,148,92
qj 49,225
qj 50,226
qh 136,148,84
qg 188,4,15360
qj 51,227
qi 136,144,80
q0 8
buffer_store_dwordx4 a[48:51],v2,s[32:35],s50 offen offset:0 sc1
qb 160,160,161
qh 140,152,80
qg 128,4,16384
qb 161,162,163
qi 140,156,84
qb 162,164,165
qb 163,166,167
qh 204,156,92
qg 132,4,17408
qj 16,160
qi 204,152,88
q0 8
qj 17,161
qj 18,162
qh 208,160,88
qg 136,4,18432
qj 19,163
qi 208,164,92
buffer_load_dwordx4 v[68:71],v2,s[32:35],s48 offen offset:3072 sc1
s_add_u32 s48,s79,4096
qh 144,164,84
qg 140,4,19456
qb 232,232,233
qi 144,160,80
q0 8
buffer_store_dwordx4 a[16:19],v2,s[32:35],s80 offen offset:0 sc1
qb 233,234,235
qh 148,168,80
qg 144,4,20480
qb 234,236,237
qi 148,172,84
qb 235,238,239
qj 52,232
qh 212,172,92
qg 148,4,21504
qj 53,233
qi 212,168,88
q0 8
qj 54,234
qj 55,235
qh 216,176,88
qg 152,4,22528
qb 168,168,169
qi 216,180,92
buffer_store_dwordx4 a[52:55],v2,s[32:35],s50 offen offset:1024 sc1
qb 169,170,171
qh 152,180,84
qg 156,4,23552
qb 170,172,173
qi 152,176,80
q0 8
qb 171,174,175
qj 20,168
qh 156,184,80
qg 160,4,24576
qj 21,169
qi 156,188,84
buffer_load_dwordx4 v[72:75],v2,s[32:35],s48 offen offset:2048 sc1
qj 22,170
qh 220,188,92
qg 164,4,25600
qj 23,171
qi 220,184,88
q0 8
buffer_store_dwordx4 a[20:23],v2,s[32:35],s80 offen offset:1024 sc1
qb 240,240,241
qh 224,128,88
qg 168,4,26624
qb 241,242,243
qi 224,132,92
qb 242,244,245
qb 243,246,247
qh 160,132,84
qg 172,4,27648
qj 56,240
qi 160,128,80
q0 8
qj 57,241
qj 58,242
qh 164,136,80
qg 176,4,28672
qj 59,243
qi 164,140,84
buffer_store_dwordx4 a[56:59],v2,s[32:35],s50 offen offset:2048 sc1
qb 176,176,177
qh 228,140,92
qg 180,4,29696
qb 177,178,179
qi 228,136,88
q0 8
qb 178,180,181
qb 179,182,183
qh 232,144,88
qg 184,4,30720
qj 24,176
qi 232,148,92
buffer_load_dwordx4 v[76:79],v2,s[32:35],s48 offen offset:3072 sc1
qj 25,177
qh 168,148,84
qg 188,4,31744
qj 26,178
qi 168,144,80
s_waitcnt vmcnt(21) lgkmcnt(8)
s_barrier
v_add_u32 v4,s75,v2
qh 172,152,80
qg 128,4,0
qj 27,179
qi 172,156,84
buffer_store_dwordx4 a[24:27],v2,s[32:35],s80 offen offset:2048 sc1
qb 248,248,249
qh 236,156,92
qg 132,4,1024
qb 249,250,251
qi 236,152,88
q0 8
qb 250,252,253
qb 251,254,255
qh 240,160,88
qg 136,4,2048
qj 60,248
qi 240,164,92
qj 61,249
qj 62,250
qh 176,164,84
qg 140,4,3072
qj 63,251
qi 176,160,80
q0 8
buffer_store_dwordx4 a[60:63],v2,s[32:35],s50 offen offset:3072 sc1
qb 184,184,185
qh 180,168,80
qg 144,4,4096
qb 185,186,187
qi 180,172,84
qb 186,188,189
qb 187,190,191
qj 28,184
qj 29,185
qj 30,186
qj 31,187
qh 244,172,92
qg 148,4,5120
v_mov_b32 v16,s81
qi 244,168,88
q0 8
buffer_store_dwordx4 a[28:31],v2,s[32:35],s80 offen offset:3072 sc1
v_mov_b32 v17,0
qh 248,176,88
qg 152,4,6144
qi 248,180,92
buffer_store_dword v16,v17,s[32:35],s91 offen sc1
qh 184,180,84
qg 156,4,7168
qi 184,176,80
q0 8
qh 188,184,80
qg 160,4,8192
qi 188,188,84
qh 252,188,92
qg 164,4,9216
qi 252,184,88
s_waitcnt vmcnt(27) lgkmcnt(8)
qi 192,128,40
s_add_u32 s72,s72,1
qg 168,4,10240
v_readlane_b32 s73,v5,s72
qi 192,132,44
v_readlane_b32 s74,v6,s72
v_readlane_b32 s75,v7,s72
qi 128,132,36
qg 172,4,11264
qi 128,128,32
q0 8
qi 132,136,32
qg 176,4,12288
qi 132,140,36
s_add_u32 s48,s78,4096
buffer_load_dwordx4 v[80:83],v2,s[32:35],s48 offen offset:0 sc1
qi 196,140,44
qg 180,4,13312
qi 196,136,40
q0 8
qi 200,144,40
qg 184,4,14336
qi 200,148,44
qi 136,148,36
qg 188,4,15360
qi 136,144,32
q0 8
qi 140,152,32
qg 128,4,16384
qi 140,156,36
qi 204,156,44
qg 132,4,17408
qi 204,152,40
q0 8
qi 208,160,40
qg 136,4,18432
qi 208,164,44
LHH8_3kf:
s_cbranch_execz LHR8kf
buffer_load_dwordx4 v[84:87],v2,s[32:35],s48 offen offset:1024 sc1
s_add_u32 s48,s79,4096
qi 144,164,36
qg 140,4,19456
qi 144,160,32
q0 8
qi 148,168,32
qg 144,4,20480
qi 148,172,36
qi 212,172,44
qg 148,4,21504
qi 212,168,40
q0 8
qi 216,176,40
qg 152,4,22528
qi 216,180,44
qi 152,180,36
qg 156,4,23552
qi 152,176,32
q0 8
qi 156,184,32
qg 160,4,24576
qi 156,188,36
buffer_load_dwordx4 v[88:91],v2,s[32:35],s48 offen offset:0 sc1
qi 220,188,44
qg 164,4,25600
qi 220,184,40
q0 8
qi 224,128,40
qg 168,4,26624
qi 224,132,44
qi 160,132,36
qg 172,4,27648
qi 160,128,32
q0 8
qi 164,136,32
qg 176,4,28672
qi 164,140,36
qi 228,140,44
qg 180,4,29696
qi 228,136,40
q0 8
qi 232,144,40
qg 184,4,30720
qi 232,148,44
buffer_load_dwordx4 v[92:95],v2,s[32:35],s48 offen offset:1024 sc1
qi 168,148,36
qg 188,4,31744
qi 168,144,32
q0 8
s_barrier
v_add_u32 v4,s75,v2
qi 172,152,32
qg 128,4,0
qi 172,156,36
qi 236,156,44
qg 132,4,1024
qi 236,152,40
q0 8
qi 240,160,40
qg 136,4,2048
qi 240,164,44
qi 176,164,36
qg 140,4,3072
qi 176,160,32
q0 8
qi 180,168,32
qg 144,4,4096
qi 180,172,36
qi 244,172,44
qg 148,4,5120
qi 244,168,40
q0 8
qi 248,176,40
qg 152,4,6144
qi 248,180,44
qi 184,180,36
qg 156,4,7168
q3 5
qi 184,176,32
q0 8
v_mov_b32 v20,s81
v_mov_b32 v21,0
qi 188,184,32
qg 160,4,8192
qi 188,188,36
buffer_store_dword v20,v21,s[32:35],s93 offen sc1
qi 252,188,44
qg 164,4,9216
qi 252,184,40
s_waitcnt vmcnt(22) lgkmcnt(8)
qi 192,128,56
s_mov_b32 m0,s74
qg 168,4,10240
qi 192,132,60
qc 2,0
qi 128,132,52
qg 172,4,11264
qi 128,128,48
q0 8
qi 132,136,48
s_mov_b32 s44,s6
qg 176,4,12288
s_mov_b32 s45,s7
qi 132,140,52
s_add_u32 s48,s78,0
qc 2,1024
s_mov_b32 s46,LENDkf-LCODEkf
qi 196,140,60
s_mov_b32 s47,0x20000
qg 180,4,13312
s_and_b32 s54,s68,1
qi 196,136,56
q0 8
buffer_load_dwordx4 v[32:35],v2,s[32:35],s48 offen offset:2048 sc1
s_lshl1_add_u32 s54,s70,s54
qi 200,144,56
s_lshl2_add_u32 s54,s54,s31
qg 184,4,14336
s_lshl_b32 s54,s54,12
qi 200,148,60
qc 2,2048
v_and_b32 v23,31,v1
qi 136,148,52
qg 188,4,15360
v_lshlrev_b32 v23,7,v23
qi 136,144,48
q0 8
buffer_load_dword v23,v23,s[44:47],s54 offen
qi 140,152,48
qg 128,4,16384
qi 140,156,52
qi 204,156,60
qg 132,4,17408
qi 204,152,56
q0 8
qc 2,3072
s_add_u32 m0,s74,4096
qi 208,160,56
qg 136,4,18432
qi 208,164,60
qi 144,164,52
qg 140,4,19456
qi 144,160,48
q0 8
qc 3,0
qi 148,168,48
qg 144,4,20480
qi 148,172,52
buffer_load_dwordx4 v[36:39],v2,s[32:35],s48 offen offset:3072 sc1
s_add_u32 s48,s79,0
qi 212,172,60
qg 148,4,21504
qi 212,168,56
q0 8
qc 3,1024
qi 216,176,56
qg 152,4,22528
qi 216,180,60
qi 152,180,52
qg 156,4,23552
qi 152,176,48
q0 8
qc 3,2048
qi 156,184,48
qg 160,4,24576
qi 156,188,52
qi 220,188,60
qg 164,4,25600
qi 220,184,56
q0 8
qc 3,3072
s_add_u32 s72,s72,1
qi 224,128,56
qg 168,4,26624
v_readlane_b32 s73,v5,s72
qi 224,132,60
v_readlane_b32 s74,v6,s72
v_readlane_b32 s75,v7,s72
qi 160,132,52
qg 172,4,27648
qi 160,128,48
q0 8
buffer_load_dwordx4 v[40:43],v2,s[32:35],s48 offen offset:2048 sc1
qi 164,136,48
qg 176,4,28672
qi 164,140,52
qi 228,140,60
qg 180,4,29696
qi 228,136,56
q0 8
qi 232,144,56
qg 184,4,30720
qi 232,148,60
qi 168,148,52
qg 188,4,31744
qi 168,144,48
q0 8
s_barrier
v_add_u32 v4,s75,v2
qi 172,152,48
qg 128,4,0
qi 172,156,52
qi 236,156,60
qg 132,4,1024
qi 236,152,56
q0 8
buffer_load_dwordx4 v[44:47],v2,s[32:35],s48 offen offset:3072 sc1
qi 240,160,56
qg 136,4,2048
qi 240,164,60
qi 176,164,52
qg 140,4,3072
qi 176,160,48
q0 8
qi 180,168,48
qg 144,4,4096
qi 180,172,52
qi 244,172,60
qg 148,4,5120
qi 244,168,56
q0 8
qi 248,176,56
qg 152,4,6144
qi 248,180,60
qi 184,180,52
qg 156,4,7168
qi 184,176,48
q0 8
qi 188,184,48
qg 160,4,8192
qi 188,188,52
qi 252,188,60
qg 164,4,9216
qi 252,184,56
s_waitcnt vmcnt(22) lgkmcnt(8)
qi 192,128,72
s_mov_b32 m0,s74
qg 168,4,10240
qi 192,132,76
qc 2,0
qi 128,132,68
qg 172,4,11264
qi 128,128,64
q0 8
qi 132,136,64
qg 176,4,12288
qi 132,140,68
s_add_u32 s48,s78,0
qc 2,1024
qi 196,140,76
qg 180,4,13312
qi 196,136,72
q0 8
buffer_load_dwordx4 v[48:51],v2,s[32:35],s48 offen offset:0 sc1
qi 200,144,72
qg 184,4,14336
v_and_b32 v19,15,v1
qi 200,148,76
qc 2,2048
v_lshlrev_b32 v19,7,v19
qi 136,148,68
qg 188,4,15360
qi 136,144,64
q0 8
buffer_load_dword v18,v19,s[32:35],s92 offen sc1
qi 140,152,64
qg 128,4,16384
qi 140,156,68
qi 204,156,76
qg 132,4,17408
qi 204,152,72
q0 8
qc 2,3072
s_add_u32 m0,s74,4096
qi 208,160,72
qg 136,4,18432
qi 208,164,76
qi 144,164,68
qg 140,4,19456
qi 144,160,64
q0 8
qc 3,0
qi 148,168,64
qg 144,4,20480
qi 148,172,68
buffer_load_dwordx4 v[52:55],v2,s[32:35],s48 offen offset:1024 sc1
s_add_u32 s48,s79,0
qi 212,172,76
qg 148,4,21504
qi 212,168,72
q0 8
qc 3,1024
qi 216,176,72
qg 152,4,22528
qi 216,180,76
qi 152,180,68
qg 156,4,23552
qi 152,176,64
q0 8
qc 3,2048
qi 156,184,64
qg 160,4,24576
qi 156,188,68
qi 220,188,76
qg 164,4,25600
qi 220,184,72
q0 8
qc 3,3072
s_add_u32 s72,s72,1
qi 224,128,72
qg 168,4,26624
v_readlane_b32 s73,v5,s72
qi 224,132,76
v_readlane_b32 s74,v6,s72
v_readlane_b32 s75,v7,s72
qi 160,132,68
qg 172,4,27648
qi 160,128,64
q0 8
buffer_load_dwordx4 v[56:59],v2,s[32:35],s48 offen offset:0 sc1
qi 164,136,64
qg 176,4,28672
qi 164,140,68
qi 228,140,76
qg 180,4,29696
qi 228,136,72
q0 8
qi 232,144,72
qg 184,4,30720
qi 232,148,76
qi 168,148,68
qg 188,4,31744
qi 168,144,64
q0 8
s_barrier
v_add_u32 v4,s75,v2
qi 172,152,64
qg 128,4,0
qi 172,156,68
qi 236,156,76
qg 132,4,1024
qi 236,152,72
q0 8
buffer_load_dwordx4 v[60:63],v2,s[32:35],s48 offen offset:1024 sc1
qi 240,160,72
qg 136,4,2048
qi 240,164,76
qi 176,164,68
qg 140,4,3072
qi 176,160,64
q0 8
qi 180,168,64
qg 144,4,4096
qi 180,172,68
qi 244,172,76
qg 148,4,5120
qi 244,168,72
q0 8
qi 248,176,72
qg 152,4,6144
qi 248,180,76
qi 184,180,68
qg 156,4,7168
qi 184,176,64
q0 8
qi 188,184,64
qg 160,4,8192
qi 188,188,68
qi 252,188,76
qg 164,4,9216
qi 252,184,72
s_waitcnt vmcnt(8) lgkmcnt(8)
v_cmp_ne_u32 vcc,s81,v18
s_nop 0
s_cbranch_vccnz Lslow39kf_nb
Lback40kf_nb:
qi 192,128,88
s_mov_b32 m0,s74
qg 168,4,10240
qi 192,132,92
qc 2,0
qi 128,132,84
qg 172,4,11264
qi 128,128,80
q0 8
qi 132,136,80
qg 176,4,12288
qi 132,140,84
qc 2,1024
qi 196,140,92
qg 180,4,13312
qi 196,136,88
q0 8
qi 200,144,88
qg 184,4,14336
v_mov_b32 v126,0xff800000
qi 200,148,92
qc 2,2048
v_mov_b32 v127,0x49800000
qi 136,148,84
s_sub_u32 s77,s84,1
qg 188,4,15360
qi 136,144,80
q0 8
s_mul_i32 s80,s83,17408
qi 140,152,80
s_add_u32 s80,s80,55713792
qg 128,4,16384
qi 140,156,84
s_lshl_b32 s93,s83,7
qi 204,156,92
s_add_u32 s93,s93,262144
qg 132,4,17408
qi 204,152,88
q0 8
qc 2,3072
s_add_u32 m0,s74,4096
qi 208,160,88
qg 136,4,18432
qi 208,164,92
s_cmp_eq_u32 s70,0
s_cselect_b32 s50,0,4096
qi 144,164,84
qg 140,4,19456
v_and_b32 v24,15,v1
qi 144,160,80
q0 8
qc 3,0
v_lshrrev_b32 v25,4,v1
qi 148,168,80
qg 144,4,20480
v_lshlrev_b32 v25,2,v25
qi 148,172,84
v_sub_u32 v24,v24,v25
qi 212,172,92
s_lshl_b32 s48,s31,4
qg 148,4,21504
qi 212,168,88
q0 8
qc 3,1024
s_sub_u32 s49,48,s48
qi 216,176,88
s_add_u32 s48,s48,s50
qg 152,4,22528
qi 216,180,92
v_add_u32 v14,s48,v24
qi 152,180,84
qg 156,4,23552
v_add_u32 v15,s49,v24
qi 152,176,80
q0 8
qc 3,2048
s_mov_b32 s82,s70
qi 156,184,80
s_lshr_b32 s52,s68,2
qg 160,4,24576
qi 156,188,84
s_lshl_b32 s53,s82,2
qi 220,188,92
s_add_u32 s53,s53,s52
qg 164,4,25600
qi 220,184,88
q0 8
qc 3,3072
s_add_u32 s72,s72,1
qi 224,128,88
s_lshl_b32 s94,s53,10
qg 168,4,26624
v_readlane_b32 s73,v5,s72
qi 224,132,92
s_add_u32 s94,s94,327680
v_readlane_b32 s74,v6,s72
v_readlane_b32 s75,v7,s72
qi 160,132,84
s_lshl_b32 s95,s53,11
qg 172,4,27648
qi 160,128,80
q0 8
s_add_u32 s95,s95,393216
qi 164,136,80
s_lshl_b32 s54,s31,4
qg 176,4,28672
qi 164,140,84
s_add_u32 s54,s54,s68
qi 228,140,92
s_lshl_b32 s54,s54,10
qg 180,4,29696
qi 228,136,88
q0 8
s_mul_i32 s48,s82,917504
qi 232,144,88
s_add_u32 s48,s48,s54
qg 184,4,30720
qi 232,148,92
s_add_u32 s96,s48,64626688
qi 168,148,84
s_mul_i32 s48,s82,1835008
qg 188,4,31744
qi 168,144,80
s_waitcnt vmcnt(23) lgkmcnt(8)
s_barrier
v_add_u32 v4,s75,v2
qi 172,152,80
s_add_u32 s48,s48,s54
qg 128,4,0
qi 172,156,84
s_add_u32 s98,s48,79306752
qi 236,156,92
qg 132,4,1024
qi 236,152,88
q0 8
s_cmp_eq_u32 s31,3
s_cselect_b32 s88,0,65536
qi 240,160,88
s_add_u32 s97,s96,s88
qg 136,4,2048
qi 240,164,92
s_sub_u32 s49,7,s52
qi 176,164,84
qg 140,4,3072
qi 176,160,80
q0 8
s_bitcmp1_b32 s68,1
s_cselect_b32 s49,s49,s52
qi 180,168,80
s_lshl_b32 s50,s82,3
qg 144,4,4096
qi 180,172,84
s_add_u32 s49,s49,s50
qi 244,172,92
s_mul_i32 s89,s49,14336
qg 148,4,5120
qi 244,168,88
q0 8
s_and_b32 s50,s68,1
qi 248,176,88
s_lshl_b32 s50,s50,6
qg 152,4,6144
qi 248,180,92
s_lshl_b32 s51,s31,7
qi 184,180,84
s_add_u32 s90,s50,s51
qg 156,4,7168
qi 184,176,80
q0 8
qi 188,184,80
qg 160,4,8192
qi 188,188,84
qi 252,188,92
qg 164,4,9216
qi 252,184,88
s_waitcnt vmcnt(21) lgkmcnt(8)
qi 192,128,40
s_mov_b32 m0,s74
qg 168,4,10240
qi 192,132,44
qc 2,0
qi 128,132,36
qg 172,4,11264
qi 128,128,32
q0 8
qi 132,136,32
qg 176,4,12288
qi 132,140,36
qc 2,1024
qi 196,140,44
qg 180,4,13312
qi 196,136,40
q0 8
qi 200,144,40
qg 184,4,14336
qi 200,148,44
qc 2,2048
qi 136,148,36
qg 188,4,15360
qi 136,144,32
q0 8
qi 140,152,32
LHH9_0kf:
s_cbranch_execz LHR9kf
qg 128,4,16384
qi 140,156,36
qi 204,156,44
qg 132,4,17408
qi 204,152,40
q0 8
qc 2,3072
s_add_u32 m0,s74,4096
qi 208,160,40
qg 136,4,18432
qi 208,164,44
qi 144,164,36
qg 140,4,19456
qi 144,160,32
q0 8
qc 3,0
qi 148,168,32
qg 144,4,20480
qi 148,172,36
qi 212,172,44
qg 148,4,21504
qi 212,168,40
q0 8
qc 3,1024
qi 216,176,40
qg 152,4,22528
qi 216,180,44
qi 152,180,36
qg 156,4,23552
qi 152,176,32
q0 8
qc 3,2048
qi 156,184,32
qg 160,4,24576
qi 156,188,36
qi 220,188,44
qg 164,4,25600
qi 220,184,40
q0 8
qc 3,3072
s_add_u32 s72,s72,1
qi 224,128,40
qg 168,4,26624
v_readlane_b32 s73,v5,s72
qi 224,132,44
v_readlane_b32 s74,v6,s72
v_readlane_b32 s75,v7,s72
qi 160,132,36
qg 172,4,27648
qi 160,128,32
q0 8
qi 164,136,32
qg 176,4,28672
qi 164,140,36
qi 228,140,44
qg 180,4,29696
qi 228,136,40
q0 8
qi 232,144,40
qg 184,4,30720
qi 232,148,44
qi 168,148,36
qg 188,4,31744
qi 168,144,32
s_waitcnt vmcnt(18) lgkmcnt(8)
s_barrier
v_add_u32 v4,s75,v2
qi 172,152,32
qg 128,4,0
qi 172,156,36
qi 236,156,44
qg 132,4,1024
qi 236,152,40
q0 8
qi 240,160,40
qg 136,4,2048
qi 240,164,44
qi 176,164,36
qg 140,4,3072
qi 176,160,32
q0 8
qi 180,168,32
qg 144,4,4096
qi 180,172,36
qi 244,172,44
qg 148,4,5120
qi 244,168,40
q0 8
qi 248,176,40
qg 152,4,6144
qi 248,180,44
qi 184,180,36
qg 156,4,7168
qi 184,176,32
q0 8
qi 188,184,32
qg 160,4,8192
qi 188,188,36
qi 252,188,44
qg 164,4,9216
qi 252,184,40
s_waitcnt vmcnt(16) lgkmcnt(8)
qi 192,128,56
s_mov_b32 m0,s74
qg 168,4,10240
qi 192,132,60
qc 2,0
qi 128,132,52
qg 172,4,11264
qi 128,128,48
q0 8
qi 132,136,48
qg 176,4,12288
qi 132,140,52
qc 2,1024
qi 196,140,60
qg 180,4,13312
qi 196,136,56
q0 8
qi 200,144,56
qg 184,4,14336
qi 200,148,60
qc 2,2048
qi 136,148,52
qg 188,4,15360
qi 136,144,48
q0 8
qi 140,152,48
qg 128,4,16384
qb 192,192,193
qi 140,156,52
qb 193,194,195
qb 194,196,197
qi 204,156,60
qg 132,4,17408
qb 195,198,199
qi 204,152,56
q0 8
qc 2,3072
s_add_u32 m0,s74,4096
qi 208,160,56
qg 136,4,18432
qj 96,192
qi 208,164,60
qj 97,193
qj 98,194
qi 144,164,52
qg 140,4,19456
qj 99,195
qi 144,160,48
q0 8
qc 3,0
qb 128,128,129
qi 148,168,48
qg 144,4,20480
qb 129,130,131
qi 148,172,52
qb 130,132,133
qb 131,134,135
qi 212,172,60
qg 148,4,21504
qj 64,128
qi 212,168,56
q0 8
qc 3,1024
qj 65,129
qi 216,176,56
qg 152,4,22528
qj 66,130
qi 216,180,60
qj 67,131
qb 200,200,201
qi 152,180,52
qg 156,4,23552
qb 201,202,203
qi 152,176,48
q0 8
qc 3,2048
qb 202,204,205
qi 156,184,48
qg 160,4,24576
qb 203,206,207
qi 156,188,52
qj 100,200
qj 101,201
qi 220,188,60
qg 164,4,25600
qj 102,202
qi 220,184,56
q0 8
qc 3,3072
s_add_u32 s72,s72,1
qi 224,128,56
qg 168,4,26624
v_readlane_b32 s73,v5,s72
qi 224,132,60
v_readlane_b32 s74,v6,s72
v_readlane_b32 s75,v7,s72
qi 160,132,52
qg 172,4,27648
qj 103,203
qi 160,128,48
q0 8
qb 136,136,137
qb 137,138,139
qi 164,136,48
qg 176,4,28672
qb 138,140,141
qi 164,140,52
qb 139,142,143
qj 68,136
qi 228,140,60
qg 180,4,29696
qj 69,137
qi 228,136,56
q0 8
qj 70,138
qj 71,139
qi 232,144,56
qg 184,4,30720
qb 208,208,209
qi 232,148,60
qb 209,210,211
qb 210,212,213
qi 168,148,52
qg 188,4,31744
qb 211,214,215
qi 168,144,48
s_waitcnt vmcnt(16) lgkmcnt(8)
s_barrier
v_add_u32 v4,s75,v2
qi 172,152,48
qg 128,4,0
qj 104,208
qi 172,156,52
qj 105,209
qj 106,210
qi 236,156,60
qg 132,4,1024
qj 107,211
qi 236,152,56
q0 8
qb 144,144,145
qb 145,146,147
qi 240,160,56
qg 136,4,2048
qb 146,148,149
qi 240,164,60
qb 147,150,151
qj 72,144
qi 176,164,52
qg 140,4,3072
qj 73,145
qi 176,160,48
q0 8
qj 74,146
qj 75,147
qi 180,168,48
qg 144,4,4096
qb 216,216,217
qi 180,172,52
qb 217,218,219
qb 218,220,221
qi 244,172,60
qg 148,4,5120
qb 219,222,223
qi 244,168,56
q0 8
qj 108,216
qj 109,217
qi 248,176,56
qg 152,4,6144
qj 110,218
qi 248,180,60
qj 111,219
qb 152,152,153
qi 184,180,52
qg 156,4,7168
qb 153,154,155
qi 184,176,48
q0 8
qb 154,156,157
qb 155,158,159
qi 188,184,48
qg 160,4,8192
qj 76,152
qi 188,188,52
qj 77,153
qj 78,154
qi 252,188,60
qg 164,4,9216
qj 79,155
qi 252,184,56
qb 224,224,225
s_cmp_eq_u32 s3,0
s_cbranch_scc1 Lnoh43kf
q0 8
qk 36,128,32
s_mov_b32 m0,s74
qg 168,4,10240
ql 36,132,36
q0 7
qc 2,0
ql 36,136,40
qg 172,4,11264
ql 36,140,44
q0 6
qb 225,226,227
qb 226,228,229
ql 36,144,48
qg 176,4,12288
qb 227,230,231
ql 36,148,52
q0 5
qc 2,1024
qj 112,224
ql 36,152,56
qg 180,4,13312
qj 113,225
ql 36,156,60
qj 114,226
qj 115,227
qk 32,128,0
qg 184,4,14336
qb 160,160,161
ql 32,132,4
qc 2,2048
qb 161,162,163
ql 32,136,8
qg 188,4,15360
qb 162,164,165
ql 32,140,12
qb 163,166,167
qj 80,160
ql 32,144,16
qg 128,4,16384
qj 81,161
ql 32,148,20
qj 82,162
qj 83,163
ql 32,152,24
qg 132,4,17408
qb 232,232,233
ql 32,156,28
q0 8
qc 2,3072
s_add_u32 m0,s74,4096
qk 44,160,32
qg 136,4,18432
qb 233,234,235
ql 44,164,36
q0 7
qb 234,236,237
qb 235,238,239
ql 44,168,40
qg 140,4,19456
qj 116,232
ql 44,172,44
q0 6
qc 3,0
qj 117,233
ql 44,176,48
qg 144,4,20480
qj 118,234
ql 44,180,52
q0 5
qj 119,235
qb 168,168,169
ql 44,184,56
qg 148,4,21504
qb 169,170,171
ql 44,188,60
qc 3,1024
qb 170,172,173
qk 40,160,0
qg 152,4,22528
qb 171,174,175
ql 40,164,4
qj 84,168
qj 85,169
ql 40,168,8
qg 156,4,23552
qj 86,170
ql 40,172,12
qc 3,2048
qj 87,171
ql 40,176,16
qg 160,4,24576
qb 240,240,241
ql 40,180,20
qb 241,242,243
qb 242,244,245
ql 40,184,24
qg 164,4,25600
qb 243,246,247
ql 40,188,28
q0 8
qc 3,3072
s_add_u32 s72,s72,1
qk 52,128,32
qg 168,4,26624
v_readlane_b32 s73,v5,s72
ql 52,132,36
q0 7
v_readlane_b32 s74,v6,s72
v_readlane_b32 s75,v7,s72
ql 52,136,40
qg 172,4,27648
qj 120,240
ql 52,140,44
q0 6
qj 121,241
qj 122,242
ql 52,144,48
qg 176,4,28672
qj 123,243
ql 52,148,52
q0 5
qb 176,176,177
qb 177,178,179
ql 52,152,56
qg 180,4,29696
qb 178,180,181
ql 52,156,60
qb 179,182,183
qj 88,176
qk 48,128,0
qg 184,4,30720
qj 89,177
ql 48,132,4
qj 90,178
qj 91,179
ql 48,136,8
qg 188,4,31744
qb 248,248,249
ql 48,140,12
q3 16
s_barrier
v_add_u32 v4,s75,v2
ql 48,144,16
qg 128,4,0
qb 249,250,251
ql 48,148,20
qb 250,252,253
qb 251,254,255
ql 48,152,24
qg 132,4,1024
qj 124,248
ql 48,156,28
q0 8
qj 125,249
qj 126,250
qk 60,160,32
qg 136,4,2048
qj 127,251
ql 60,164,36
q0 7
qb 184,184,185
qb 185,186,187
ql 60,168,40
qg 140,4,3072
qb 186,188,189
ql 60,172,44
q0 6
qb 187,190,191
qj 92,184
ql 60,176,48
qg 144,4,4096
qj 93,185
ql 60,180,52
q0 5
qj 94,186
qj 95,187
ql 60,184,56
qg 148,4,5120
v_max3_f32 v121,v36,v37,v38
ql 60,188,60
v_max_f32 v121,v121,v39
v_max3_f32 v120,v32,v33,v34
qk 56,160,0
qg 152,4,6144
v_max_f32 v120,v120,v35
ql 56,164,4
v_max3_f32 v121,v121,v44,v45
v_max3_f32 v121,v121,v46,v47
ql 56,168,8
qg 156,4,7168
v_max3_f32 v120,v120,v40,v41
ql 56,172,12
v_max3_f32 v120,v120,v42,v43
v_max3_f32 v121,v121,v52,v53
ql 56,176,16
qg 160,4,8192
v_max3_f32 v121,v121,v54,v55
ql 56,180,20
v_max3_f32 v120,v120,v48,v49
v_max3_f32 v120,v120,v50,v51
ql 56,184,24
qg 164,4,9216
v_max3_f32 v121,v121,v60,v61
ql 56,188,28
v_max3_f32 v121,v121,v62,v63
v_mov_b64 v[128:129],0
v_mov_b64 v[130:131],0
v_mov_b64 v[132:133],0
v_mov_b64 v[134:135],0
v_mov_b64 v[136:137],0
v_mov_b64 v[138:139],0
v_mov_b64 v[140:141],0
v_mov_b64 v[142:143],0
v_mov_b64 v[144:145],0
v_mov_b64 v[146:147],0
v_mov_b64 v[148:149],0
v_mov_b64 v[150:151],0
v_mov_b64 v[152:153],0
v_mov_b64 v[154:155],0
v_mov_b64 v[156:157],0
v_mov_b64 v[158:159],0
v_mov_b64 v[160:161],0
v_mov_b64 v[162:163],0
v_mov_b64 v[164:165],0
v_mov_b64 v[166:167],0
v_mov_b64 v[168:169],0
v_mov_b64 v[170:171],0
v_mov_b64 v[172:173],0
v_mov_b64 v[174:175],0
v_mov_b64 v[176:177],0
v_mov_b64 v[178:179],0
v_mov_b64 v[180:181],0
v_mov_b64 v[182:183],0
v_mov_b64 v[184:185],0
v_mov_b64 v[186:187],0
v_mov_b64 v[188:189],0
v_mov_b64 v[190:191],0
v_mov_b64 v[192:193],0
v_mov_b64 v[194:195],0
v_mov_b64 v[196:197],0
v_mov_b64 v[198:199],0
v_mov_b64 v[200:201],0
v_mov_b64 v[202:203],0
v_mov_b64 v[204:205],0
v_mov_b64 v[206:207],0
v_mov_b64 v[208:209],0
v_mov_b64 v[210:211],0
v_mov_b64 v[212:213],0
v_mov_b64 v[214:215],0
v_mov_b64 v[216:217],0
v_mov_b64 v[218:219],0
v_mov_b64 v[220:221],0
v_mov_b64 v[222:223],0
v_mov_b64 v[224:225],0
v_mov_b64 v[226:227],0
v_mov_b64 v[228:229],0
v_mov_b64 v[230:231],0
v_mov_b64 v[232:233],0
v_mov_b64 v[234:235],0
v_mov_b64 v[236:237],0
v_mov_b64 v[238:239],0
v_mov_b64 v[240:241],0
v_mov_b64 v[242:243],0
v_mov_b64 v[244:245],0
v_mov_b64 v[246:247],0
v_mov_b64 v[248:249],0
v_mov_b64 v[250:251],0
v_mov_b64 v[252:253],0
v_mov_b64 v[254:255],0
v_max3_f32 v120,v120,v56,v57
v_max3_f32 v120,v120,v58,v59
v_mov_b32 v122,v120
v_mov_b32 v123,v121
s_nop 0
v_permlane32_swap_b32 v120,v122
v_permlane32_swap_b32 v121,v123
s_nop 0
v_max_f32 v120,v120,v122
v_max_f32 v121,v121,v123
v_mov_b32 v122,v120
v_mov_b32 v123,v121
s_nop 0
v_permlane16_swap_b32 v120,v122
v_permlane16_swap_b32 v121,v123
s_nop 0
v_max_f32 v120,v120,v122
v_max_f32 v121,v121,v123
v_mov_b32 v12,v120
LHH9_1kf:
s_cbranch_execz LHR9kf
v_mov_b32 v8,0
v_sub_f32 v112,0,v120
v_sub_f32 v113,0,v120
v_sub_f32 v114,0,v120
v_sub_f32 v115,0,v120
v_mov_b32 v13,v121
v_mov_b32 v9,0
v_sub_f32 v116,0,v121
v_sub_f32 v117,0,v121
v_sub_f32 v118,0,v121
v_sub_f32 v119,0,v121
v_pk_add_f32 v[32:33],v[32:33],v[112:113]
v_pk_add_f32 v[34:35],v[34:35],v[112:113]
v_pk_add_f32 v[36:37],v[36:37],v[116:117]
v_pk_add_f32 v[38:39],v[38:39],v[116:117]
v_pk_add_f32 v[40:41],v[40:41],v[112:113]
v_pk_add_f32 v[42:43],v[42:43],v[112:113]
v_pk_add_f32 v[44:45],v[44:45],v[116:117]
v_pk_add_f32 v[46:47],v[46:47],v[116:117]
v_pk_add_f32 v[48:49],v[48:49],v[112:113]
v_pk_add_f32 v[50:51],v[50:51],v[112:113]
v_pk_add_f32 v[52:53],v[52:53],v[116:117]
v_pk_add_f32 v[54:55],v[54:55],v[116:117]
v_pk_add_f32 v[56:57],v[56:57],v[112:113]
v_pk_add_f32 v[58:59],v[58:59],v[112:113]
v_pk_add_f32 v[60:61],v[60:61],v[116:117]
v_pk_add_f32 v[62:63],v[62:63],v[116:117]
qm 64,32
qm 65,33
qm 66,34
qm 67,35
qm 68,36
qm 69,37
qm 70,38
qm 71,39
qm 72,40
qm 73,41
qm 74,42
qm 75,43
qm 76,44
qm 77,45
qm 78,46
qm 79,47
qm 80,48
qm 81,49
qm 82,50
qm 83,51
qm 84,52
qm 85,53
qm 86,54
qm 87,55
qm 88,56
qm 89,57
qm 90,58
qm 91,59
qm 92,60
qm 93,61
qm 94,62
qm 95,63
v_pk_add_f32 v[120:121],v[64:65],v[66:67]
v_pk_add_f32 v[122:123],v[68:69],v[70:71]
v_pk_add_f32 v[120:121],v[120:121],v[72:73]
v_pk_add_f32 v[122:123],v[122:123],v[76:77]
v_pk_add_f32 v[120:121],v[120:121],v[74:75]
v_pk_add_f32 v[122:123],v[122:123],v[78:79]
v_pk_add_f32 v[120:121],v[120:121],v[80:81]
v_pk_add_f32 v[122:123],v[122:123],v[84:85]
v_pk_add_f32 v[120:121],v[120:121],v[82:83]
v_pk_add_f32 v[122:123],v[122:123],v[86:87]
v_pk_add_f32 v[120:121],v[120:121],v[88:89]
v_pk_add_f32 v[122:123],v[122:123],v[92:93]
v_pk_add_f32 v[120:121],v[120:121],v[90:91]
v_pk_add_f32 v[122:123],v[122:123],v[94:95]
q9 10,120,121
q9 11,122,123
s_cmp_eq_u32 s77,0
s_cbranch_scc1 Lhlx46kf
.p2align 6
Lhl45kf:
q0 8
qn 32,128,0,112
s_mov_b32 m0,s74
qg 168,4,10240
q9 8,8,10
ql 32,132,4
q0 7
qc 2,0
q9 9,9,11
ql 32,136,8
qg 172,4,11264
qb 96,64,65
ql 32,140,12
q0 6
qb 97,66,67
qb 98,72,73
ql 32,144,16
qg 176,4,12288
qb 99,74,75
ql 32,148,20
q0 5
qc 2,1024
qb 100,68,69
ql 32,152,24
qg 180,4,13312
)"
R"(qb 101,70,71
ql 32,156,28
qb 102,76,77
qb 103,78,79
qn 36,128,32,116
qg 184,4,14336
qb 104,80,81
ql 36,132,36
qc 2,2048
qb 105,82,83
ql 36,136,40
qg 188,4,15360
qb 106,88,89
ql 36,140,44
qb 107,90,91
qb 108,84,85
ql 36,144,48
qg 128,4,16384
qb 109,86,87
ql 36,148,52
qb 110,92,93
qb 111,94,95
ql 36,152,56
qg 132,4,17408
ql 36,156,60
q0 8
qc 2,3072
s_add_u32 m0,s74,4096
qn 40,160,0,112
qg 136,4,18432
ql 40,164,4
q0 7
qm 64,32
ql 40,168,8
qg 140,4,19456
ql 40,172,12
q0 6
qc 3,0
ql 40,176,16
qg 144,4,20480
ql 40,180,20
q0 5
qm 65,33
ql 40,184,24
qg 148,4,21504
q9 10,64,65
ql 40,188,28
qc 3,1024
qn 44,160,32,116
qg 152,4,22528
ql 44,164,36
qm 66,34
ql 44,168,40
qg 156,4,23552
q9 10,10,66
ql 44,172,44
qc 3,2048
ql 44,176,48
qg 160,4,24576
ql 44,180,52
qm 67,35
ql 44,184,56
qg 164,4,25600
q9 10,10,67
ql 44,188,60
q0 8
qc 3,3072
s_add_u32 s72,s72,1
qn 48,128,0,112
qg 168,4,26624
v_readlane_b32 s73,v5,s72
ql 48,132,4
q0 7
qm 68,36
ql 48,136,8
qg 172,4,27648
v_readlane_b32 s74,v6,s72
ql 48,140,12
q0 6
qm 69,37
ql 48,144,16
qg 176,4,28672
v_readlane_b32 s75,v7,s72
ql 48,148,20
q0 5
qm 70,38
ql 48,152,24
qg 180,4,29696
q9 11,68,69
ql 48,156,28
qm 71,39
qn 52,128,32,116
qg 184,4,30720
q9 11,11,70
ql 52,132,36
qm 72,40
ql 52,136,40
qg 188,4,31744
q9 11,11,71
ql 52,140,44
q3 16
s_barrier
v_add_u32 v4,s75,v2
ql 52,144,48
qg 128,4,0
q9 10,10,72
ql 52,148,52
qm 73,41
ql 52,152,56
qg 132,4,1024
q9 10,10,73
ql 52,156,60
q0 8
qm 74,42
qn 56,160,0,112
qg 136,4,2048
q9 10,10,74
ql 56,164,4
q0 7
qm 75,43
ql 56,168,8
qg 140,4,3072
q9 10,10,75
ql 56,172,12
q0 6
qm 76,44
ql 56,176,16
qg 144,4,4096
q9 11,11,76
ql 56,180,20
q0 5
qm 77,45
ql 56,184,24
qg 148,4,5120
q9 11,11,77
ql 56,188,28
qn 60,160,32,116
qg 152,4,6144
ql 60,164,36
qm 78,46
ql 60,168,40
qg 156,4,7168
q9 11,11,78
ql 60,172,44
qm 79,47
ql 60,176,48
qg 160,4,8192
q9 11,11,79
ql 60,180,52
ql 60,184,56
qg 164,4,9216
ql 60,188,60
q0 8
qm 80,48
qi 128,128,96
s_mov_b32 m0,s74
qg 168,4,10240
q9 10,10,80
qi 128,132,104
qc 2,0
qi 192,132,108
qg 172,4,11264
qi 192,128,100
q0 8
qm 81,49
qi 196,136,100
qg 176,4,12288
q9 10,10,81
qi 196,140,108
qc 2,1024
qi 132,140,104
qg 180,4,13312
qi 132,136,96
q0 8
qm 82,50
qi 136,144,96
qg 184,4,14336
q9 10,10,82
qi 136,148,104
qc 2,2048
qi 200,148,108
qg 188,4,15360
qi 200,144,100
q0 8
qm 83,51
qi 204,152,100
qg 128,4,16384
q9 10,10,83
qi 204,156,108
qm 84,52
qi 140,156,104
qg 132,4,17408
q9 11,11,84
qi 140,152,96
q0 8
qc 2,3072
s_add_u32 m0,s74,4096
qi 144,160,96
qg 136,4,18432
qi 144,164,104
qm 85,53
qi 208,164,108
qg 140,4,19456
q9 11,11,85
qi 208,160,100
q0 8
qc 3,0
qi 212,168,100
qg 144,4,20480
qi 212,172,108
qm 86,54
qi 148,172,104
qg 148,4,21504
q9 11,11,86
qi 148,168,96
q0 8
qc 3,1024
qi 152,176,96
qg 152,4,22528
qi 152,180,104
qm 87,55
qi 216,180,108
qg 156,4,23552
q9 11,11,87
qi 216,176,100
q0 8
qc 3,2048
qi 220,184,100
qg 160,4,24576
qi 220,188,108
qm 88,56
qi 156,188,104
qg 164,4,25600
q9 10,10,88
qi 156,184,96
q0 8
qc 3,3072
s_add_u32 s72,s72,1
qi 160,128,96
qg 168,4,26624
v_readlane_b32 s73,v5,s72
qi 160,132,104
qm 89,57
qi 224,132,108
qg 172,4,27648
v_readlane_b32 s74,v6,s72
qi 224,128,100
q0 8
qm 90,58
qi 228,136,100
qg 176,4,28672
v_readlane_b32 s75,v7,s72
qi 228,140,108
qm 91,59
qi 164,140,104
qg 180,4,29696
q9 10,10,89
qi 164,136,96
q0 8
qm 92,60
qi 168,144,96
qg 184,4,30720
q9 10,10,90
qi 168,148,104
qm 93,61
qi 232,148,108
qg 188,4,31744
q9 10,10,91
qi 232,144,100
s_waitcnt vmcnt(16) lgkmcnt(8)
s_barrier
v_add_u32 v4,s75,v2
qi 236,152,100
qg 128,4,0
q9 11,11,92
qi 236,156,108
qm 94,62
qi 172,156,104
qg 132,4,1024
q9 11,11,93
qi 172,152,96
q0 8
qm 95,63
qi 176,160,96
qg 136,4,2048
q9 11,11,94
qi 176,164,104
q9 11,11,95
qi 240,164,108
qg 140,4,3072
q9 120,10,11
qi 240,160,100
q0 8
v_cmp_nle_f32 vcc,v120,v127
qi 244,168,100
qg 144,4,4096
qi 244,172,108
qi 180,172,104
qg 148,4,5120
qi 180,168,96
q0 8
qi 184,176,96
qg 152,4,6144
qi 184,180,104
qi 248,180,108
qg 156,4,7168
qi 248,176,100
q0 8
qi 252,184,100
qg 160,4,8192
qi 252,188,108
qi 188,188,104
qg 164,4,9216
s_sub_u32 s77,s77,1
s_cmp_lg_u32 s77,0
qi 188,184,96
s_cbranch_vccnz Lit47kf_rs_nb
Lit47kf_ok_nb:
s_cbranch_scc1 Lhl45kf
Lhlx46kf:
q9 8,8,10
q9 9,9,11
qb 96,64,65
qb 97,66,67
qb 98,72,73
qb 99,74,75
qb 100,68,69
qb 101,70,71
qb 102,76,77
qb 103,78,79
qb 104,80,81
qb 105,82,83
qb 106,88,89
qb 107,90,91
qb 108,84,85
qb 109,86,87
qb 110,92,93
qb 111,94,95
q0 8
qi 128,128,96
s_mov_b32 m0,s74
qg 168,4,10240
v_mov_b32 v122,v8
qi 128,132,104
qc 2,0
v_mov_b32 v123,v9
qi 192,132,108
qg 172,4,11264
qi 192,128,100
q0 8
v_permlane32_swap_b32 v8,v122
v_permlane32_swap_b32 v9,v123
qi 196,136,100
qg 176,4,12288
q9 8,8,122
qi 196,140,108
qc 2,1024
q9 9,9,123
qi 132,140,104
qg 180,4,13312
v_mov_b32 v122,v8
qi 132,136,96
q0 8
v_mov_b32 v123,v9
qi 136,144,96
qg 184,4,14336
qi 136,148,104
qc 2,2048
v_permlane16_swap_b32 v8,v122
qi 200,148,108
qg 188,4,15360
v_permlane16_swap_b32 v9,v123
qi 200,144,100
q0 8
q9 8,8,122
q9 9,9,123
qi 204,152,100
qg 128,4,16384
v_mov_b32 v16,v12
qi 204,156,108
v_mov_b32 v17,v13
v_mov_b32 v18,v8
qi 140,156,104
s_add_u32 s50,s80,16384
qg 132,4,17408
v_mov_b32 v19,v9
qi 140,152,96
q0 8
qc 2,3072
s_add_u32 m0,s74,4096
qi 144,160,96
qg 136,4,18432
qi 144,164,104
buffer_store_dwordx4 v[16:19],v2,s[32:35],s50 offen sc1
qb 32,128,129
qi 208,164,108
qg 140,4,19456
qb 33,130,131
qi 208,160,100
q0 8
qc 3,0
qb 34,132,133
qi 212,168,100
s_add_u32 s50,s80,0
qg 144,4,20480
qb 35,134,135
qi 212,172,108
buffer_store_dwordx4 v[32:35],v2,s[32:35],s50 offen offset:0 sc1
qb 64,192,193
qi 148,172,104
qg 148,4,21504
qb 65,194,195
qi 148,168,96
q0 8
qc 3,1024
qb 66,196,197
qi 152,176,96
s_add_u32 s50,s80,8192
qg 152,4,22528
qb 67,198,199
qi 152,180,104
buffer_store_dwordx4 v[64:67],v2,s[32:35],s50 offen offset:0 sc1
qb 36,136,137
qi 216,180,108
qg 156,4,23552
qb 37,138,139
qi 216,176,100
q0 8
qc 3,2048
qb 38,140,141
qi 220,184,100
s_add_u32 s50,s80,0
qg 160,4,24576
qb 39,142,143
qi 220,188,108
buffer_store_dwordx4 v[36:39],v2,s[32:35],s50 offen offset:1024 sc1
qb 68,200,201
qi 156,188,104
qg 164,4,25600
qb 69,202,203
qi 156,184,96
q0 8
qc 3,3072
s_add_u32 s72,s72,1
qi 160,128,96
qg 168,4,26624
v_readlane_b32 s73,v5,s72
qi 160,132,104
v_readlane_b32 s74,v6,s72
v_readlane_b32 s75,v7,s72
qi 224,132,108
qg 172,4,27648
qb 70,204,205
qi 224,128,100
q0 8
s_add_u32 s50,s80,8192
qb 71,206,207
qi 228,136,100
qg 176,4,28672
qi 228,140,108
buffer_store_dwordx4 v[68:71],v2,s[32:35],s50 offen offset:1024 sc1
qb 40,144,145
qi 164,140,104
qg 180,4,29696
qb 41,146,147
qi 164,136,96
q0 8
qb 42,148,149
qb 43,150,151
qi 168,144,96
s_add_u32 s50,s80,0
qg 184,4,30720
qi 168,148,104
buffer_store_dwordx4 v[40:43],v2,s[32:35],s50 offen offset:2048 sc1
qb 72,208,209
qi 232,148,108
qg 188,4,31744
qb 73,210,211
qi 232,144,100
s_waitcnt vmcnt(22) lgkmcnt(8)
s_barrier
v_add_u32 v4,s75,v2
qi 236,152,100
qg 128,4,0
qb 74,212,213
qi 236,156,108
s_add_u32 s50,s80,8192
qb 75,214,215
qi 172,156,104
qg 132,4,1024
qi 172,152,96
q0 8
buffer_store_dwordx4 v[72:75],v2,s[32:35],s50 offen offset:2048 sc1
qb 44,152,153
qi 176,160,96
qg 136,4,2048
qb 45,154,155
qi 176,164,104
s_add_u32 s50,s80,0
qb 46,156,157
qb 47,158,159
qi 240,164,108
LHH9_2kf:
s_cbranch_execz LHR9kf
qg 140,4,3072
qi 240,160,100
q0 8
buffer_store_dwordx4 v[44:47],v2,s[32:35],s50 offen offset:3072 sc1
qb 76,216,217
qi 244,168,100
qg 144,4,4096
qb 77,218,219
qi 244,172,108
s_add_u32 s50,s80,8192
qb 78,220,221
qb 79,222,223
qi 180,172,104
qg 148,4,5120
qi 180,168,96
q0 8
buffer_store_dwordx4 v[76:79],v2,s[32:35],s50 offen offset:3072 sc1
qb 48,160,161
qi 184,176,96
qg 152,4,6144
qb 49,162,163
qi 184,180,104
s_add_u32 s50,s80,4096
qb 50,164,165
qb 51,166,167
qi 248,180,108
qg 156,4,7168
qi 248,176,100
q0 8
buffer_store_dwordx4 v[48:51],v2,s[32:35],s50 offen offset:0 sc1
qb 80,224,225
qi 252,184,100
qg 160,4,8192
qb 81,226,227
qi 252,188,108
s_add_u32 s50,s80,12288
qb 82,228,229
qb 83,230,231
qi 188,188,104
qg 164,4,9216
qi 188,184,96
buffer_store_dwordx4 v[80:83],v2,s[32:35],s50 offen offset:0 sc1
s_add_u32 s50,s80,4096
qb 52,168,169
qb 53,170,171
qb 54,172,173
qb 55,174,175
buffer_store_dwordx4 v[52:55],v2,s[32:35],s50 offen offset:1024 sc1
s_add_u32 s50,s80,12288
qb 84,232,233
qb 85,234,235
qb 86,236,237
qb 87,238,239
buffer_store_dwordx4 v[84:87],v2,s[32:35],s50 offen offset:1024 sc1
s_add_u32 s50,s80,4096
qb 56,176,177
qb 57,178,179
qb 58,180,181
qb 59,182,183
buffer_store_dwordx4 v[56:59],v2,s[32:35],s50 offen offset:2048 sc1
s_add_u32 s50,s80,12288
qb 88,240,241
qb 89,242,243
qb 90,244,245
qb 91,246,247
buffer_store_dwordx4 v[88:91],v2,s[32:35],s50 offen offset:2048 sc1
s_nop 4
qb 60,184,185
qb 61,186,187
qb 62,188,189
qb 63,190,191
s_add_u32 s50,s80,4096
buffer_store_dwordx4 v[60:63],v2,s[32:35],s50 offen offset:3072 sc1
qb 92,248,249
qb 93,250,251
qb 94,252,253
qb 95,254,255
s_add_u32 s50,s80,12288
buffer_store_dwordx4 v[92:95],v2,s[32:35],s50 offen offset:3072 sc1
Lown44kf:
q0 8
qk 36,128,96
s_mov_b32 m0,s74
qg 168,4,10240
v_cmp_gt_i32 s[56:57],0,v14
v_cmp_gt_i32 s[58:59],1,v14
v_cmp_gt_i32 s[60:61],2,v14
ql 36,132,100
q0 7
qc 2,0
v_cmp_gt_i32 s[62:63],3,v14
v_cndmask_b32 v16,0,v126,s[56:57]
v_cndmask_b32 v17,0,v126,s[58:59]
ql 36,136,104
qg 172,4,11264
v_cndmask_b32 v18,0,v126,s[60:61]
v_cndmask_b32 v19,0,v126,s[62:63]
ql 36,140,108
q0 6
v_cmp_gt_i32 s[56:57],16,v14
v_cmp_gt_i32 s[58:59],17,v14
ql 36,144,112
qg 176,4,12288
v_cmp_gt_i32 s[60:61],18,v14
ql 36,148,116
q0 5
qc 2,1024
v_cmp_gt_i32 s[62:63],19,v14
ql 36,152,120
qg 180,4,13312
v_cndmask_b32 v20,0,v126,s[56:57]
ql 36,156,124
v_cndmask_b32 v21,0,v126,s[58:59]
v_cndmask_b32 v22,0,v126,s[60:61]
qn 32,128,64,16
qg 184,4,14336
v_cndmask_b32 v23,0,v126,s[62:63]
ql 32,132,68
qc 2,2048
v_cmp_gt_i32 s[56:57],32,v14
ql 32,136,72
qg 188,4,15360
v_cmp_gt_i32 s[58:59],33,v14
ql 32,140,76
v_cmp_gt_i32 s[60:61],34,v14
v_cmp_gt_i32 s[62:63],35,v14
ql 32,144,80
qg 128,4,16384
v_cndmask_b32 v24,0,v126,s[56:57]
ql 32,148,84
v_cndmask_b32 v25,0,v126,s[58:59]
v_cndmask_b32 v26,0,v126,s[60:61]
ql 32,152,88
qg 132,4,17408
v_cndmask_b32 v27,0,v126,s[62:63]
ql 32,156,92
q0 8
qc 2,3072
s_add_u32 m0,s74,4096
qk 44,160,96
qg 136,4,18432
v_cmp_gt_i32 s[56:57],48,v14
ql 44,164,100
q0 7
v_cmp_gt_i32 s[58:59],49,v14
v_cmp_gt_i32 s[60:61],50,v14
ql 44,168,104
qg 140,4,19456
v_cmp_gt_i32 s[62:63],51,v14
ql 44,172,108
q0 6
qc 3,0
v_cndmask_b32 v28,0,v126,s[56:57]
ql 44,176,112
qg 144,4,20480
v_cndmask_b32 v29,0,v126,s[58:59]
ql 44,180,116
q0 5
v_cndmask_b32 v30,0,v126,s[60:61]
v_cndmask_b32 v31,0,v126,s[62:63]
ql 44,184,120
qg 148,4,21504
v_max3_f32 v121,v36,v37,v38
ql 44,188,124
qc 3,1024
v_max_f32 v121,v121,v39
qn 40,160,64,20
qg 152,4,22528
v_max3_f32 v120,v32,v33,v34
ql 40,164,68
v_max_f32 v120,v120,v35
v_mov_b64 v[128:129],0
ql 40,168,72
qg 156,4,23552
v_mov_b64 v[130:131],0
ql 40,172,76
qc 3,2048
v_mov_b64 v[132:133],0
ql 40,176,80
qg 160,4,24576
v_max3_f32 v121,v121,v44,v45
ql 40,180,84
v_max3_f32 v121,v121,v46,v47
v_mov_b64 v[134:135],0
ql 40,184,88
qg 164,4,25600
v_mov_b64 v[136:137],0
ql 40,188,92
q0 8
qc 3,3072
s_add_u32 s72,s72,1
qk 52,128,96
qg 168,4,26624
v_readlane_b32 s73,v5,s72
ql 52,132,100
q0 7
v_readlane_b32 s74,v6,s72
v_readlane_b32 s75,v7,s72
ql 52,136,104
qg 172,4,27648
v_mov_b64 v[138:139],0
ql 52,140,108
q0 6
v_mov_b64 v[140:141],0
v_mov_b64 v[142:143],0
ql 52,144,112
qg 176,4,28672
v_max3_f32 v120,v120,v40,v41
ql 52,148,116
q0 5
v_max3_f32 v120,v120,v42,v43
v_mov_b64 v[144:145],0
ql 52,152,120
qg 180,4,29696
v_mov_b64 v[146:147],0
ql 52,156,124
v_mov_b64 v[148:149],0
v_mov_b64 v[150:151],0
qn 48,128,64,24
qg 184,4,30720
v_mov_b64 v[152:153],0
ql 48,132,68
v_mov_b64 v[154:155],0
v_mov_b64 v[156:157],0
ql 48,136,72
qg 188,4,31744
v_mov_b64 v[158:159],0
ql 48,140,76
q3 16
s_barrier
v_add_u32 v4,s75,v2
ql 48,144,80
qg 128,4,0
v_mov_b32 v124,s81
ql 48,148,84
v_mov_b32 v125,0
v_max3_f32 v121,v121,v52,v53
ql 48,152,88
qg 132,4,1024
v_max3_f32 v121,v121,v54,v55
ql 48,156,92
s_waitcnt vmcnt(8) lgkmcnt(8)
buffer_store_dword v124,v125,s[32:35],s93 offen sc1
v_mov_b64 v[160:161],0
qk 60,160,96
qg 136,4,2048
v_mov_b64 v[162:163],0
ql 60,164,100
q0 7
v_mov_b64 v[164:165],0
v_mov_b64 v[166:167],0
ql 60,168,104
qg 140,4,3072
v_mov_b64 v[168:169],0
ql 60,172,108
q0 6
v_mov_b64 v[170:171],0
v_mov_b64 v[172:173],0
ql 60,176,112
qg 144,4,4096
v_max3_f32 v120,v120,v48,v49
ql 60,180,116
q0 5
v_max3_f32 v120,v120,v50,v51
v_mov_b64 v[174:175],0
ql 60,184,120
qg 148,4,5120
v_mov_b64 v[176:177],0
ql 60,188,124
v_mov_b64 v[178:179],0
v_mov_b64 v[180:181],0
qn 56,160,64,28
qg 152,4,6144
v_mov_b64 v[182:183],0
ql 56,164,68
v_mov_b64 v[184:185],0
v_mov_b64 v[186:187],0
ql 56,168,72
qg 156,4,7168
v_mov_b64 v[188:189],0
ql 56,172,76
v_mov_b64 v[190:191],0
v_mov_b64 v[192:193],0
ql 56,176,80
qg 160,4,8192
v_max3_f32 v121,v121,v60,v61
ql 56,180,84
v_max3_f32 v121,v121,v62,v63
v_mov_b64 v[194:195],0
ql 56,184,88
qg 164,4,9216
v_mov_b64 v[196:197],0
ql 56,188,92
v_mov_b64 v[198:199],0
v_mov_b64 v[200:201],0
v_mov_b64 v[202:203],0
v_mov_b64 v[204:205],0
v_mov_b64 v[206:207],0
v_mov_b64 v[208:209],0
v_mov_b64 v[210:211],0
v_mov_b64 v[212:213],0
v_mov_b64 v[214:215],0
v_mov_b64 v[216:217],0
v_mov_b64 v[218:219],0
v_mov_b64 v[220:221],0
v_mov_b64 v[222:223],0
v_mov_b64 v[224:225],0
v_mov_b64 v[226:227],0
v_mov_b64 v[228:229],0
v_mov_b64 v[230:231],0
v_mov_b64 v[232:233],0
v_mov_b64 v[234:235],0
v_mov_b64 v[236:237],0
v_mov_b64 v[238:239],0
v_mov_b64 v[240:241],0
v_mov_b64 v[242:243],0
v_mov_b64 v[244:245],0
v_mov_b64 v[246:247],0
v_mov_b64 v[248:249],0
v_mov_b64 v[250:251],0
v_mov_b64 v[252:253],0
v_mov_b64 v[254:255],0
v_max3_f32 v120,v120,v56,v57
v_max3_f32 v120,v120,v58,v59
v_mov_b32 v122,v120
v_mov_b32 v123,v121
s_nop 0
v_permlane32_swap_b32 v120,v122
v_permlane32_swap_b32 v121,v123
s_nop 0
v_max_f32 v120,v120,v122
v_max_f32 v121,v121,v123
v_mov_b32 v122,v120
v_mov_b32 v123,v121
s_nop 0
v_permlane16_swap_b32 v120,v122
v_permlane16_swap_b32 v121,v123
s_nop 0
v_max_f32 v120,v120,v122
v_max_f32 v121,v121,v123
v_mov_b32 v12,v120
v_mov_b32 v8,0
v_sub_f32 v112,0,v120
v_sub_f32 v113,0,v120
v_sub_f32 v114,0,v120
v_sub_f32 v115,0,v120
v_mov_b32 v13,v121
v_mov_b32 v9,0
v_sub_f32 v116,0,v121
v_sub_f32 v117,0,v121
v_sub_f32 v118,0,v121
v_sub_f32 v119,0,v121
v_pk_add_f32 v[32:33],v[32:33],v[112:113]
v_pk_add_f32 v[34:35],v[34:35],v[112:113]
v_pk_add_f32 v[36:37],v[36:37],v[116:117]
v_pk_add_f32 v[38:39],v[38:39],v[116:117]
v_pk_add_f32 v[40:41],v[40:41],v[112:113]
v_pk_add_f32 v[42:43],v[42:43],v[112:113]
v_pk_add_f32 v[44:45],v[44:45],v[116:117]
v_pk_add_f32 v[46:47],v[46:47],v[116:117]
v_pk_add_f32 v[48:49],v[48:49],v[112:113]
v_pk_add_f32 v[50:51],v[50:51],v[112:113]
v_pk_add_f32 v[52:53],v[52:53],v[116:117]
v_pk_add_f32 v[54:55],v[54:55],v[116:117]
v_pk_add_f32 v[56:57],v[56:57],v[112:113]
v_pk_add_f32 v[58:59],v[58:59],v[112:113]
v_pk_add_f32 v[60:61],v[60:61],v[116:117]
v_pk_add_f32 v[62:63],v[62:63],v[116:117]
qm 64,32
qm 65,33
qm 66,34
qm 67,35
qm 68,36
qm 69,37
qm 70,38
qm 71,39
qm 72,40
qm 73,41
qm 74,42
qm 75,43
qm 76,44
qm 77,45
qm 78,46
qm 79,47
qm 80,48
qm 81,49
qm 82,50
qm 83,51
qm 84,52
qm 85,53
qm 86,54
qm 87,55
qm 88,56
qm 89,57
qm 90,58
qm 91,59
qm 92,60
qm 93,61
qm 94,62
qm 95,63
v_pk_add_f32 v[120:121],v[64:65],v[66:67]
v_pk_add_f32 v[122:123],v[68:69],v[70:71]
v_pk_add_f32 v[120:121],v[120:121],v[72:73]
v_pk_add_f32 v[122:123],v[122:123],v[76:77]
v_pk_add_f32 v[120:121],v[120:121],v[74:75]
v_pk_add_f32 v[122:123],v[122:123],v[78:79]
v_pk_add_f32 v[120:121],v[120:121],v[80:81]
v_pk_add_f32 v[122:123],v[122:123],v[84:85]
v_pk_add_f32 v[120:121],v[120:121],v[82:83]
v_pk_add_f32 v[122:123],v[122:123],v[86:87]
v_pk_add_f32 v[120:121],v[120:121],v[88:89]
v_pk_add_f32 v[122:123],v[122:123],v[92:93]
v_pk_add_f32 v[120:121],v[120:121],v[90:91]
v_pk_add_f32 v[122:123],v[122:123],v[94:95]
q9 10,120,121
q9 11,122,123
s_cmp_eq_u32 s70,0
s_cbranch_scc1 Lskipd048kf
s_lshl_b32 s77,s70,1
s_sub_u32 s77,s77,1
v_subrev_u32 v14,4096,v14
s_cmp_eq_u32 s77,0
s_cbranch_scc1 Lolx50kf
.p2align 6
Lol49kf:
q0 8
qn 32,128,64,112
s_mov_b32 m0,s74
qg 168,4,10240
q9 8,8,10
ql 32,132,68
q0 7
qc 2,0
q9 9,9,11
ql 32,136,72
qg 172,4,11264
qb 96,64,65
ql 32,140,76
q0 6
qb 97,66,67
qb 98,72,73
ql 32,144,80
qg 176,4,12288
qb 99,74,75
ql 32,148,84
q0 5
qc 2,1024
qb 100,68,69
ql 32,152,88
qg 180,4,13312
qb 101,70,71
ql 32,156,92
qb 102,76,77
qb 103,78,79
qn 36,128,96,116
qg 184,4,14336
qb 104,80,81
ql 36,132,100
qc 2,2048
qb 105,82,83
ql 36,136,104
qg 188,4,15360
qb 106,88,89
ql 36,140,108
qb 107,90,91
qb 108,84,85
ql 36,144,112
qg 128,4,16384
qb 109,86,87
ql 36,148,116
qb 110,92,93
qb 111,94,95
ql 36,152,120
qg 132,4,17408
ql 36,156,124
q0 8
qc 2,3072
s_add_u32 m0,s74,4096
qn 40,160,64,112
qg 136,4,18432
ql 40,164,68
q0 7
qm 64,32
ql 40,168,72
qg 140,4,19456
ql 40,172,76
q0 6
qc 3,0
ql 40,176,80
qg 144,4,20480
ql 40,180,84
q0 5
qm 65,33
ql 40,184,88
qg 148,4,21504
q9 10,64,65
ql 40,188,92
qc 3,1024
qn 44,160,96,116
qg 152,4,22528
ql 44,164,100
qm 66,34
ql 44,168,104
qg 156,4,23552
q9 10,10,66
ql 44,172,108
qc 3,2048
ql 44,176,112
qg 160,4,24576
ql 44,180,116
qm 67,35
ql 44,184,120
qg 164,4,25600
q9 10,10,67
ql 44,188,124
q0 8
qc 3,3072
s_add_u32 s72,s72,1
qn 48,128,64,112
qg 168,4,26624
v_readlane_b32 s73,v5,s72
ql 48,132,68
q0 7
qm 68,36
ql 48,136,72
qg 172,4,27648
v_readlane_b32 s74,v6,s72
ql 48,140,76
q0 6
qm 69,37
ql 48,144,80
qg 176,4,28672
v_readlane_b32 s75,v7,s72
ql 48,148,84
q0 5
qm 70,38
ql 48,152,88
qg 180,4,29696
q9 11,68,69
ql 48,156,92
qm 71,39
qn 52,128,96,116
qg 184,4,30720
q9 11,11,70
ql 52,132,100
qm 72,40
ql 52,136,104
qg 188,4,31744
q9 11,11,71
ql 52,140,108
q3 16
s_barrier
v_add_u32 v4,s75,v2
ql 52,144,112
qg 128,4,0
q9 10,10,72
ql 52,148,116
qm 73,41
ql 52,152,120
qg 132,4,1024
q9 10,10,73
ql 52,156,124
q0 8
qm 74,42
qn 56,160,64,112
qg 136,4,2048
q9 10,10,74
ql 56,164,68
q0 7
qm 75,43
ql 56,168,72
qg 140,4,3072
q9 10,10,75
ql 56,172,76
q0 6
qm 76,44
ql 56,176,80
qg 144,4,4096
q9 11,11,76
ql 56,180,84
q0 5
qm 77,45
ql 56,184,88
qg 148,4,5120
q9 11,11,77
ql 56,188,92
qn 60,160,96,116
qg 152,4,6144
ql 60,164,100
qm 78,46
ql 60,168,104
qg 156,4,7168
q9 11,11,78
ql 60,172,108
qm 79,47
ql 60,176,112
qg 160,4,8192
q9 11,11,79
ql 60,180,116
ql 60,184,120
qg 164,4,9216
ql 60,188,124
q0 8
qm 80,48
qi 128,128,96
s_mov_b32 m0,s74
qg 168,4,10240
q9 10,10,80
qi 128,132,104
qc 2,0
qi 192,132,108
qg 172,4,11264
qi 192,128,100
q0 8
qm 81,49
qi 196,136,100
qg 176,4,12288
q9 10,10,81
qi 196,140,108
qc 2,1024
qi 132,140,104
qg 180,4,13312
qi 132,136,96
LHH9_3kf:
s_cbranch_execz LHR9kf
q0 8
qm 82,50
qi 136,144,96
qg 184,4,14336
q9 10,10,82
qi 136,148,104
qc 2,2048
qi 200,148,108
qg 188,4,15360
qi 200,144,100
q0 8
qm 83,51
qi 204,152,100
qg 128,4,16384
q9 10,10,83
qi 204,156,108
qm 84,52
qi 140,156,104
qg 132,4,17408
q9 11,11,84
qi 140,152,96
q0 8
qc 2,3072
s_add_u32 m0,s74,4096
qi 144,160,96
qg 136,4,18432
qi 144,164,104
qm 85,53
qi 208,164,108
qg 140,4,19456
q9 11,11,85
qi 208,160,100
q0 8
qc 3,0
qi 212,168,100
qg 144,4,20480
qi 212,172,108
qm 86,54
qi 148,172,104
qg 148,4,21504
q9 11,11,86
qi 148,168,96
q0 8
qc 3,1024
qi 152,176,96
qg 152,4,22528
qi 152,180,104
qm 87,55
qi 216,180,108
qg 156,4,23552
q9 11,11,87
qi 216,176,100
q0 8
qc 3,2048
qi 220,184,100
qg 160,4,24576
qi 220,188,108
qm 88,56
qi 156,188,104
qg 164,4,25600
q9 10,10,88
qi 156,184,96
q0 8
qc 3,3072
s_add_u32 s72,s72,1
qi 160,128,96
qg 168,4,26624
v_readlane_b32 s73,v5,s72
qi 160,132,104
qm 89,57
qi 224,132,108
qg 172,4,27648
v_readlane_b32 s74,v6,s72
qi 224,128,100
q0 8
qm 90,58
qi 228,136,100
qg 176,4,28672
v_readlane_b32 s75,v7,s72
qi 228,140,108
qm 91,59
qi 164,140,104
qg 180,4,29696
q9 10,10,89
qi 164,136,96
q0 8
qm 92,60
qi 168,144,96
qg 184,4,30720
q9 10,10,90
qi 168,148,104
qm 93,61
qi 232,148,108
qg 188,4,31744
q9 10,10,91
qi 232,144,100
s_waitcnt vmcnt(16) lgkmcnt(8)
s_barrier
v_add_u32 v4,s75,v2
qi 236,152,100
qg 128,4,0
q9 11,11,92
qi 236,156,108
qm 94,62
qi 172,156,104
qg 132,4,1024
q9 11,11,93
qi 172,152,96
q0 8
qm 95,63
qi 176,160,96
qg 136,4,2048
q9 11,11,94
qi 176,164,104
q9 11,11,95
qi 240,164,108
qg 140,4,3072
q9 120,10,11
qi 240,160,100
q0 8
v_cmp_nle_f32 vcc,v120,v127
qi 244,168,100
qg 144,4,4096
qi 244,172,108
qi 180,172,104
qg 148,4,5120
qi 180,168,96
q0 8
qi 184,176,96
qg 152,4,6144
qi 184,180,104
qi 248,180,108
qg 156,4,7168
qi 248,176,100
q0 8
qi 252,184,100
qg 160,4,8192
qi 252,188,108
qi 188,188,104
qg 164,4,9216
s_sub_u32 s77,s77,1
s_cmp_lg_u32 s77,0
qi 188,184,96
s_cbranch_vccnz Lit51kf_rs_nb
Lit51kf_ok_nb:
s_cbranch_scc1 Lol49kf
Lolx50kf:
q0 8
v_cmp_gt_i32 s[56:57],0,v14
v_cmp_gt_i32 s[58:59],1,v14
v_cmp_gt_i32 s[60:61],2,v14
v_cmp_gt_i32 s[62:63],3,v14
v_cndmask_b32 v16,v112,v126,s[56:57]
v_cndmask_b32 v17,v112,v126,s[58:59]
v_cndmask_b32 v18,v112,v126,s[60:61]
v_cndmask_b32 v19,v112,v126,s[62:63]
qn 36,128,96,116
s_mov_b32 m0,s74
qg 168,4,10240
v_cmp_gt_i32 s[56:57],16,v14
ql 36,132,100
q0 7
qc 2,0
v_cmp_gt_i32 s[58:59],17,v14
ql 36,136,104
qg 172,4,11264
v_cmp_gt_i32 s[60:61],18,v14
ql 36,140,108
q0 6
q9 8,8,10
q9 9,9,11
ql 36,144,112
qg 176,4,12288
v_cmp_gt_i32 s[62:63],19,v14
ql 36,148,116
q0 5
qc 2,1024
v_cndmask_b32 v20,v112,v126,s[56:57]
ql 36,152,120
qg 180,4,13312
v_cndmask_b32 v21,v112,v126,s[58:59]
ql 36,156,124
qb 96,64,65
qb 97,66,67
qn 32,128,64,16
qg 184,4,14336
v_cndmask_b32 v22,v112,v126,s[60:61]
ql 32,132,68
qc 2,2048
v_cndmask_b32 v23,v112,v126,s[62:63]
ql 32,136,72
qg 188,4,15360
v_cmp_gt_i32 s[56:57],32,v14
ql 32,140,76
qb 98,72,73
qb 99,74,75
ql 32,144,80
qg 128,4,16384
v_cmp_gt_i32 s[58:59],33,v14
ql 32,148,84
qb 100,68,69
qb 101,70,71
ql 32,152,88
qg 132,4,17408
v_cmp_gt_i32 s[60:61],34,v14
ql 32,156,92
q0 8
qc 2,3072
s_add_u32 m0,s74,4096
qn 44,160,96,116
qg 136,4,18432
v_cmp_gt_i32 s[62:63],35,v14
ql 44,164,100
q0 7
qb 102,76,77
qb 103,78,79
ql 44,168,104
qg 140,4,19456
v_cndmask_b32 v24,v112,v126,s[56:57]
ql 44,172,108
q0 6
qc 3,0
v_cndmask_b32 v25,v112,v126,s[58:59]
ql 44,176,112
qg 144,4,20480
v_cndmask_b32 v26,v112,v126,s[60:61]
ql 44,180,116
q0 5
qb 104,80,81
qb 105,82,83
ql 44,184,120
qg 148,4,21504
v_cndmask_b32 v27,v112,v126,s[62:63]
ql 44,188,124
qc 3,1024
v_cmp_gt_i32 s[56:57],48,v14
qn 40,160,64,20
qg 152,4,22528
v_cmp_gt_i32 s[58:59],49,v14
ql 40,164,68
qb 106,88,89
qb 107,90,91
ql 40,168,72
qg 156,4,23552
v_cmp_gt_i32 s[60:61],50,v14
ql 40,172,76
qc 3,2048
v_cmp_gt_i32 s[62:63],51,v14
ql 40,176,80
qg 160,4,24576
v_cndmask_b32 v28,v112,v126,s[56:57]
ql 40,180,84
qb 108,84,85
qb 109,86,87
ql 40,184,88
qg 164,4,25600
v_cndmask_b32 v29,v112,v126,s[58:59]
ql 40,188,92
q0 8
qc 3,3072
s_add_u32 s72,s72,1
qn 52,128,96,116
qg 168,4,26624
v_readlane_b32 s73,v5,s72
ql 52,132,100
q0 7
v_cndmask_b32 v30,v112,v126,s[60:61]
v_cndmask_b32 v31,v112,v126,s[62:63]
ql 52,136,104
qg 172,4,27648
v_readlane_b32 s74,v6,s72
ql 52,140,108
q0 6
qb 110,92,93
qb 111,94,95
ql 52,144,112
qg 176,4,28672
v_readlane_b32 s75,v7,s72
ql 52,148,116
q0 5
qm 68,36
ql 52,152,120
qg 180,4,29696
ql 52,156,124
qm 69,37
qn 48,128,64,24
qg 184,4,30720
q9 11,68,69
ql 48,132,68
qm 70,38
ql 48,136,72
qg 188,4,31744
q9 11,11,70
ql 48,140,76
q3 16
s_barrier
v_add_u32 v4,s75,v2
ql 48,144,80
qg 128,4,0
ql 48,148,84
qm 71,39
ql 48,152,88
qg 132,4,1024
q9 11,11,71
ql 48,156,92
q0 8
qm 64,32
qn 60,160,96,116
qg 136,4,2048
ql 60,164,100
q0 7
qm 65,33
ql 60,168,104
qg 140,4,3072
q9 10,64,65
ql 60,172,108
q0 6
qm 66,34
ql 60,176,112
qg 144,4,4096
q9 10,10,66
ql 60,180,116
q0 5
qm 67,35
ql 60,184,120
qg 148,4,5120
q9 10,10,67
ql 60,188,124
qm 76,44
qn 56,160,64,28
qg 152,4,6144
q9 11,11,76
ql 56,164,68
qm 77,45
ql 56,168,72
qg 156,4,7168
q9 11,11,77
ql 56,172,76
qm 78,46
ql 56,176,80
qg 160,4,8192
q9 11,11,78
ql 56,180,84
qm 79,47
ql 56,184,88
qg 164,4,9216
q9 11,11,79
ql 56,188,92
q0 8
qm 72,40
qi 128,128,96
s_mov_b32 m0,s74
qg 168,4,10240
q9 10,10,72
qi 128,132,104
qc 2,0
qi 192,132,108
qg 172,4,11264
qi 192,128,100
q0 8
qm 73,41
qi 196,136,100
qg 176,4,12288
q9 10,10,73
qi 196,140,108
qc 2,1024
qi 132,140,104
qg 180,4,13312
qi 132,136,96
q0 8
qm 74,42
)"
R"(qi 136,144,96
qg 184,4,14336
q9 10,10,74
qi 136,148,104
qc 2,2048
qi 200,148,108
qg 188,4,15360
qi 200,144,100
q0 8
qm 75,43
qi 204,152,100
qg 128,4,16384
q9 10,10,75
qi 204,156,108
qm 84,52
qi 140,156,104
qg 132,4,17408
q9 11,11,84
qi 140,152,96
q0 8
qc 2,3072
s_add_u32 m0,s74,4096
qi 144,160,96
qg 136,4,18432
qi 144,164,104
qm 85,53
qi 208,164,108
qg 140,4,19456
q9 11,11,85
qi 208,160,100
q0 8
qc 3,0
qi 212,168,100
qg 144,4,20480
qi 212,172,108
qm 86,54
qi 148,172,104
qg 148,4,21504
q9 11,11,86
qi 148,168,96
q0 8
qc 3,1024
qi 152,176,96
qg 152,4,22528
qi 152,180,104
qm 87,55
qi 216,180,108
qg 156,4,23552
q9 11,11,87
qi 216,176,100
q0 8
qc 3,2048
qi 220,184,100
qg 160,4,24576
qi 220,188,108
qm 80,48
qi 156,188,104
qg 164,4,25600
q9 10,10,80
qi 156,184,96
q0 8
qc 3,3072
s_add_u32 s72,s72,1
qi 160,128,96
qg 168,4,26624
v_readlane_b32 s73,v5,s72
qi 160,132,104
qm 81,49
qi 224,132,108
qg 172,4,27648
v_readlane_b32 s74,v6,s72
qi 224,128,100
q0 8
qm 82,50
qi 228,136,100
qg 176,4,28672
v_readlane_b32 s75,v7,s72
qi 228,140,108
qm 83,51
qi 164,140,104
qg 180,4,29696
q9 10,10,81
qi 164,136,96
q0 8
qm 92,60
qi 168,144,96
qg 184,4,30720
q9 10,10,82
qi 168,148,104
qm 93,61
qi 232,148,108
qg 188,4,31744
q9 10,10,83
qi 232,144,100
s_waitcnt vmcnt(16) lgkmcnt(8)
s_barrier
v_add_u32 v4,s75,v2
qi 236,152,100
qg 128,4,0
q9 11,11,92
qi 236,156,108
qm 94,62
qi 172,156,104
qg 132,4,1024
q9 11,11,93
qi 172,152,96
q0 8
qm 95,63
qi 176,160,96
qg 136,4,2048
q9 11,11,94
qi 176,164,104
qm 88,56
qi 240,164,108
qg 140,4,3072
q9 11,11,95
qi 240,160,100
q0 8
qm 89,57
qi 244,168,100
qg 144,4,4096
q9 10,10,88
qi 244,172,108
qm 90,58
qi 180,172,104
qg 148,4,5120
q9 10,10,89
qi 180,168,96
q0 8
qm 91,59
qi 184,176,96
qg 152,4,6144
q9 10,10,90
qi 184,180,104
q9 10,10,91
qi 248,180,108
qg 156,4,7168
q9 120,10,11
qi 248,176,100
q0 8
v_cmp_nle_f32 vcc,v120,v127
qi 252,184,100
qg 160,4,8192
qi 252,188,108
qi 188,188,104
qg 164,4,9216
qi 188,184,96
s_cbranch_vccnz Ld052kf_rs_nb
Ld052kf_ok_nb:
Lskipd048kf:
v_and_b32 v124,7,v1
v_lshlrev_b32 v124,7,v124
buffer_load_dword v125,v124,s[32:35],s94 offen sc1
q0 8
v_cmp_gt_i32 s[56:57],0,v15
v_cmp_gt_i32 s[58:59],1,v15
v_cmp_gt_i32 s[60:61],2,v15
v_cmp_gt_i32 s[62:63],3,v15
v_cndmask_b32 v16,v116,v126,s[56:57]
v_cndmask_b32 v17,v116,v126,s[58:59]
v_cndmask_b32 v18,v116,v126,s[60:61]
v_cndmask_b32 v19,v116,v126,s[62:63]
v_cmp_gt_i32 s[56:57],16,v15
v_cmp_gt_i32 s[58:59],17,v15
v_cmp_gt_i32 s[60:61],18,v15
v_cmp_gt_i32 s[62:63],19,v15
v_cndmask_b32 v20,v116,v126,s[56:57]
v_cndmask_b32 v21,v116,v126,s[58:59]
v_cndmask_b32 v22,v116,v126,s[60:61]
v_cndmask_b32 v23,v116,v126,s[62:63]
qn 36,128,96,16
s_mov_b32 m0,s74
qg 168,4,10240
v_cmp_gt_i32 s[56:57],32,v15
ql 36,132,100
q0 7
qg 172,4,11264
v_cmp_gt_i32 s[58:59],33,v15
ql 36,136,104
qg 176,4,12288
v_cmp_gt_i32 s[60:61],34,v15
ql 36,140,108
q0 7
qg 180,4,13312
v_cmp_gt_i32 s[62:63],35,v15
ql 36,144,112
qg 184,4,14336
v_cndmask_b32 v24,v116,v126,s[56:57]
ql 36,148,116
q0 7
qg 188,4,15360
v_cndmask_b32 v25,v116,v126,s[58:59]
ql 36,152,120
qg 128,4,16384
v_cndmask_b32 v26,v116,v126,s[60:61]
ql 36,156,124
q0 7
qg 132,4,17408
v_cndmask_b32 v27,v116,v126,s[62:63]
qn 44,160,96,20
qg 136,4,18432
v_cmp_gt_i32 s[56:57],48,v15
ql 44,164,100
q0 7
qg 140,4,19456
v_cmp_gt_i32 s[58:59],49,v15
ql 44,168,104
qg 144,4,20480
v_cmp_gt_i32 s[60:61],50,v15
ql 44,172,108
q0 7
qg 148,4,21504
v_cmp_gt_i32 s[62:63],51,v15
ql 44,176,112
qg 152,4,22528
v_cndmask_b32 v28,v116,v126,s[56:57]
ql 44,180,116
q0 7
qg 156,4,23552
v_cndmask_b32 v29,v116,v126,s[58:59]
ql 44,184,120
qg 160,4,24576
v_cndmask_b32 v30,v116,v126,s[60:61]
ql 44,188,124
q0 7
qg 164,4,25600
v_cndmask_b32 v31,v116,v126,s[62:63]
qn 52,128,96,24
qg 168,4,26624
q9 8,8,10
ql 52,132,100
LHH10_0kf:
s_cbranch_execz LHR10kf
q0 7
qg 172,4,27648
q9 9,9,11
ql 52,136,104
qg 176,4,28672
qb 96,64,65
ql 52,140,108
q0 7
qg 180,4,29696
qb 97,66,67
ql 52,144,112
s_add_u32 s72,s72,1
qg 184,4,30720
qc 2,0
qc 2,1024
qc 2,2048
qc 2,3072
s_add_u32 m0,s74,4096
s_nop 0
qc 3,0
qc 3,1024
qc 3,2048
qc 3,3072
v_readlane_b32 s73,v5,s72
v_readlane_b32 s74,v6,s72
v_readlane_b32 s75,v7,s72
ql 52,148,116
q0 7
qg 188,4,31744
q3 17
s_barrier
v_add_u32 v4,s75,v2
ql 52,152,120
qg 128,4,0
qb 98,72,73
ql 52,156,124
q0 7
qg 132,4,1024
qb 99,74,75
qn 60,160,96,28
qg 136,4,2048
qb 100,68,69
ql 60,164,100
q0 7
qg 140,4,3072
qb 101,70,71
ql 60,168,104
qg 144,4,4096
q3 8
ql 60,172,108
q0 7
qg 148,4,5120
v_cmp_ne_u32 vcc,s81,v125
ql 60,176,112
qg 152,4,6144
s_cbranch_vccnz Lslow41kf_nb
Lback42kf_nb:
ql 60,180,116
q0 7
qg 156,4,7168
qb 102,76,77
qb 103,78,79
qb 104,80,81
qb 105,82,83
qb 106,88,89
qb 107,90,91
qb 108,84,85
qb 109,86,87
qb 110,92,93
qb 111,94,95
ql 60,184,120
qg 160,4,8192
v_mov_b32 v10,0
ql 60,188,124
q0 7
qg 164,4,9216
qi 128,128,96
s_mov_b32 m0,s74
qg 168,4,10240
qi 128,132,104
qc 2,0
qi 192,132,108
qg 172,4,11264
qi 192,128,100
q0 8
buffer_load_dwordx4 a[192:195],v2,s[32:35],s96 offen sc1
s_add_u32 s96,s96,114688
qi 196,136,100
qg 176,4,12288
qi 196,140,108
qc 2,1024
qi 132,140,104
qg 180,4,13312
qi 132,136,96
q0 8
buffer_load_dwordx4 a[224:227],v2,s[32:35],s97 offen sc1
s_add_u32 s97,s97,114688
qi 136,144,96
qg 184,4,14336
qi 136,148,104
qc 2,2048
qi 200,148,108
qg 188,4,15360
qi 200,144,100
q0 8
buffer_load_dwordx4 a[196:199],v2,s[32:35],s96 offen sc1
s_add_u32 s96,s96,114688
qi 204,152,100
qg 128,4,16384
qi 204,156,108
qm 68,36
qi 140,156,104
qg 132,4,17408
qi 140,152,96
q0 8
qc 2,3072
s_add_u32 m0,s74,4096
qi 144,160,96
qg 136,4,18432
qi 144,164,104
buffer_load_dwordx4 a[228:231],v2,s[32:35],s97 offen sc1
s_add_u32 s97,s97,114688
qi 208,164,108
qg 140,4,19456
qi 208,160,100
q0 8
qc 3,0
qi 212,168,100
qg 144,4,20480
qi 212,172,108
buffer_load_dwordx4 a[200:203],v2,s[32:35],s96 offen sc1
s_add_u32 s96,s96,114688
qi 148,172,104
qg 148,4,21504
qi 148,168,96
q0 8
qc 3,1024
qi 152,176,96
qg 152,4,22528
qi 152,180,104
buffer_load_dwordx4 a[232:235],v2,s[32:35],s97 offen sc1
s_add_u32 s97,s97,114688
qi 216,180,108
qg 156,4,23552
qi 216,176,100
q0 8
qc 3,2048
qi 220,184,100
qg 160,4,24576
qi 220,188,108
buffer_load_dwordx4 a[204:207],v2,s[32:35],s96 offen sc1
s_add_u32 s96,s96,114688
qi 156,188,104
qg 164,4,25600
qi 156,184,96
q0 8
qc 3,3072
s_add_u32 s72,s72,1
qi 160,128,96
qg 168,4,26624
v_readlane_b32 s73,v5,s72
qi 160,132,104
buffer_load_dwordx4 a[236:239],v2,s[32:35],s97 offen sc1
s_add_u32 s97,s97,114688
qi 224,132,108
qg 172,4,27648
v_readlane_b32 s74,v6,s72
qi 224,128,100
q0 8
qm 69,37
qi 228,136,100
qg 176,4,28672
v_readlane_b32 s75,v7,s72
qi 228,140,108
buffer_load_dwordx4 a[208:211],v2,s[32:35],s96 offen sc1
s_add_u32 s96,s96,114688
qi 164,140,104
qg 180,4,29696
q9 11,68,69
qi 164,136,96
q0 8
qm 70,38
qi 168,144,96
qg 184,4,30720
q9 11,11,70
qi 168,148,104
buffer_load_dwordx4 a[240:243],v2,s[32:35],s97 offen sc1
s_add_u32 s97,s97,114688
qi 232,148,108
qg 188,4,31744
qi 232,144,100
s_waitcnt vmcnt(27) lgkmcnt(8)
s_barrier
v_add_u32 v4,s75,v2
qi 236,152,100
qg 128,4,0
qi 236,156,108
buffer_load_dwordx4 a[212:215],v2,s[32:35],s96 offen sc1
s_add_u32 s96,s96,114688
qi 172,156,104
qg 132,4,1024
qi 172,152,96
q0 8
qm 71,39
qi 176,160,96
qg 136,4,2048
q9 11,11,71
qi 176,164,104
qm 76,44
qi 240,164,108
qg 140,4,3072
q9 11,11,76
qi 240,160,100
q0 8
qm 77,45
qi 244,168,100
qg 144,4,4096
q9 11,11,77
qi 244,172,108
qm 78,46
qi 180,172,104
qg 148,4,5120
q9 11,11,78
qi 180,168,96
q0 8
qm 79,47
qi 184,176,96
qg 152,4,6144
q9 11,11,79
qi 184,180,104
qm 84,52
qi 248,180,108
qg 156,4,7168
q9 11,11,84
qi 248,176,100
q0 8
qm 85,53
qi 252,184,100
qg 160,4,8192
q9 11,11,85
qi 252,188,108
qm 86,54
qi 188,188,104
qg 164,4,9216
q9 11,11,86
qi 188,184,96
qm 87,55
qm 92,60
qm 93,61
qm 94,62
qm 95,63
q9 11,11,87
q9 11,11,92
q9 11,11,93
q9 11,11,94
q9 11,11,95
v_cmp_nle_f32 vcc,v11,v127
s_nop 0
s_cbranch_vccnz Ld153kf_rs_nb
Ld153kf_ok_nb:
q9 8,8,10
q9 9,9,11
qb 100,68,69
qb 101,70,71
qb 102,76,77
qb 103,78,79
qb 108,84,85
qb 109,86,87
qb 110,92,93
qb 111,94,95
q0 8
qi 192,128,100
s_mov_b32 m0,s74
qg 168,4,10240
v_mov_b32 v122,v8
qi 192,132,108
q0 7
qg 172,4,11264
v_mov_b32 v123,v9
qi 196,140,108
qg 176,4,12288
qi 196,136,100
q0 7
qg 180,4,13312
v_permlane32_swap_b32 v8,v122
qi 200,144,100
qg 184,4,14336
v_permlane32_swap_b32 v9,v123
qi 200,148,108
q0 7
qg 188,4,15360
q9 8,8,122
qi 204,156,108
qg 128,4,16384
q9 9,9,123
qi 204,152,100
q0 7
qg 132,4,17408
v_mov_b32 v122,v8
qi 208,160,100
qg 136,4,18432
v_mov_b32 v123,v9
qi 208,164,108
q0 7
qg 140,4,19456
qb 32,128,129
qi 212,172,108
qg 144,4,20480
v_permlane16_swap_b32 v8,v122
qi 212,168,100
q0 7
qg 148,4,21504
v_permlane16_swap_b32 v9,v123
qi 216,176,100
qg 152,4,22528
q9 8,8,122
qi 216,180,108
q0 7
qg 156,4,23552
q9 9,9,123
qi 220,188,108
qg 160,4,24576
qb 33,130,131
qi 220,184,100
q0 7
qg 164,4,25600
qb 34,132,133
qi 224,128,100
qg 168,4,26624
qb 35,134,135
qi 224,132,108
q0 7
qg 172,4,27648
qb 36,136,137
qi 228,140,108
qg 176,4,28672
qb 37,138,139
qi 228,136,100
q0 7
qg 180,4,29696
qb 38,140,141
qi 232,144,100
s_add_u32 s72,s72,1
qg 184,4,30720
qc 2,0
qc 2,1024
qc 2,2048
qc 2,3072
s_add_u32 m0,s74,4096
s_nop 0
qc 3,0
qc 3,1024
qc 3,2048
qc 3,3072
v_readlane_b32 s73,v5,s72
v_readlane_b32 s74,v6,s72
v_readlane_b32 s75,v7,s72
qi 232,148,108
q0 7
qg 188,4,31744
q3 27
s_barrier
v_add_u32 v4,s75,v2
qi 236,156,108
qg 128,4,0
qb 39,142,143
qi 236,152,100
q0 7
qg 132,4,1024
qb 40,144,145
qi 240,160,100
qg 136,4,2048
qb 41,146,147
qi 240,164,108
q0 7
qg 140,4,3072
qb 42,148,149
qi 244,172,108
qg 144,4,4096
qb 43,150,151
qi 244,168,100
q0 7
qg 148,4,5120
qb 44,152,153
qi 248,176,100
qg 152,4,6144
qb 45,154,155
qi 248,180,108
q0 7
qg 156,4,7168
qb 46,156,157
qi 252,188,108
qg 160,4,8192
qb 47,158,159
qi 252,184,100
qg 164,4,9216
v_rcp_f32 v8,v8
buffer_load_dwordx4 a[244:247],v2,s[32:35],s97 offen sc1
v_rcp_f32 v9,v9
qb 48,160,161
qb 49,162,163
qb 50,164,165
s_add_u32 s97,s97,114688
qb 51,166,167
qb 52,168,169
qb 53,170,171
qb 54,172,173
buffer_load_dwordx4 a[216:219],v2,s[32:35],s96 offen sc1
qb 55,174,175
qb 56,176,177
qb 57,178,179
qb 58,180,181
s_add_u32 s96,s96,114688
qb 59,182,183
qb 60,184,185
qb 61,186,187
qb 62,188,189
buffer_load_dwordx4 a[248:251],v2,s[32:35],s97 offen sc1
qb 63,190,191
qb 64,192,193
qb 65,194,195
qb 66,196,197
s_add_u32 s97,s97,114688
qb 67,198,199
qb 68,200,201
qb 69,202,203
qb 70,204,205
buffer_load_dwordx4 a[220:223],v2,s[32:35],s96 offen sc1
qb 71,206,207
qb 72,208,209
qb 73,210,211
qb 74,212,213
buffer_load_dwordx4 a[252:255],v2,s[32:35],s97 offen sc1
qb 75,214,215
qb 76,216,217
qb 77,218,219
qb 78,220,221
qb 79,222,223
qb 80,224,225
qb 81,226,227
qb 82,228,229
qb 83,230,231
qb 84,232,233
qb 85,234,235
qb 86,236,237
qb 87,238,239
qb 88,240,241
qb 89,242,243
qb 90,244,245
qb 91,246,247
qb 92,248,249
qb 93,250,251
qb 94,252,253
qb 95,254,255
s_lshl4_add_u32 s48,s82,s68
s_mul_i32 s48,s48,114688
s_lshl_b32 s49,s31,12
s_add_u32 s48,s48,s49
s_add_u32 s80,s48,79306752
q0 8
qh 192,128,32
s_mov_b32 m0,s74
qg 168,4,10240
qi 192,132,36
q0 7
qc 2,0
qi 192,136,40
s_mov_b32 s44,s22
qg 172,4,11264
qi 192,140,44
q0 6
s_and_b32 s45,s23,0xffff
qi 192,144,48
s_mov_b32 s46,896
qg 176,4,12288
qi 192,148,52
q0 5
qc 2,1024
s_mov_b32 s47,0x20000
qi 192,152,56
s_mov_b32 s36,s4
qg 180,4,13312
qi 192,156,60
s_and_b32 s37,s5,0xffff
qh 196,128,64
s_mov_b32 s38,1835008
qg 184,4,14336
qi 196,132,68
s_mov_b32 s39,0x20000
qc 2,2048
qi 196,136,72
s_mov_b32 s40,s24
qg 188,4,15360
qi 196,140,76
s_and_b32 s41,s25,0xffff
qi 196,144,80
s_mov_b32 s42,1835008
qg 128,4,16384
qi 196,148,84
s_mov_b32 s43,0x20000
qi 196,152,88
s_lshl2_add_u32 s48,s82,s31
qg 132,4,17408
qi 196,156,92
q0 8
qc 2,3072
s_add_u32 m0,s74,4096
qh 200,160,32
s_lshl4_add_u32 s48,s48,s68
qg 136,4,18432
qi 200,164,36
q0 7
s_lshl_b32 s48,s48,7
qi 200,168,40
s_add_u32 s93,s48,393216
qg 140,4,19456
qi 200,172,44
q0 6
qc 3,0
s_add_u32 s4,s93,131072
qi 200,176,48
s_add_u32 s5,s93,262144
qg 144,4,20480
qi 200,180,52
q0 5
s_add_u32 s8,s93,393216
qi 200,184,56
qg 148,4,21504
v_mov_b32 v120,s81
qi 200,188,60
qc 3,1024
v_mov_b32 v121,0
qh 204,160,64
s_lshl_b32 s9,s70,1
qg 152,4,22528
qi 204,164,68
s_sub_u32 s9,15,s9
qi 204,168,72
s_lshl_b32 s50,s9,12
qg 156,4,23552
qi 204,172,76
s_add_u32 s14,s94,s50
qc 3,2048
qi 204,176,80
qg 160,4,24576
v_and_b32 v27,7,v1
qi 204,180,84
v_lshlrev_b32 v27,7,v27
qa 192,8,192
qi 204,184,88
qg 164,4,25600
qa 193,8,193
qi 204,188,92
q0 8
qc 3,3072
s_add_u32 s72,s72,1
qh 208,128,32
qg 168,4,26624
v_readlane_b32 s73,v5,s72
qi 208,132,36
q0 7
v_readlane_b32 s74,v6,s72
v_readlane_b32 s75,v7,s72
qi 208,136,40
qg 172,4,27648
qa 194,8,194
qi 208,140,44
q0 6
buffer_load_dword v28,v27,s[32:35],s14 offen sc1
qa 195,8,195
qi 208,144,48
s_mul_i32 s50,s9,917504
qg 176,4,28672
qa 200,8,200
qi 208,148,52
q0 5
s_sub_u32 s50,s50,802816
qa 201,8,201
qi 208,152,56
s_add_u32 s12,s96,s50
qg 180,4,29696
qa 202,8,202
qi 208,156,60
s_add_u32 s13,s97,s50
qa 203,8,203
qb 224,192,193
qh 212,128,64
s_add_u32 s10,s95,131072
qg 184,4,30720
qb 225,194,195
qi 212,132,68
LHH10_1kf:
s_cbranch_execz LHR10kf
s_add_u32 s48,s31,2
qb 226,200,201
qb 227,202,203
qi 212,136,72
s_and_b32 s48,s48,3
qg 188,4,31744
qi 212,140,76
q3 26
s_barrier
v_add_u32 v4,s75,v2
qi 212,144,80
s_lshl_b32 s48,s48,17
qg 128,4,0
qi 212,148,84
s_add_u32 s11,s95,s48
buffer_store_dwordx4 v[224:227],v2,s[32:35],s80 offen offset:0 sc1
qa 196,9,196
qi 212,152,88
qg 132,4,1024
v_bfe_u32 v22,v1,2,2
qi 212,156,92
q0 8
v_lshrrev_b32 v23,4,v1
qa 197,9,197
qh 216,160,32
qg 136,4,2048
v_cmp_eq_u32 vcc,v22,v23
qi 216,164,36
q0 7
v_and_b32 v22,1,v1
qa 198,9,198
qi 216,168,40
qg 140,4,3072
v_lshlrev_b32 v22,4,v22
qi 216,172,44
q0 6
v_mov_b32 v23,0x3f80
qa 199,9,199
qi 216,176,48
qg 144,4,4096
v_lshlrev_b32 v22,v22,v23
qi 216,180,52
q0 5
v_cndmask_b32 v22,0,v22,vcc
qa 204,9,204
qi 216,184,56
qg 148,4,5120
v_and_b32 v23,2,v1
qi 216,188,60
v_cmp_eq_u32 vcc,0,v23
qa 205,9,205
qh 220,160,64
qg 152,4,6144
v_cndmask_b32 v18,0,v22,vcc
qi 220,164,68
v_cndmask_b32 v19,v22,0,vcc
qa 206,9,206
qi 220,168,72
qg 156,4,7168
v_mov_b32 v16,0
qi 220,172,76
v_mov_b32 v17,0
qa 207,9,207
qi 220,176,80
qg 160,4,8192
v_mov_b32 v20,0
qi 220,180,84
v_mov_b32 v21,0
qb 228,196,197
qb 229,198,199
qb 230,204,205
qb 231,206,207
buffer_store_dwordx4 v[228:231],v2,s[32:35],s80 offen offset:2048 sc1
qi 220,184,88
qg 164,4,9216
v_and_b32 v25,48,v1
qi 220,188,92
q0 8
v_add_u32 v25,s90,v25
qa 208,8,208
qh 192,128,32
s_mov_b32 m0,s74
qg 168,4,10240
v_and_b32 v24,15,v1
qi 192,132,36
q0 7
qc 2,0
v_lshlrev_b32 v22,7,v24
qi 192,136,40
qg 172,4,11264
v_mul_u32_u24 v24,896,v24
qi 192,140,44
q0 6
v_add3_u32 v24,v24,v25,s89
qa 209,8,209
qi 192,144,48
s_mul_i32 s48,s9,114688
qg 176,4,12288
qa 210,8,210
qi 192,148,52
q0 5
qc 2,1024
v_add_u32 v26,s48,v24
qi 192,152,56
s_lshr_b32 s55,s88,7
qg 180,4,13312
qa 211,8,211
qi 192,156,60
qa 216,8,216
qa 217,8,217
qh 196,128,64
qg 184,4,14336
qa 218,8,218
qi 196,132,68
qc 2,2048
qa 219,8,219
qi 196,136,72
qg 188,4,15360
qb 232,208,209
qi 196,140,76
qb 233,210,211
qb 234,216,217
qi 196,144,80
qg 128,4,16384
qb 235,218,219
qi 196,148,84
buffer_store_dwordx4 v[232:235],v2,s[32:35],s80 offen offset:1024 sc1
qa 212,9,212
qi 196,152,88
qg 132,4,17408
qa 213,9,213
qi 196,156,92
q0 8
qc 2,3072
s_add_u32 m0,s74,4096
qh 200,160,32
qg 136,4,18432
qa 214,9,214
qi 200,164,36
q0 7
qa 215,9,215
qa 220,9,220
qi 200,168,40
qg 140,4,19456
qa 221,9,221
qi 200,172,44
q0 6
qc 3,0
qa 222,9,222
qi 200,176,48
qg 144,4,20480
qa 223,9,223
qi 200,180,52
q0 5
qb 236,212,213
qb 237,214,215
qi 200,184,56
qg 148,4,21504
qb 238,220,221
qi 200,188,60
qc 3,1024
qb 239,222,223
qh 204,160,64
qg 152,4,22528
qi 204,164,68
buffer_store_dwordx4 v[236:239],v2,s[32:35],s80 offen offset:3072 sc1
s_add_u32 s80,s80,16384
qi 204,168,72
qg 156,4,23552
qi 204,172,76
qc 3,2048
qi 204,176,80
qg 160,4,24576
qa 192,8,192
qi 204,180,84
qa 193,8,193
qa 194,8,194
qi 204,184,88
qg 164,4,25600
qa 195,8,195
qi 204,188,92
q0 8
qc 3,3072
s_add_u32 s72,s72,1
qh 208,128,32
qg 168,4,26624
v_readlane_b32 s73,v5,s72
qi 208,132,36
q0 7
v_readlane_b32 s74,v6,s72
v_readlane_b32 s75,v7,s72
qi 208,136,40
qg 172,4,27648
qa 200,8,200
qi 208,140,44
q0 6
qa 201,8,201
qa 202,8,202
qi 208,144,48
qg 176,4,28672
qa 203,8,203
qi 208,148,52
q0 5
qb 224,192,193
qb 225,194,195
qi 208,152,56
qg 180,4,29696
qb 226,200,201
qi 208,156,60
qb 227,202,203
qh 212,128,64
qg 184,4,30720
qi 212,132,68
buffer_store_dwordx4 v[224:227],v2,s[32:35],s80 offen offset:0 sc1
qa 196,9,196
qi 212,136,72
qg 188,4,31744
qa 197,9,197
qi 212,140,76
q3 27
s_barrier
v_add_u32 v4,s75,v2
qi 212,144,80
qg 128,4,0
qa 198,9,198
qi 212,148,84
qa 199,9,199
qa 204,9,204
qi 212,152,88
qg 132,4,1024
qa 205,9,205
qi 212,156,92
q0 8
qa 206,9,206
qa 207,9,207
qh 216,160,32
qg 136,4,2048
qb 228,196,197
qi 216,164,36
q0 7
qb 229,198,199
qb 230,204,205
qi 216,168,40
qg 140,4,3072
qb 231,206,207
qi 216,172,44
q0 6
buffer_store_dwordx4 v[228:231],v2,s[32:35],s80 offen offset:2048 sc1
qi 216,176,48
qg 144,4,4096
qi 216,180,52
q0 5
qi 216,184,56
qg 148,4,5120
qi 216,188,60
qh 220,160,64
qg 152,4,6144
qi 220,164,68
qi 220,168,72
qg 156,4,7168
qi 220,172,76
qi 220,176,80
qg 160,4,8192
qa 208,8,208
qi 220,180,84
qa 209,8,209
qa 210,8,210
qi 220,184,88
qg 164,4,9216
qa 211,8,211
qi 220,188,92
q0 8
qa 216,8,216
qa 217,8,217
qh 192,128,32
s_mov_b32 m0,s74
qg 168,4,10240
qa 218,8,218
qi 192,132,36
q0 7
qc 2,0
qa 219,8,219
qi 192,136,40
qg 172,4,11264
qb 232,208,209
qi 192,140,44
q0 6
qb 233,210,211
qb 234,216,217
qi 192,144,48
qg 176,4,12288
qb 235,218,219
qi 192,148,52
q0 5
qc 2,1024
qi 192,152,56
qg 180,4,13312
qi 192,156,60
buffer_store_dwordx4 v[232:235],v2,s[32:35],s80 offen offset:1024 sc1
qa 212,9,212
qh 196,128,64
qg 184,4,14336
qa 213,9,213
qi 196,132,68
qc 2,2048
qa 214,9,214
qi 196,136,72
qg 188,4,15360
qa 215,9,215
qi 196,140,76
qa 220,9,220
qa 221,9,221
qi 196,144,80
qg 128,4,16384
qa 222,9,222
qi 196,148,84
qa 223,9,223
qb 236,212,213
qi 196,152,88
qg 132,4,17408
qb 237,214,215
qi 196,156,92
q0 8
qc 2,3072
s_add_u32 m0,s74,4096
qh 200,160,32
qg 136,4,18432
qb 238,220,221
qi 200,164,36
q0 7
qb 239,222,223
qi 200,168,40
qg 140,4,19456
qi 200,172,44
q0 6
qc 3,0
qi 200,176,48
qg 144,4,20480
qi 200,180,52
q0 5
buffer_store_dwordx4 v[236:239],v2,s[32:35],s80 offen offset:3072 sc1
s_add_u32 s80,s80,16384
qi 200,184,56
qg 148,4,21504
qi 200,188,60
qc 3,1024
qh 204,160,64
qg 152,4,22528
qi 204,164,68
qi 204,168,72
qg 156,4,23552
qi 204,172,76
qc 3,2048
qi 204,176,80
qg 160,4,24576
qa 192,8,192
qi 204,180,84
qa 193,8,193
qa 194,8,194
qi 204,184,88
qg 164,4,25600
qa 195,8,195
qi 204,188,92
q0 8
qc 3,3072
s_add_u32 s72,s72,1
qh 208,128,32
qg 168,4,26624
v_readlane_b32 s73,v5,s72
qi 208,132,36
q0 7
v_readlane_b32 s74,v6,s72
v_readlane_b32 s75,v7,s72
qi 208,136,40
qg 172,4,27648
qa 200,8,200
qi 208,140,44
q0 6
qa 201,8,201
qa 202,8,202
qi 208,144,48
qg 176,4,28672
qa 203,8,203
qi 208,148,52
q0 5
qb 224,192,193
qb 225,194,195
qi 208,152,56
qg 180,4,29696
qb 226,200,201
qi 208,156,60
qb 227,202,203
qh 212,128,64
qg 184,4,30720
qi 212,132,68
buffer_store_dwordx4 v[224:227],v2,s[32:35],s80 offen offset:0 sc1
qa 196,9,196
qi 212,136,72
qg 188,4,31744
qa 197,9,197
qi 212,140,76
q3 26
s_barrier
v_add_u32 v4,s75,v2
qi 212,144,80
qg 128,4,0
qa 198,9,198
qi 212,148,84
qa 199,9,199
qa 204,9,204
qi 212,152,88
qg 132,4,1024
qa 205,9,205
qi 212,156,92
q0 8
qa 206,9,206
qa 207,9,207
qh 216,160,32
qg 136,4,2048
qb 228,196,197
qi 216,164,36
q0 7
qb 229,198,199
qb 230,204,205
qi 216,168,40
qg 140,4,3072
qb 231,206,207
qi 216,172,44
q0 6
buffer_store_dwordx4 v[228:231],v2,s[32:35],s80 offen offset:2048 sc1
qi 216,176,48
qg 144,4,4096
qi 216,180,52
q0 5
qi 216,184,56
qg 148,4,5120
qi 216,188,60
qh 220,160,64
qg 152,4,6144
qi 220,164,68
qi 220,168,72
qg 156,4,7168
qi 220,172,76
qi 220,176,80
qg 160,4,8192
qa 208,8,208
qi 220,180,84
qa 209,8,209
qa 210,8,210
qi 220,184,88
qg 164,4,9216
qa 211,8,211
qi 220,188,92
q0 8
qa 216,8,216
qa 217,8,217
qh 192,128,32
s_mov_b32 m0,s74
qg 168,4,10240
qa 218,8,218
qi 192,132,36
q0 7
qc 2,0
qa 219,8,219
qi 192,136,40
qg 172,4,11264
qb 232,208,209
qi 192,140,44
q0 6
qb 233,210,211
qb 234,216,217
qi 192,144,48
qg 176,4,12288
qb 235,218,219
qi 192,148,52
q0 5
qc 2,1024
qi 192,152,56
qg 180,4,13312
qi 192,156,60
buffer_store_dwordx4 v[232:235],v2,s[32:35],s80 offen offset:1024 sc1
qa 212,9,212
qh 196,128,64
qg 184,4,14336
q3 29
qi 196,132,68
qc 2,2048
v_cmp_ne_u32 vcc,s81,v28
qi 196,136,72
qg 188,4,15360
s_cbranch_vccnz Lslow54kf_nb
Lback55kf_nb:
qi 196,140,76
qa 213,9,213
qa 214,9,214
qi 196,144,80
qg 128,4,16384
qa 215,9,215
qi 196,148,84
qa 220,9,220
qa 221,9,221
qi 196,152,88
qg 132,4,17408
qa 222,9,222
qi 196,156,92
q0 8
qc 2,3072
s_add_u32 m0,s74,4096
qh 200,160,32
qg 136,4,18432
qa 223,9,223
qi 200,164,36
q0 7
qb 236,212,213
qb 237,214,215
qi 200,168,40
qg 140,4,19456
qb 238,220,221
qi 200,172,44
q0 6
qc 3,0
qb 239,222,223
qi 200,176,48
qg 144,4,20480
qi 200,180,52
q0 5
buffer_store_dwordx4 v[236:239],v2,s[32:35],s80 offen offset:3072 sc1
s_add_u32 s80,s80,16384
qi 200,184,56
qg 148,4,21504
qi 200,188,60
qc 3,1024
qh 204,160,64
qg 152,4,22528
qi 204,164,68
qi 204,168,72
qg 156,4,23552
qi 204,172,76
qc 3,2048
qi 204,176,80
qg 160,4,24576
qa 192,8,192
qi 204,180,84
qa 193,8,193
qa 194,8,194
qi 204,184,88
qg 164,4,25600
qa 195,8,195
qi 204,188,92
q0 8
qc 3,3072
s_add_u32 s72,s72,1
qh 208,128,32
qg 168,4,26624
v_readlane_b32 s73,v5,s72
qi 208,132,36
q0 7
v_readlane_b32 s74,v6,s72
v_readlane_b32 s75,v7,s72
qi 208,136,40
qg 172,4,27648
qa 200,8,200
qi 208,140,44
q0 6
qa 201,8,201
qa 202,8,202
qi 208,144,48
qg 176,4,28672
qa 203,8,203
qi 208,148,52
q0 5
qb 224,192,193
qb 225,194,195
qi 208,152,56
qg 180,4,29696
qb 226,200,201
qi 208,156,60
qb 227,202,203
qh 212,128,64
qg 184,4,30720
qi 212,132,68
buffer_store_dwordx4 v[224:227],v2,s[32:35],s80 offen offset:0 sc1
qa 196,9,196
qi 212,136,72
qg 188,4,31744
qa 197,9,197
qi 212,140,76
q3 25
s_barrier
v_add_u32 v4,s75,v2
qi 212,144,80
qg 128,4,0
qa 198,9,198
qi 212,148,84
qa 199,9,199
qa 204,9,204
qi 212,152,88
qg 132,4,1024
qa 205,9,205
qi 212,156,92
q0 8
qa 206,9,206
qa 207,9,207
qh 216,160,32
qg 136,4,2048
qb 228,196,197
qi 216,164,36
q0 7
qb 229,198,199
qb 230,204,205
qi 216,168,40
LHH10_2kf:
s_cbranch_execz LHR10kf
qg 140,4,3072
qb 231,206,207
qi 216,172,44
q0 6
buffer_store_dwordx4 v[228:231],v2,s[32:35],s80 offen offset:2048 sc1
qi 216,176,48
qg 144,4,4096
qi 216,180,52
q0 5
qi 216,184,56
qg 148,4,5120
qi 216,188,60
qh 220,160,64
qg 152,4,6144
qi 220,164,68
qi 220,168,72
qg 156,4,7168
qi 220,172,76
buffer_load_dwordx4 v[180:183],v25,s[44:47],s55 offen
qi 220,176,80
qg 160,4,8192
qa 208,8,208
qi 220,180,84
qa 209,8,209
qa 210,8,210
qi 220,184,88
qg 164,4,9216
qa 211,8,211
qi 220,188,92
q0 8
qa 216,8,216
qa 217,8,217
qh 192,128,32
s_add_u32 s72,s72,1
qg 168,4,10240
v_readlane_b32 s73,v5,s72
qi 192,132,36
q0 7
v_readlane_b32 s74,v6,s72
v_readlane_b32 s75,v7,s72
qi 192,136,40
qg 172,4,11264
qa 218,8,218
qi 192,140,44
q0 6
qa 219,8,219
qb 232,208,209
qi 192,144,48
qg 176,4,12288
qb 233,210,211
qi 192,148,52
q0 5
qb 234,216,217
qb 235,218,219
qi 192,152,56
qg 180,4,13312
qi 192,156,60
buffer_store_dwordx4 v[232:235],v2,s[32:35],s80 offen offset:1024 sc1
qa 212,9,212
qh 196,128,64
qg 184,4,14336
)"
R"(qa 213,9,213
qi 196,132,68
qa 214,9,214
qa 215,9,215
qi 196,136,72
qg 188,4,15360
qa 220,9,220
qi 196,140,76
qa 221,9,221
qa 222,9,222
qi 196,144,80
qg 128,4,16384
qa 223,9,223
qi 196,148,84
qb 236,212,213
qb 237,214,215
qi 196,152,88
qg 132,4,17408
qb 238,220,221
qi 196,156,92
q0 8
qb 239,222,223
qh 200,160,32
qg 136,4,18432
qi 200,164,36
q0 7
buffer_load_dwordx4 v[100:103],v24,s[36:39],s55 offen
qi 200,168,40
qg 140,4,19456
qi 200,172,44
q0 6
buffer_store_dwordx4 v[236:239],v2,s[32:35],s80 offen offset:3072 sc1
s_add_u32 s80,s80,16384
qi 200,176,48
qg 144,4,20480
qi 200,180,52
q0 5
qi 200,184,56
qg 148,4,21504
qi 200,188,60
qh 204,160,64
qg 152,4,22528
qi 204,164,68
qi 204,168,72
qg 156,4,23552
qi 204,172,76
qi 204,176,80
qg 160,4,24576
qa 192,8,192
qi 204,180,84
qa 193,8,193
qa 194,8,194
qi 204,184,88
qg 164,4,25600
qa 195,8,195
qi 204,188,92
q0 8
qa 200,8,200
qa 201,8,201
qh 208,128,32
qg 168,4,26624
qa 202,8,202
qi 208,132,36
q0 7
qa 203,8,203
qb 224,192,193
qi 208,136,40
qg 172,4,27648
v_add_u32 v3,s88,v2
qi 208,140,44
q0 6
qb 225,194,195
qb 226,200,201
qi 208,144,48
qg 176,4,28672
qb 227,202,203
qi 208,148,52
q0 5
buffer_store_dwordx4 v[224:227],v2,s[32:35],s80 offen offset:0 sc1
qa 196,9,196
qi 208,152,56
qg 180,4,29696
qa 197,9,197
qi 208,156,60
buffer_load_dwordx4 v[176:179],v25,s[44:47],0 offen
qa 198,9,198
qh 212,128,64
qg 184,4,30720
qa 199,9,199
qi 212,132,68
qa 204,9,204
qa 205,9,205
qi 212,136,72
qg 188,4,31744
qa 206,9,206
qi 212,140,76
q3 20
s_barrier
v_add_u32 v4,s75,v2
qi 212,144,80
qg 128,4,0
qa 207,9,207
qi 212,148,84
qb 228,196,197
qb 229,198,199
qi 212,152,88
qg 132,4,1024
qb 230,204,205
qi 212,156,92
q0 8
qb 231,206,207
qh 216,160,32
qg 136,4,2048
qi 216,164,36
q0 7
buffer_store_dwordx4 v[228:231],v2,s[32:35],s80 offen offset:2048 sc1
qi 216,168,40
qg 140,4,3072
qi 216,172,44
q0 6
qi 216,176,48
qg 144,4,4096
qi 216,180,52
q0 5
qi 216,184,56
qg 148,4,5120
qi 216,188,60
qh 220,160,64
qg 152,4,6144
qi 220,164,68
qi 220,168,72
qg 156,4,7168
qi 220,172,76
qi 220,176,80
qg 160,4,8192
qa 208,8,208
qi 220,180,84
buffer_load_dwordx4 v[96:99],v24,s[36:39],0 offen
qa 209,8,209
qi 220,184,88
qg 164,4,9216
qa 210,8,210
qi 220,188,92
q0 8
qa 211,8,211
qa 216,8,216
qh 192,128,32
s_add_u32 s72,s72,1
qg 168,4,10240
v_readlane_b32 s73,v5,s72
qi 192,132,36
q0 7
v_readlane_b32 s74,v6,s72
v_readlane_b32 s75,v7,s72
qi 192,136,40
qg 172,4,11264
qa 217,8,217
qi 192,140,44
q0 6
qa 218,8,218
qa 219,8,219
qi 192,144,48
qg 176,4,12288
qb 232,208,209
qi 192,148,52
q0 5
qb 233,210,211
qb 234,216,217
qi 192,152,56
qg 180,4,13312
qb 235,218,219
qi 192,156,60
buffer_store_dwordx4 v[232:235],v2,s[32:35],s80 offen offset:1024 sc1
qa 212,9,212
qh 196,128,64
qg 184,4,14336
qa 213,9,213
qi 196,132,68
qa 214,9,214
qa 215,9,215
qi 196,136,72
qg 188,4,15360
qa 220,9,220
qi 196,140,76
qa 221,9,221
qa 222,9,222
qi 196,144,80
qg 128,4,16384
qa 223,9,223
qi 196,148,84
qb 236,212,213
qb 237,214,215
qi 196,152,88
qg 132,4,17408
qb 238,220,221
qi 196,156,92
q0 8
qb 239,222,223
qh 200,160,32
qg 136,4,18432
qi 200,164,36
q0 7
buffer_store_dwordx4 v[236:239],v2,s[32:35],s80 offen offset:3072 sc1
s_add_u32 s80,s80,16384
qi 200,168,40
qg 140,4,19456
qi 200,172,44
q0 6
buffer_load_dwordx4 v[184:187],v26,s[36:39],0 offen
qi 200,176,48
qg 144,4,20480
qi 200,180,52
q0 5
qi 200,184,56
qg 148,4,21504
qi 200,188,60
qh 204,160,64
qg 152,4,22528
qi 204,164,68
q3 7
buffer_store_dword v120,v121,s[32:35],s4 offen sc1
qi 204,168,72
qg 156,4,23552
qi 204,172,76
qi 204,176,80
qg 160,4,24576
qa 192,8,192
qi 204,180,84
qa 193,8,193
qa 194,8,194
qi 204,184,88
qg 164,4,25600
qa 195,8,195
qi 204,188,92
q0 8
qa 200,8,200
qa 201,8,201
qh 208,128,32
qg 168,4,26624
qa 202,8,202
qi 208,132,36
q0 7
qa 203,8,203
qb 224,192,193
qi 208,136,40
qg 172,4,27648
qb 225,194,195
qi 208,140,44
q0 6
qb 226,200,201
qb 227,202,203
qi 208,144,48
qg 176,4,28672
qi 208,148,52
q0 5
buffer_store_dwordx4 v[224:227],v2,s[32:35],s80 offen offset:0 sc1
qa 196,9,196
qi 208,152,56
qg 180,4,29696
qa 197,9,197
qi 208,156,60
qa 198,9,198
qa 199,9,199
qh 212,128,64
qg 184,4,30720
qa 204,9,204
qi 212,132,68
qa 205,9,205
qa 206,9,206
qi 212,136,72
qg 188,4,31744
qa 207,9,207
qi 212,140,76
q3 15
s_barrier
v_add_u32 v4,s75,v2
qi 212,144,80
qg 128,4,0
qb 228,196,197
qi 212,148,84
qb 229,198,199
qb 230,204,205
qi 212,152,88
qg 132,4,1024
qb 231,206,207
qi 212,156,92
q0 8
buffer_store_dwordx4 v[228:231],v2,s[32:35],s80 offen offset:2048 sc1
qh 216,160,32
qg 136,4,2048
qi 216,164,36
q0 7
qi 216,168,40
qg 140,4,3072
qi 216,172,44
q0 6
qi 216,176,48
qg 144,4,4096
qi 216,180,52
q0 5
qi 216,184,56
qg 148,4,5120
qi 216,188,60
qh 220,160,64
qg 152,4,6144
qi 220,164,68
qi 220,168,72
qg 156,4,7168
qi 220,172,76
qi 220,176,80
qg 160,4,8192
qa 208,8,208
qi 220,180,84
qa 209,8,209
qa 210,8,210
qi 220,184,88
qg 164,4,9216
qa 211,8,211
qi 220,188,92
q0 8
qa 216,8,216
qa 217,8,217
qh 192,128,32
s_add_u32 s72,s72,1
qg 168,4,10240
v_readlane_b32 s73,v5,s72
qi 192,132,36
q0 7
v_readlane_b32 s74,v6,s72
v_readlane_b32 s75,v7,s72
qi 192,136,40
qg 172,4,11264
qa 218,8,218
qi 192,140,44
q0 6
qa 219,8,219
qb 232,208,209
qi 192,144,48
qg 176,4,12288
qb 233,210,211
qi 192,148,52
q0 5
qb 234,216,217
qb 235,218,219
qi 192,152,56
qg 180,4,13312
qi 192,156,60
buffer_store_dwordx4 v[232:235],v2,s[32:35],s80 offen offset:1024 sc1
qa 212,9,212
qh 196,128,64
qg 184,4,14336
qa 213,9,213
qi 196,132,68
qa 214,9,214
qa 215,9,215
qi 196,136,72
qg 188,4,15360
qa 220,9,220
qi 196,140,76
qa 221,9,221
qa 222,9,222
qi 196,144,80
qg 128,4,16384
qa 223,9,223
qi 196,148,84
qb 236,212,213
qb 237,214,215
qi 196,152,88
qg 132,4,17408
qb 238,220,221
qi 196,156,92
q0 8
qb 239,222,223
qh 200,160,32
qg 136,4,18432
qi 200,164,36
q0 7
buffer_store_dwordx4 v[236:239],v2,s[32:35],s80 offen offset:3072 sc1
s_add_u32 s80,s80,16384
qi 200,168,40
qg 140,4,19456
qi 200,172,44
q0 6
qi 200,176,48
qg 144,4,20480
qi 200,180,52
q0 5
buffer_load_dword v23,v22,s[32:35],s10 offen sc1
qi 200,184,56
qg 148,4,21504
qi 200,188,60
qh 204,160,64
qg 152,4,22528
qi 204,164,68
qi 204,168,72
qg 156,4,23552
qi 204,172,76
qi 204,176,80
qg 160,4,24576
qa 192,8,192
qi 204,180,84
qa 193,8,193
qa 194,8,194
qi 204,184,88
qg 164,4,25600
qa 195,8,195
qi 204,188,92
q0 8
qa 200,8,200
qa 201,8,201
qh 208,128,32
qg 168,4,26624
qa 202,8,202
qi 208,132,36
q0 7
qa 203,8,203
qb 224,192,193
qi 208,136,40
qg 172,4,27648
qb 225,194,195
qi 208,140,44
q0 6
qb 226,200,201
qb 227,202,203
qi 208,144,48
qg 176,4,28672
qi 208,148,52
q0 5
buffer_store_dwordx4 v[224:227],v2,s[32:35],s80 offen offset:0 sc1
qa 196,9,196
qi 208,152,56
qg 180,4,29696
qa 197,9,197
qi 208,156,60
qa 198,9,198
qa 199,9,199
qh 212,128,64
qg 184,4,30720
qa 204,9,204
qi 212,132,68
qa 205,9,205
qa 206,9,206
qi 212,136,72
qg 188,4,31744
qa 207,9,207
qi 212,140,76
qb 228,196,197
qb 229,198,199
qi 212,144,80
qb 230,204,205
qb 231,206,207
qi 212,148,84
buffer_store_dwordx4 v[228:231],v2,s[32:35],s80 offen offset:2048 sc1
qi 212,152,88
qi 212,156,92
q0 6
qh 216,160,32
qi 216,164,36
q0 4
qi 216,168,40
qi 216,172,44
q0 2
qi 216,176,48
qi 216,180,52
q0 0
qi 216,184,56
qi 216,188,60
qh 220,160,64
qi 220,164,68
qi 220,168,72
qi 220,172,76
qi 220,176,80
qa 208,8,208
qa 209,8,209
qi 220,180,84
qa 210,8,210
qa 211,8,211
qi 220,184,88
qa 216,8,216
qa 217,8,217
qi 220,188,92
qa 218,8,218
qa 219,8,219
qb 232,208,209
qb 233,210,211
qb 234,216,217
qb 235,218,219
buffer_store_dwordx4 v[232:235],v2,s[32:35],s80 offen offset:1024 sc1
s_nop 7
qa 212,9,212
qa 213,9,213
qa 214,9,214
qa 215,9,215
qa 220,9,220
qa 221,9,221
qa 222,9,222
qa 223,9,223
qb 236,212,213
qb 237,214,215
qb 238,220,221
qb 239,222,223
buffer_store_dwordx4 v[236:239],v2,s[32:35],s80 offen offset:3072 sc1
s_add_u32 s80,s80,16384
q3 11
buffer_store_dword v120,v121,s[32:35],s5 offen sc1
q3 6
buffer_store_dword v120,v121,s[32:35],s8 offen sc1
q3 12
qp 104,18,192
qo 104,18,196
qo 104,18,200
qo 104,18,204
qo 104,18,208
qo 104,18,212
qo 104,18,216
qo 104,18,220
qq 128,18,176
qp 108,16,192
qo 108,16,196
qo 108,16,200
qo 108,16,204
qo 108,16,208
qo 108,16,212
qo 108,16,216
qo 108,16,220
qq 132,16,176
qp 112,18,224
qo 112,18,228
qo 112,18,232
qo 112,18,236
qo 112,18,240
qo 112,18,244
qo 112,18,248
qo 112,18,252
qq 136,18,180
qp 116,16,224
qo 116,16,228
LHH10_3kf:
s_cbranch_execz LHR10kf
qo 116,16,232
qo 116,16,236
qo 116,16,240
qo 116,16,244
qo 116,16,248
qo 116,16,252
qq 140,16,180
qq 144,18,96
qq 148,16,96
qq 152,18,100
qq 156,16,100
s_nop 5
q3 2
buffer_store_dword v120,v121,s[32:35],s93 offen sc1
buffer_load_dwordx4 a[128:131],v2,s[32:35],s12 offen sc1
s_add_u32 s12,s12,114688
buffer_load_dwordx4 a[132:135],v2,s[32:35],s12 offen sc1
s_add_u32 s12,s12,114688
buffer_load_dwordx4 a[136:139],v2,s[32:35],s12 offen sc1
s_add_u32 s12,s12,114688
buffer_load_dwordx4 a[140:143],v2,s[32:35],s12 offen sc1
s_add_u32 s12,s12,114688
buffer_load_dwordx4 a[144:147],v2,s[32:35],s12 offen sc1
s_add_u32 s12,s12,114688
buffer_load_dwordx4 a[148:151],v2,s[32:35],s12 offen sc1
s_add_u32 s12,s12,114688
buffer_load_dwordx4 a[152:155],v2,s[32:35],s12 offen sc1
s_add_u32 s12,s12,114688
buffer_load_dwordx4 a[156:159],v2,s[32:35],s12 offen sc1
v_cmp_ne_u32 vcc,s81,v23
s_nop 0
s_cbranch_vccnz Lslow56kf_nb
Lback57kf_nb:
s_mov_b32 s48,s98
buffer_load_dwordx4 a[0:3],v2,s[32:35],s48 offen sc1
s_add_u32 s48,s48,114688
buffer_load_dwordx4 a[4:7],v2,s[32:35],s48 offen sc1
s_add_u32 s48,s48,114688
buffer_load_dwordx4 a[8:11],v2,s[32:35],s48 offen sc1
s_add_u32 s48,s48,114688
buffer_load_dwordx4 a[12:15],v2,s[32:35],s48 offen sc1
s_add_u32 s48,s48,114688
buffer_load_dwordx4 a[16:19],v2,s[32:35],s48 offen sc1
s_add_u32 s48,s48,114688
buffer_load_dwordx4 a[20:23],v2,s[32:35],s48 offen sc1
s_add_u32 s48,s48,114688
buffer_load_dwordx4 a[24:27],v2,s[32:35],s48 offen sc1
s_add_u32 s48,s48,114688
buffer_load_dwordx4 a[28:31],v2,s[32:35],s48 offen sc1
s_add_u32 s48,s48,114688
buffer_load_dwordx4 a[32:35],v2,s[32:35],s48 offen sc1
s_add_u32 s48,s48,114688
buffer_load_dwordx4 a[36:39],v2,s[32:35],s48 offen sc1
s_add_u32 s48,s48,114688
buffer_load_dwordx4 a[40:43],v2,s[32:35],s48 offen sc1
s_add_u32 s48,s48,114688
buffer_load_dwordx4 a[44:47],v2,s[32:35],s48 offen sc1
s_add_u32 s48,s48,114688
buffer_load_dwordx4 a[48:51],v2,s[32:35],s48 offen sc1
s_add_u32 s48,s48,114688
buffer_load_dwordx4 a[52:55],v2,s[32:35],s48 offen sc1
s_add_u32 s48,s48,114688
buffer_load_dwordx4 a[56:59],v2,s[32:35],s48 offen sc1
s_add_u32 s48,s48,114688
buffer_load_dwordx4 a[60:63],v2,s[32:35],s48 offen sc1
v_fmamk_f32 v104,v104,0x42f5fdf0,v128
v_fmamk_f32 v105,v105,0x42f5fdf0,v129
v_fmamk_f32 v106,v106,0x42f5fdf0,v130
v_fmamk_f32 v107,v107,0x42f5fdf0,v131
v_fmamk_f32 v108,v108,0x42f5fdf0,v132
v_fmamk_f32 v109,v109,0x42f5fdf0,v133
v_fmamk_f32 v110,v110,0x42f5fdf0,v134
v_fmamk_f32 v111,v111,0x42f5fdf0,v135
v_fmamk_f32 v112,v112,0x42f5fdf0,v136
v_fmamk_f32 v113,v113,0x42f5fdf0,v137
v_fmamk_f32 v114,v114,0x42f5fdf0,v138
v_fmamk_f32 v115,v115,0x42f5fdf0,v139
v_fmamk_f32 v116,v116,0x42f5fdf0,v140
v_fmamk_f32 v117,v117,0x42f5fdf0,v141
v_fmamk_f32 v118,v118,0x42f5fdf0,v142
v_fmamk_f32 v119,v119,0x42f5fdf0,v143
v_mul_f32 v128,0xbfb8aa3b,v104
v_mul_f32 v129,0xbfb8aa3b,v105
v_mul_f32 v130,0xbfb8aa3b,v106
v_mul_f32 v131,0xbfb8aa3b,v107
v_mul_f32 v132,0xbfb8aa3b,v108
v_mul_f32 v133,0xbfb8aa3b,v109
v_mul_f32 v134,0xbfb8aa3b,v110
v_mul_f32 v135,0xbfb8aa3b,v111
v_mul_f32 v136,0xbfb8aa3b,v112
v_mul_f32 v137,0xbfb8aa3b,v113
v_mul_f32 v138,0xbfb8aa3b,v114
v_mul_f32 v139,0xbfb8aa3b,v115
v_mul_f32 v140,0xbfb8aa3b,v116
v_mul_f32 v141,0xbfb8aa3b,v117
v_mul_f32 v142,0xbfb8aa3b,v118
v_mul_f32 v143,0xbfb8aa3b,v119
qm 128,128
qm 129,129
qm 130,130
qm 131,131
qm 132,132
qm 133,133
qm 134,134
qm 135,135
qm 136,136
qm 137,137
qm 138,138
qm 139,139
qm 140,140
qm 141,141
qm 142,142
qm 143,143
v_add_f32 v128,1.0,v128
v_add_f32 v129,1.0,v129
v_add_f32 v130,1.0,v130
v_add_f32 v131,1.0,v131
v_add_f32 v132,1.0,v132
v_add_f32 v133,1.0,v133
v_add_f32 v134,1.0,v134
v_add_f32 v135,1.0,v135
v_add_f32 v136,1.0,v136
v_add_f32 v137,1.0,v137
v_add_f32 v138,1.0,v138
v_add_f32 v139,1.0,v139
v_add_f32 v140,1.0,v140
v_add_f32 v141,1.0,v141
v_add_f32 v142,1.0,v142
v_add_f32 v143,1.0,v143
v_rcp_f32 v128,v128
v_rcp_f32 v129,v129
v_rcp_f32 v130,v130
v_rcp_f32 v131,v131
v_rcp_f32 v132,v132
v_rcp_f32 v133,v133
v_rcp_f32 v134,v134
v_rcp_f32 v135,v135
v_rcp_f32 v136,v136
v_rcp_f32 v137,v137
v_rcp_f32 v138,v138
v_rcp_f32 v139,v139
v_rcp_f32 v140,v140
v_rcp_f32 v141,v141
v_rcp_f32 v142,v142
v_rcp_f32 v143,v143
qa 104,104,128
qa 105,105,129
qa 106,106,130
qa 107,107,131
qa 108,108,132
qa 109,109,133
qa 110,110,134
qa 111,111,135
qa 112,112,136
qa 113,113,137
qa 114,114,138
qa 115,115,139
qa 116,116,140
qa 117,117,141
qa 118,118,142
qa 119,119,143
s_cmp_eq_u32 s31,3
s_cbranch_scc1 Lw358kf
s_mov_b32 s85,200000
Lpoll60kf:
buffer_load_dword v23,v22,s[32:35],s11 offen sc1
q3 0
v_cmp_ne_u32 vcc,s81,v23
s_nop 0
s_cbranch_vccz Lpdone61kf
s_sleep 2
s_sub_u32 s85,s85,1
s_cmp_lg_u32 s85,0
s_cbranch_scc1 Lpoll60kf
Lpdone61kf:
s_mov_b32 s48,s98
buffer_load_dwordx4 a[64:67],v3,s[32:35],s48 offen sc1
s_add_u32 s48,s48,114688
buffer_load_dwordx4 a[68:71],v3,s[32:35],s48 offen sc1
s_add_u32 s48,s48,114688
buffer_load_dwordx4 a[72:75],v3,s[32:35],s48 offen sc1
s_add_u32 s48,s48,114688
buffer_load_dwordx4 a[76:79],v3,s[32:35],s48 offen sc1
s_add_u32 s48,s48,114688
buffer_load_dwordx4 a[80:83],v3,s[32:35],s48 offen sc1
s_add_u32 s48,s48,114688
buffer_load_dwordx4 a[84:87],v3,s[32:35],s48 offen sc1
s_add_u32 s48,s48,114688
buffer_load_dwordx4 a[88:91],v3,s[32:35],s48 offen sc1
s_add_u32 s48,s48,114688
buffer_load_dwordx4 a[92:95],v3,s[32:35],s48 offen sc1
s_add_u32 s48,s48,114688
buffer_load_dwordx4 a[96:99],v3,s[32:35],s48 offen sc1
s_add_u32 s48,s48,114688
buffer_load_dwordx4 a[100:103],v3,s[32:35],s48 offen sc1
s_add_u32 s48,s48,114688
buffer_load_dwordx4 a[104:107],v3,s[32:35],s48 offen sc1
s_add_u32 s48,s48,114688
buffer_load_dwordx4 a[108:111],v3,s[32:35],s48 offen sc1
s_add_u32 s48,s48,114688
buffer_load_dwordx4 a[112:115],v3,s[32:35],s48 offen sc1
s_add_u32 s48,s48,114688
buffer_load_dwordx4 a[116:119],v3,s[32:35],s48 offen sc1
s_add_u32 s48,s48,114688
buffer_load_dwordx4 a[120:123],v3,s[32:35],s48 offen sc1
s_add_u32 s48,s48,114688
buffer_load_dwordx4 a[124:127],v3,s[32:35],s48 offen sc1
qp 160,18,0
qp 164,16,0
qo 160,18,4
qo 164,16,4
qo 160,18,8
qo 164,16,8
qo 160,18,12
qo 164,16,12
qo 160,18,16
qo 164,16,16
qo 160,18,20
qo 164,16,20
qo 160,18,24
qo 164,16,24
qo 160,18,28
qo 164,16,28
qo 160,18,32
qo 164,16,32
qo 160,18,36
qo 164,16,36
qo 160,18,40
qo 164,16,40
qo 160,18,44
qo 164,16,44
qo 160,18,48
qo 164,16,48
qo 160,18,52
qo 164,16,52
qo 160,18,56
qo 164,16,56
qo 160,18,60
qo 164,16,60
s_nop 7
v_fma_f32 v160,v160,v104,v144
v_fma_f32 v161,v161,v105,v145
v_fma_f32 v162,v162,v106,v146
v_fma_f32 v163,v163,v107,v147
v_fma_f32 v164,v164,v108,v148
v_fma_f32 v165,v165,v109,v149
v_fma_f32 v166,v166,v110,v150
v_fma_f32 v167,v167,v111,v151
qb 160,160,161
qb 161,162,163
qb 162,164,165
qb 163,166,167
buffer_store_dwordx4 v[160:163],v24,s[40:43],0 offen
q3 16
qp 168,18,64
qp 172,16,64
q3 15
qo 168,18,68
qo 172,16,68
q3 14
qo 168,18,72
qo 172,16,72
q3 13
qo 168,18,76
qo 172,16,76
q3 12
qo 168,18,80
qo 172,16,80
q3 11
qo 168,18,84
qo 172,16,84
q3 10
qo 168,18,88
qo 172,16,88
q3 9
qo 168,18,92
qo 172,16,92
q3 8
qo 168,18,96
qo 172,16,96
q3 7
qo 168,18,100
qo 172,16,100
q3 6
qo 168,18,104
qo 172,16,104
q3 5
qo 168,18,108
qo 172,16,108
q3 4
qo 168,18,112
qo 172,16,112
q3 3
qo 168,18,116
qo 172,16,116
q3 2
qo 168,18,120
qo 172,16,120
q3 1
qo 168,18,124
qo 172,16,124
s_nop 7
v_fma_f32 v168,v168,v112,v152
v_fma_f32 v169,v169,v113,v153
v_fma_f32 v170,v170,v114,v154
v_fma_f32 v171,v171,v115,v155
v_fma_f32 v172,v172,v116,v156
v_fma_f32 v173,v173,v117,v157
v_fma_f32 v174,v174,v118,v158
v_fma_f32 v175,v175,v119,v159
qb 168,168,169
qb 169,170,171
qb 170,172,173
qb 171,174,175
buffer_store_dwordx4 v[168:171],v24,s[40:43],s55 offen
s_branch Le59kf
Lw358kf:
q3 0
qp 160,18,0
qp 164,16,0
qo 160,18,4
qo 164,16,4
qo 160,18,8
qo 164,16,8
qo 160,18,12
qo 164,16,12
qo 160,18,16
qo 164,16,16
qo 160,18,20
qo 164,16,20
qo 160,18,24
qo 164,16,24
qo 160,18,28
qo 164,16,28
qo 160,18,32
qo 164,16,32
qo 160,18,36
qo 164,16,36
qo 160,18,40
qo 164,16,40
qo 160,18,44
qo 164,16,44
qo 160,18,48
qo 164,16,48
qo 160,18,52
qo 164,16,52
qo 160,18,56
qo 164,16,56
qo 160,18,60
qo 164,16,60
s_nop 7
v_fma_f32 v160,v160,v104,v144
v_fma_f32 v161,v161,v105,v145
v_fma_f32 v162,v162,v106,v146
v_fma_f32 v163,v163,v107,v147
v_fma_f32 v164,v164,v108,v148
v_fma_f32 v165,v165,v109,v149
v_fma_f32 v166,v166,v110,v150
v_fma_f32 v167,v167,v111,v151
qb 160,160,161
qb 161,162,163
qb 162,164,165
qb 163,166,167
buffer_store_dwordx4 v[160:163],v24,s[40:43],0 offen
Le59kf:
s_lshl_b32 s48,s9,13
s_add_u32 s10,s10,s48
s_add_u32 s11,s11,s48
s_mul_i32 s48,s9,1835008
s_add_u32 s98,s98,s48
buffer_load_dword v23,v22,s[32:35],s10 offen sc1
qp 104,18,128
qo 104,18,132
qo 104,18,136
qo 104,18,140
qo 104,18,144
qo 104,18,148
qo 104,18,152
qo 104,18,156
qq 128,18,176
qp 108,16,128
qo 108,16,132
qo 108,16,136
qo 108,16,140
qo 108,16,144
qo 108,16,148
qo 108,16,152
qo 108,16,156
qq 132,16,176
qq 144,18,184
qq 148,16,184
s_nop 5
v_fmamk_f32 v104,v104,0x42f5fdf0,v128
v_fmamk_f32 v105,v105,0x42f5fdf0,v129
v_fmamk_f32 v106,v106,0x42f5fdf0,v130
v_fmamk_f32 v107,v107,0x42f5fdf0,v131
v_fmamk_f32 v108,v108,0x42f5fdf0,v132
v_fmamk_f32 v109,v109,0x42f5fdf0,v133
v_fmamk_f32 v110,v110,0x42f5fdf0,v134
v_fmamk_f32 v111,v111,0x42f5fdf0,v135
v_mul_f32 v128,0xbfb8aa3b,v104
v_mul_f32 v129,0xbfb8aa3b,v105
v_mul_f32 v130,0xbfb8aa3b,v106
v_mul_f32 v131,0xbfb8aa3b,v107
v_mul_f32 v132,0xbfb8aa3b,v108
v_mul_f32 v133,0xbfb8aa3b,v109
v_mul_f32 v134,0xbfb8aa3b,v110
v_mul_f32 v135,0xbfb8aa3b,v111
qm 128,128
qm 129,129
qm 130,130
qm 131,131
qm 132,132
qm 133,133
qm 134,134
qm 135,135
v_add_f32 v128,1.0,v128
v_add_f32 v129,1.0,v129
v_add_f32 v130,1.0,v130
v_add_f32 v131,1.0,v131
v_add_f32 v132,1.0,v132
v_add_f32 v133,1.0,v133
v_add_f32 v134,1.0,v134
v_add_f32 v135,1.0,v135
v_rcp_f32 v128,v128
v_rcp_f32 v129,v129
v_rcp_f32 v130,v130
v_rcp_f32 v131,v131
v_rcp_f32 v132,v132
v_rcp_f32 v133,v133
v_rcp_f32 v134,v134
v_rcp_f32 v135,v135
qa 104,104,128
qa 105,105,129
qa 106,106,130
qa 107,107,131
qa 108,108,132
qa 109,109,133
qa 110,110,134
qa 111,111,135
q3 0
v_cmp_ne_u32 vcc,s81,v23
s_nop 0
s_cbranch_vccnz Lslow62kf_nb
Lback63kf_nb:
s_mov_b32 s48,s98
buffer_load_dwordx4 a[0:3],v2,s[32:35],s48 offen sc1
s_add_u32 s48,s48,114688
buffer_load_dwordx4 a[4:7],v2,s[32:35],s48 offen sc1
s_add_u32 s48,s48,114688
buffer_load_dwordx4 a[8:11],v2,s[32:35],s48 offen sc1
s_add_u32 s48,s48,114688
buffer_load_dwordx4 a[12:15],v2,s[32:35],s48 offen sc1
s_add_u32 s48,s48,114688
buffer_load_dwordx4 a[16:19],v2,s[32:35],s48 offen sc1
s_add_u32 s48,s48,114688
buffer_load_dwordx4 a[20:23],v2,s[32:35],s48 offen sc1
s_add_u32 s48,s48,114688
buffer_load_dwordx4 a[24:27],v2,s[32:35],s48 offen sc1
s_add_u32 s48,s48,114688
buffer_load_dwordx4 a[28:31],v2,s[32:35],s48 offen sc1
s_add_u32 s48,s48,114688
buffer_load_dwordx4 a[32:35],v2,s[32:35],s48 offen sc1
s_add_u32 s48,s48,114688
buffer_load_dwordx4 a[36:39],v2,s[32:35],s48 offen sc1
s_add_u32 s48,s48,114688
buffer_load_dwordx4 a[40:43],v2,s[32:35],s48 offen sc1
s_add_u32 s48,s48,114688
buffer_load_dwordx4 a[44:47],v2,s[32:35],s48 offen sc1
s_add_u32 s48,s48,114688
buffer_load_dwordx4 a[48:51],v2,s[32:35],s48 offen sc1
s_add_u32 s48,s48,114688
buffer_load_dwordx4 a[52:55],v2,s[32:35],s48 offen sc1
s_add_u32 s48,s48,114688
buffer_load_dwordx4 a[56:59],v2,s[32:35],s48 offen sc1
s_add_u32 s48,s48,114688
buffer_load_dwordx4 a[60:63],v2,s[32:35],s48 offen sc1
q3 15
qp 160,18,0
qp 164,16,0
q3 14
qo 160,18,4
qo 164,16,4
q3 13
qo 160,18,8
qo 164,16,8
q3 12
qo 160,18,12
qo 164,16,12
q3 11
qo 160,18,16
qo 164,16,16
q3 10
qo 160,18,20
qo 164,16,20
q3 9
qo 160,18,24
qo 164,16,24
q3 8
qo 160,18,28
qo 164,16,28
q3 7
qo 160,18,32
qo 164,16,32
q3 6
qo 160,18,36
qo 164,16,36
q3 5
qo 160,18,40
qo 164,16,40
q3 4
qo 160,18,44
qo 164,16,44
q3 3
qo 160,18,48
qo 164,16,48
q3 2
qo 160,18,52
qo 164,16,52
q3 1
qo 160,18,56
qo 164,16,56
q3 0
qo 160,18,60
qo 164,16,60
s_nop 7
v_fma_f32 v160,v160,v104,v144
v_fma_f32 v161,v161,v105,v145
v_fma_f32 v162,v162,v106,v146
v_fma_f32 v163,v163,v107,v147
v_fma_f32 v164,v164,v108,v148
v_fma_f32 v165,v165,v109,v149
v_fma_f32 v166,v166,v110,v150
v_fma_f32 v167,v167,v111,v151
qb 160,160,161
qb 161,162,163
qb 162,164,165
qb 163,166,167
buffer_store_dwordx4 v[160:163],v26,s[40:43],0 offen
Lfin3kf:
s_nop 1
s_waitcnt vmcnt(0) lgkmcnt(0)
s_branch LENDkf
Lslow37kf_nb:
s_mov_b32 s85,200000
Lsl64kf:
s_sleep 2
buffer_load_dwordx4 v[80:83],v2,s[32:35],s93 offen sc1
q3 0
v_xor_b32 v80,s81,v80
v_xor_b32 v81,s81,v81
v_xor_b32 v82,s81,v82
v_xor_b32 v83,s81,v83
v_or3_b32 v80,v80,v81,v82
v_or_b32 v80,v80,v83
v_cmp_ne_u32 vcc,0,v80
s_nop 0
s_cbranch_vccz Lback38kf_nb
s_sub_u32 s85,s85,1
s_cmp_lg_u32 s85,0
s_cbranch_scc1 Lsl64kf
s_branch Lback38kf_nb
Lslow39kf_nb:
s_mov_b32 s85,200000
Lsl65kf:
s_sleep 2
buffer_load_dword v18,v19,s[32:35],s92 offen sc1
q3 0
v_cmp_ne_u32 vcc,s81,v18
s_nop 0
s_cbranch_vccz Lback40kf_nb
s_sub_u32 s85,s85,1
s_cmp_lg_u32 s85,0
s_cbranch_scc1 Lsl65kf
s_branch Lback40kf_nb
Lslow41kf_nb:
s_mov_b32 s85,200000
Lsl66kf:
s_sleep 2
buffer_load_dword v125,v124,s[32:35],s94 offen sc1
q3 0
v_cmp_ne_u32 vcc,s81,v125
s_nop 0
s_cbranch_vccz Lback42kf_nb
s_sub_u32 s85,s85,1
s_cmp_lg_u32 s85,0
s_cbranch_scc1 Lsl66kf
s_branch Lback42kf_nb
Lslow54kf_nb:
s_mov_b32 s85,200000
Lsl67kf:
s_sleep 2
buffer_load_dword v28,v27,s[32:35],s14 offen sc1
q3 0
v_cmp_ne_u32 vcc,s81,v28
s_nop 0
s_cbranch_vccz Lback55kf_nb
s_sub_u32 s85,s85,1
s_cmp_lg_u32 s85,0
s_cbranch_scc1 Lsl67kf
s_branch Lback55kf_nb
Lslow56kf_nb:
s_mov_b32 s85,200000
Lsl68kf:
s_sleep 2
buffer_load_dword v23,v22,s[32:35],s10 offen sc1
q3 0
v_cmp_ne_u32 vcc,s81,v23
s_nop 0
s_cbranch_vccz Lback57kf_nb
s_sub_u32 s85,s85,1
s_cmp_lg_u32 s85,0
s_cbranch_scc1 Lsl68kf
s_branch Lback57kf_nb
Lslow62kf_nb:
s_mov_b32 s85,200000
Lsl69kf:
s_sleep 2
buffer_load_dword v23,v22,s[32:35],s10 offen sc1
q3 0
v_cmp_ne_u32 vcc,s81,v23
s_nop 0
s_cbranch_vccz Lback63kf_nb
s_sub_u32 s85,s85,1
s_cmp_lg_u32 s85,0
s_cbranch_scc1 Lsl69kf
s_branch Lback63kf_nb
Lnoh43kf:
s_nop 7
qb 225,226,227
qb 226,228,229
qb 227,230,231
qj 112,224
qj 113,225
qj 114,226
qj 115,227
qb 160,160,161
qb 161,162,163
qb 162,164,165
qb 163,166,167
qj 80,160
qj 81,161
qj 82,162
qj 83,163
qb 232,232,233
qb 233,234,235
qb 234,236,237
qb 235,238,239
qj 116,232
qj 117,233
qj 118,234
qj 119,235
qb 168,168,169
qb 169,170,171
qb 170,172,173
qb 171,174,175
qj 84,168
qj 85,169
qj 86,170
qj 87,171
qb 240,240,241
qb 241,242,243
qb 242,244,245
qb 243,246,247
qj 120,240
qj 121,241
qj 122,242
qj 123,243
qb 176,176,177
qb 177,178,179
qb 178,180,181
qb 179,182,183
qj 88,176
qj 89,177
qj 90,178
qj 91,179
qb 248,248,249
qb 249,250,251
qb 250,252,253
qb 251,254,255
qj 124,248
qj 125,249
qj 126,250
qj 127,251
qb 184,184,185
qb 185,186,187
qb 186,188,189
qb 187,190,191
qj 92,184
qj 93,185
qj 94,186
qj 95,187
s_nop 1
s_branch Lown44kf
Lit47kf_rs_nb:
s_swappc_b64 s[64:65],s[66:67]
s_branch Lit47kf_ok_nb
Lit51kf_rs_nb:
s_swappc_b64 s[64:65],s[66:67]
s_branch Lit51kf_ok_nb
Ld052kf_rs_nb:
s_swappc_b64 s[64:65],s[66:67]
s_branch Ld052kf_ok_nb
Ld153kf_rs_nb:
s_swappc_b64 s[64:65],s[66:67]
v_mov_b32 v10,0
s_branch Ld153kf_ok_nb
LENDkf:
)"
    :: "{v0}"(tid), "{s30}"(bid), "{s[0:1]}"(__builtin_amdgcn_kernarg_segment_ptr())
    : "memory", "v1","v255","a255","s31","s99","vcc");
}
extern "C" void kernel_launch(void* const* d_in, const int* in_sizes, int n_in, void* d_out, int out_size, void* d_ws, size_t ws_size,
                              hipStream_t stream) {
  (void)hipFuncSetAttribute((const void*)kf, hipFuncAttributeMaxDynamicSharedMemorySize, 163840);
  kf<<<256, 256, 163840, stream>>>(d_in[0], d_in[1], d_in[2], d_in[3], d_in[4], d_in[5], d_in[7], d_in[8], d_in[9], d_in[10], d_out, d_ws);
}
```
